```python
import torch

def _softmax(x, axis=-1):
    return torch.softmax(x, dim=axis)
def _gelu(x):
    return torch.nn.functional.gelu(x, approximate="tanh")
def _silu(x):
    return torch.nn.functional.silu(x)
def _sigmoid(x):
    return torch.sigmoid(x)
def _layernorm(x, axis=-1, eps=1e-5):
    m = x.mean(dim=axis, keepdim=True)
    v = x.var(dim=axis, keepdim=True, unbiased=False)
    return (x - m) / torch.sqrt(v + eps)
def _scatter_add(tgt, idx, src, axis=0):
    return tgt.clone().index_add_(axis, idx, src)
def _segment_sum(src, seg, n):
    out = torch.zeros((n,) + src.shape[1:], dtype=src.dtype)
    return out.index_add_(0, seg, src)
def _pad(x, pw):
    flat = tuple(v for pair in reversed(pw) for v in pair)
    return torch.nn.functional.pad(x, flat)

kernel_name = 'parallel_sum__ssm_conv_s4d__glu_gated_geglu__prompt'

def setup_inputs():

    _w = (lambda a: (lambda t: t.to(torch.bfloat16) if t.is_floating_point() else t)(torch.as_tensor(a)))
    def _A_setup():
        Bsz, L, D, N = 1, 2048, 512, 128
        return {
            "u": _w((lambda *s: torch.randn(*s, dtype=torch.bfloat16) * 0.05)(Bsz, L, D)),
            "log_A_real": _w((lambda *s: torch.randn(*s, dtype=torch.bfloat16) * 6.4)(D, N) * 0.5),
            "A_imag": _w((lambda *s: torch.randn(*s, dtype=torch.bfloat16) * 0.05)(D, N) * 3.14),
            "C_real": _w((lambda *s: torch.randn(*s, dtype=torch.bfloat16) * 0.05)(D, N) / N ** 0.5),
            "C_imag": _w((lambda *s: torch.randn(*s, dtype=torch.bfloat16) * 0.05)(D, N) / N ** 0.5),
            "log_dt": _w((lambda *s: torch.randn(*s, dtype=torch.bfloat16) * 0.05)(D) * 0.5 - 4.0),
            "D_skip": _w((lambda *s: torch.randn(*s, dtype=torch.bfloat16) * 0.05)(D)),
        }
    _A_in = _A_setup()

    def _B_setup():
        B, S, Dm, Df = 1, 2048, 512, 1792
        return {
            "x": _w((lambda *s: torch.randn(*s, dtype=torch.bfloat16) * 0.05)(B, S, Dm)),
            "w_gate": _w((lambda *s: torch.randn(*s, dtype=torch.bfloat16) * 0.4)(Dm, Df) * (Dm ** -0.5)),
            "w_up": _w((lambda *s: torch.randn(*s, dtype=torch.bfloat16) * 0.4)(Dm, Df) * (Dm ** -0.5)),
            "w_down": _w((lambda *s: torch.randn(*s, dtype=torch.bfloat16) * 0.4)(Df, Dm) * (Df ** -0.5)),
        }
    _B_in = _B_setup()

    return {
        "A_u": _A_in["u"],
        "A_log_A_real": _A_in["log_A_real"],
        "A_A_imag": _A_in["A_imag"],
        "A_C_real": _A_in["C_real"],
        "A_C_imag": _A_in["C_imag"],
        "A_log_dt": _A_in["log_dt"],
        "A_D_skip": _A_in["D_skip"],
        "B_x": _B_in["x"],
        "B_w_gate": _B_in["w_gate"],
        "B_w_up": _B_in["w_up"],
        "B_w_down": _B_in["w_down"],
    }

def reference(A_u, A_log_A_real, A_A_imag, A_C_real, A_C_imag, A_log_dt, A_D_skip, B_x, B_w_gate, B_w_up, B_w_down):
    def _A_ref(u, log_A_real, A_imag, C_real, C_imag, log_dt, D_skip):
        L = u.shape[1]
        dt = torch.exp(log_dt)
        A = -torch.exp(log_A_real) + 1j * A_imag
        C = (C_real + 1j * C_imag) * dt[:, None]
        pos = (lambda x, dt: x.to(dt))(torch.arange(L), log_dt.dtype)
        P = torch.exp((A * dt[:, None])[:, :, None] * pos[None, None, :])
        K = 2.0 * torch.einsum("dn,dnl->dl", C, P).real
        L2 = 2 * L
        u_t = (lambda x, *d: x.permute(*d))(u, 0, 2, 1)
        Kf = (lambda x, *a, **k: torch.fft.rfft(x.float(), *a, **k))(K, L2)
        Uf = (lambda x, *a, **k: torch.fft.rfft(x.float(), *a, **k))(u_t, L2)
        y = (lambda x, *a, **k: torch.fft.irfft(x, *a, **k).to(torch.bfloat16))(Uf * Kf[None, :, :], L2)[:, :, :L]
        y = (lambda x, *d: x.permute(*d))(y, 0, 2, 1)
        return y + u * D_skip[None, None, :]

    def _B_ref(x, w_gate, w_up, w_down):
        g = torch.einsum("bsd,df->bsf", x, w_gate)
        u = torch.einsum("bsd,df->bsf", x, w_up)
        h = _gelu(g) * u
        return torch.einsum("bsf,fd->bsd", h, w_down)

    x = A_u
    xn = _layernorm(x, axis=-1)
    return x + _A_ref(xn, A_log_A_real, A_A_imag, A_C_real, A_C_imag, A_log_dt, A_D_skip) + _B_ref(xn, B_w_gate, B_w_up, B_w_down)
```

```cpp
#include <hip/hip_runtime.h>
#include <hip/hip_bf16.h>


typedef unsigned short u16;
typedef unsigned int u32;
using bf16x8 = __attribute__((ext_vector_type(8))) short;
using bf16x4 = __attribute__((ext_vector_type(4))) short;
using f32x4 = __attribute__((ext_vector_type(4))) float;
using f32x16 = __attribute__((ext_vector_type(16))) float;
using u32x4 = __attribute__((ext_vector_type(4))) unsigned int;
using u32x2 = __attribute__((ext_vector_type(2))) unsigned int;

constexpr int L_ = 2048, D_ = 512, N_ = 128, F_ = 1792;
constexpr int KC_STRIDE = 648;

constexpr size_t WS_XNT = 0;
constexpr size_t WS_XNTR = WS_XNT + (size_t)L_ * D_ * 2;
constexpr size_t WS_KC = WS_XNTR + (size_t)L_ * D_ * 2;
constexpr size_t WS_H = WS_KC + (size_t)D_ * KC_STRIDE * 4;
constexpr size_t WS_WG = WS_H + (size_t)L_ * F_ * 2;
constexpr size_t WS_WU = WS_WG + (size_t)D_ * F_ * 2;
constexpr size_t WS_WD = WS_WU + (size_t)D_ * F_ * 2;
constexpr size_t WS_AT = WS_WD + (size_t)D_ * F_ * 2;

__device__ __forceinline__ float bf2f(u16 v) { return __uint_as_float(((u32)v) << 16); }
__device__ __forceinline__ u16 f2bf(float f) {
  __bf16 b = (__bf16)f;
  return __builtin_bit_cast(u16, b);
}
__device__ __forceinline__ float rbf(float f) { return bf2f(f2bf(f)); }
__device__ __forceinline__ u32 pack2(float lo, float hi) { return (u32)f2bf(lo) | ((u32)f2bf(hi) << 16); }

__device__ __forceinline__ float wave_sum(float v) {
#pragma unroll
  for (int o = 32; o > 0; o >>= 1) v += __shfl_xor(v, o, 64);
  return v;
}

constexpr int K1_LN_BLOCKS = 64;
constexpr int K1_KG_BLOCKS = 512;
constexpr int K1_WT_BLOCKS = 336;

struct cf { float r, i; };
__device__ __forceinline__ cf csq(cf a) { return cf{a.r * a.r - a.i * a.i, 2.f * a.r * a.i}; }
__device__ __forceinline__ cf cmul(cf a, cf b) { return cf{a.r * b.r - a.i * b.i, a.r * b.i + a.i * b.r}; }
__device__ __forceinline__ cf cexp_(float zr, float zi, float t) {
  float re = zr * t, im = zi * t;
  float m = __expf(re);
  float s, c;
  sincosf(im, &s, &c);
  return cf{m * c, m * s};
}

__global__ __launch_bounds__(256) void k1_kernel(const u16* __restrict__ x, const u16* __restrict__ logA,
                                                 const u16* __restrict__ Aim, const u16* __restrict__ Cre,
                                                 const u16* __restrict__ Cim, const u16* __restrict__ logdt,
                                                 const u16* __restrict__ wg, const u16* __restrict__ wu,
                                                 const u16* __restrict__ wd, char* __restrict__ ws) {
  __shared__ __attribute__((aligned(16))) char smem[32 * 520 * 2];
  const int tid = threadIdx.x, lane = tid & 63, wid = tid >> 6;
  int bid = blockIdx.x;
  if (bid < K1_KG_BLOCKS) {
    const int d = bid;
    const float dt = rbf(expf(bf2f(logdt[d])));
    const int kq = lane >> 4, idx = lane & 15;
    f32x4 acc[4] = {};
#pragma unroll 2
    for (int s = 0; s < 8; ++s) {
      const int n = wid * 32 + s * 4 + kq;
      const float ar = -rbf(expf(bf2f(logA[d * N_ + n])));
      const float zr = ar * dt, zi = bf2f(Aim[d * N_ + n]) * dt;
      const cf C = cf{bf2f(Cre[d * N_ + n]) * dt, bf2f(Cim[d * N_ + n]) * dt};
      cf Y = cexp_(zr, zi, (float)idx);
      cf E = cexp_(zr, zi, (float)(idx == 0 ? 256 : 16 * idx));
#pragma unroll
      for (int e = 0; e < 4; ++e) {
        cf Xe = E;
        if (e == 0 && idx == 0) Xe = cf{1.f, 0.f};
        cf X = cmul(C, Xe);
        acc[e] = __builtin_amdgcn_mfma_f32_16x16x4f32(X.r, Y.r, acc[e], 0, 0, 0);
        acc[e] = __builtin_amdgcn_mfma_f32_16x16x4f32(-X.i, Y.i, acc[e], 0, 0, 0);
        if (e < 3) { Y = csq(Y); E = csq(E); }
      }
    }
    float* red = (float*)smem;
#pragma unroll
    for (int e = 0; e < 4; ++e)
#pragma unroll
      for (int r = 0; r < 4; ++r) red[(wid * 4 + e) * 256 + ((lane >> 4) * 4 + r) * 16 + (lane & 15)] = acc[e][r];
    __syncthreads();
    float* kc = (float*)(ws + WS_KC) + (size_t)d * KC_STRIDE;
#pragma unroll
    for (int e = 0; e < 4; ++e) {
      float v = 2.f * (red[(0 * 4 + e) * 256 + tid] + red[(1 * 4 + e) * 256 + tid] + red[(2 * 4 + e) * 256 + tid] +
                       red[(3 * 4 + e) * 256 + tid]);
      if (e == 0) kc[tid] = v;
      else if (tid >= 128) kc[128 * e + tid] = v;
      else if (e == 3 && tid == 0) kc[640] = v;
    }
    return;
  }
  bid -= K1_KG_BLOCKS;
  if (bid < K1_LN_BLOCKS) {
    u16* tile = (u16*)smem;
    u16* xnt = (u16*)(ws + WS_XNT);
    const int r0 = bid * 32;
#pragma unroll 2
    for (int i = 0; i < 8; ++i) {
      const int rl = wid * 8 + i, row = r0 + rl;
      u32x4 v = *(const u32x4*)(x + (size_t)row * D_ + lane * 8);
      float f[8];
#pragma unroll
      for (int j = 0; j < 4; ++j) { f[2 * j] = __uint_as_float(v[j] << 16); f[2 * j + 1] = __uint_as_float(v[j] & 0xffff0000u); }
      float s = 0.f;
#pragma unroll
      for (int j = 0; j < 8; ++j) s += f[j];
      s = wave_sum(s);
      const float mean = s * (1.f / D_);
      float q = 0.f;
#pragma unroll
      for (int j = 0; j < 8; ++j) { float t = f[j] - mean; q += t * t; }
      q = wave_sum(q);
      const float mb = rbf(mean);
      const float vb = rbf(q * (1.f / D_));
      const float sd = rbf(sqrtf(rbf(vb + 1e-5f)));
      u32x4 o;
#pragma unroll
      for (int j = 0; j < 4; ++j) o[j] = pack2(rbf(f[2 * j] - mb) / sd, rbf(f[2 * j + 1] - mb) / sd);
      *(u32x4*)(xnt + ((size_t)((row >> 4) * 16 + (lane >> 2)) * 64 + (lane & 3) * 16 + (row & 15)) * 8) = o;
      *(u32x4*)(tile + rl * 520 + lane * 8) = o;
    }
    __syncthreads();
    u16* xtr = (u16*)(ws + WS_XNTR);
#pragma unroll
    for (int h = 0; h < 2; ++h) {
      const int d = tid + h * 256;
#pragma unroll
      for (int g = 0; g < 4; ++g) {
        u32x4 o;
#pragma unroll
        for (int j = 0; j < 4; ++j)
          o[j] = (u32)tile[(g * 8 + 2 * j) * 520 + d] | ((u32)tile[(g * 8 + 2 * j + 1) * 520 + d] << 16);
        *(u32x4*)(xtr + (size_t)d * L_ + r0 + g * 8) = o;
      }
    }
    return;
  }
  bid -= K1_LN_BLOCKS;
  {
    int task = bid * 4 + wid;
    const u16* W; u16* Wt; int Kd, Nd;
    if (task < 448) { W = wg; Wt = (u16*)(ws + WS_WG); Kd = D_; Nd = F_; }
    else if (task < 896) { task -= 448; W = wu; Wt = (u16*)(ws + WS_WU); Kd = D_; Nd = F_; }
    else { task -= 896; W = wd; Wt = (u16*)(ws + WS_WD); Kd = F_; Nd = D_; }
    const int nks = Kd / 32;
    const int ks = task % nks, nb = task / nks;
    const int col = nb * 64 + lane;
    u16 v[32];
#pragma unroll
    for (int k = 0; k < 32; ++k) v[k] = W[(size_t)(ks * 32 + k) * Nd + col];
#pragma unroll
    for (int q = 0; q < 4; ++q) {
      u32x4 o;
#pragma unroll
      for (int j = 0; j < 4; ++j) o[j] = (u32)v[q * 8 + 2 * j] | ((u32)v[q * 8 + 2 * j + 1] << 16);
      *(u32x4*)(Wt + ((size_t)((col >> 4) * nks + ks) * 64 + q * 16 + (col & 15)) * 8) = o;
    }
  }
}

constexpr int G1_STAGE = 22 * 1024;
constexpr int G1_NBUF = 3;
constexpr int K2_LDS = G1_STAGE * G1_NBUF;

constexpr int UC_LEN = 2080;
constexpr int KR_LEN = 3840;
constexpr int CV_CH_BYTES = 4 * UC_LEN * 2 + 2 * KR_LEN * 2;

__device__ __forceinline__ float gelu_tanh(float g) {
  const float k0 = 0.7978845608028654f, k1 = 0.044715f;
  float inner = k0 * (g + k1 * g * g * g);
  return 0.5f * g * (1.f + tanhf(inner));
}

__device__ __forceinline__ void glds16(const void* g, void* l) {
  __builtin_amdgcn_global_load_lds((const __attribute__((address_space(1))) u32*)g,
                                   (__attribute__((address_space(3))) u32*)l, 16, 0, 0);
}

__global__ __launch_bounds__(256, 2) void k2_kernel(const u16* __restrict__ dskip, char* __restrict__ ws) {
  extern __shared__ __attribute__((aligned(16))) char smem[];
  const int tid = threadIdx.x, lane = tid & 63, wid = tid >> 6;
  const int bid = blockIdx.x;
  if (bid < 256) {
    const int xcd = bid & 7, loc = bid >> 3;
    const int mt = (xcd >> 1) * 4 + (loc & 3);
    const int nt = (xcd & 1) * 8 + (loc >> 2);
    const u16* xnt = (const u16*)(ws + WS_XNT);
    const u16* wgt = (const u16*)(ws + WS_WG);
    const u16* wut = (const u16*)(ws + WS_WU);
    auto wsrc = [&](int slot) -> const u16* {
      const int t = slot >> 1, isup = slot & 1;
      return (isup ? wut : wgt) + (size_t)(nt * 7 + t) * 16 * 512;
    };
    auto stage = [&](int ks, int buf) {
      char* base = smem + buf * G1_STAGE;
#pragma unroll
      for (int i = 0; i < 6; ++i) {
        const int f = wid + i * 4;
        if (f < 8) glds16(xnt + ((size_t)(mt * 8 + f) * 16 + ks) * 512 + lane * 8, base + f * 1024 + lane * 16);
        else if (f < 22) glds16(wsrc(f - 8) + (size_t)ks * 512 + lane * 8, base + f * 1024 + lane * 16);
      }
    };
    const int wm = wid >> 1, wn = wid & 1;
    f32x4 acc[7][4] = {};
    stage(0, 0);
    stage(1, 1);
    for (int ks = 0; ks < 16; ++ks) {
      if (ks < 15) {
        if (wid < 2) asm volatile("s_waitcnt vmcnt(6)" ::: "memory");
        else asm volatile("s_waitcnt vmcnt(5)" ::: "memory");
      } else {
        asm volatile("s_waitcnt vmcnt(0)" ::: "memory");
      }
      asm volatile("s_waitcnt lgkmcnt(0)" ::: "memory");
      __builtin_amdgcn_s_barrier();
      if (ks + 2 < 16) stage(ks + 2, (ks + 2) % 3);
      const char* base = smem + (ks % 3) * G1_STAGE;
      bf16x8 xb[4], wa[7];
#pragma unroll
      for (int i = 0; i < 4; ++i) xb[i] = *(const bf16x8*)(base + (wm * 4 + i) * 1024 + lane * 16);
#pragma unroll
      for (int j = 0; j < 7; ++j) wa[j] = *(const bf16x8*)(base + (8 + wn * 7 + j) * 1024 + lane * 16);
#pragma unroll
      for (int j = 0; j < 7; ++j)
#pragma unroll
        for (int i = 0; i < 4; ++i) acc[j][i] = __builtin_amdgcn_mfma_f32_16x16x32_bf16(wa[j], xb[i], acc[j][i], 0, 0, 0);
    }
    __syncthreads();
    float* ex = (float*)smem;
    if (wn == 0) {
#pragma unroll
      for (int i = 0; i < 4; ++i)
#pragma unroll
        for (int r = 0; r < 4; ++r) ex[((wm * 4 + i) * 4 + r) * 64 + lane] = acc[6][i][r];
    }
    __syncthreads();
    u16* hT = (u16*)(ws + WS_H);
    auto emit = [&](int t, const f32x4& g, const f32x4& u, int i) {
      float hv[4];
#pragma unroll
      for (int r = 0; r < 4; ++r) hv[r] = rbf(gelu_tanh(rbf(g[r]))) * rbf(u[r]);
      const int T = nt * 7 + t;
      const int ks2 = T >> 1;
      const int q = 2 * (T & 1) + (lane >> 5);
      const int m16 = mt * 8 + wm * 4 + i;
      u32x2 o;
      o[0] = pack2(hv[0], hv[1]);
      o[1] = pack2(hv[2], hv[3]);
      *(u32x2*)(hT + ((size_t)(m16 * 56 + ks2) * 64 + q * 16 + (lane & 15)) * 8 + ((lane >> 4) & 1) * 4) = o;
    };
    if (wn == 0) {
#pragma unroll
      for (int t = 0; t < 3; ++t)
#pragma unroll
        for (int i = 0; i < 4; ++i) emit(t, acc[2 * t][i], acc[2 * t + 1][i], i);
    } else {
#pragma unroll
      for (int i = 0; i < 4; ++i) {
        f32x4 g;
#pragma unroll
        for (int r = 0; r < 4; ++r) g[r] = ex[((wm * 4 + i) * 4 + r) * 64 + lane];
        emit(3, g, acc[0][i], i);
      }
#pragma unroll
      for (int t = 4; t < 7; ++t)
#pragma unroll
        for (int i = 0; i < 4; ++i) emit(t, acc[2 * t - 7][i], acc[2 * t - 6][i], i);
    }
    return;
  }
  {
    const int cb = bid - 256;
    const int ch = wid >> 1, w2 = wid & 1;
    const int d = cb * 2 + ch;
    const int t2 = tid & 127;
    char* cbase = smem + ch * CV_CH_BYTES;
    u16* ucp = (u16*)cbase;
    u16* krh = (u16*)(cbase + 4 * UC_LEN * 2);
    u16* krl = krh + KR_LEN;
    const u16* ug = (const u16*)(ws + WS_XNTR) + (size_t)d * L_;
    const float* kc = (const float*)(ws + WS_KC) + (size_t)d * KC_STRIDE;
    for (int tb = t2; tb < UC_LEN / 8; tb += 128) {
      u32 w[8];
      const int c0 = tb - 4, c1 = tb - 3;
      u32x4 a = {0, 0, 0, 0}, b = {0, 0, 0, 0};
      if (c0 >= 0 && c0 < 256) a = *(const u32x4*)(ug + c0 * 8);
      if (c1 >= 0 && c1 < 256) b = *(const u32x4*)(ug + c1 * 8);
#pragma unroll
      for (int j = 0; j < 4; ++j) { w[j] = a[j]; w[4 + j] = b[j]; }
      u32x4 o0, o1, o2, o3;
#pragma unroll
      for (int j = 0; j < 4; ++j) {
        o0[j] = (w[j] >> 16) | (w[j + 1] << 16);
        o1[j] = w[j + 1];
        o2[j] = (w[j + 1] >> 16) | (w[j + 2] << 16);
        o3[j] = w[j + 2];
      }
      *(u32x4*)(ucp + 0 * UC_LEN + tb * 8) = o0;
      *(u32x4*)(ucp + 1 * UC_LEN + tb * 8) = o1;
      *(u32x4*)(ucp + 2 * UC_LEN + tb * 8) = o2;
      *(u32x4*)(ucp + 3 * UC_LEN + tb * 8) = o3;
    }
    for (int tb = t2; tb < 3072 / 8; tb += 128) {
      u32x4 oh = {0, 0, 0, 0}, ol = {0, 0, 0, 0};
      if (tb < 256) {
        u16 hh[8], ll[8];
#pragma unroll
        for (int j = 0; j < 8; ++j) {
          const int jj = 2047 - (tb * 8 + j);
          int ci;
          if (jj < 256) ci = jj;
          else {
            const int e = (31 - __clz(jj)) - 7;
            const int half = 1 << (e - 1);
            int q = jj >> e;
            const int rem = jj & ((1 << e) - 1);
            if (rem > half || (rem == half && (q & 1))) q += 1;
            ci = 128 * e + q;
          }
          const float v = kc[ci];
          hh[j] = f2bf(v);
          ll[j] = f2bf(v - bf2f(hh[j]));
        }
#pragma unroll
        for (int j = 0; j < 4; ++j) {
          oh[j] = (u32)hh[2 * j] | ((u32)hh[2 * j + 1] << 16);
          ol[j] = (u32)ll[2 * j] | ((u32)ll[2 * j + 1] << 16);
        }
      }
      const int pa = tb * 8 + 8 * (tb >> 2);
      *(u32x4*)(krh + pa) = oh;
      *(u32x4*)(krl + pa) = ol;
    }
    __syncthreads();
    const int r = lane & 31, h = lane >> 5;
    const u16* ap = ucp + (r & 3) * UC_LEN + 8 * h + 4 * (r >> 2);
    const int bofs0 = 2048 - 32 * (r + 1) + 8 * h + 8 * (63 - r);
    const int bofs1 = bofs0 - 1024 - 256;
    f32x16 acc0 = {}, acc1 = {};
    auto lda = [&](int s) -> bf16x8 {
      bf16x4 lo = *(const bf16x4*)(ap + 16 * s);
      bf16x4 hi = *(const bf16x4*)(ap + 16 * s + 4);
      return __builtin_shufflevector(lo, hi, 0, 1, 2, 3, 4, 5, 6, 7);
    };
    const int sb0 = w2 ? 48 : 0, se0 = w2 ? 64 : 48;
#pragma unroll 4
    for (int s = sb0; s < se0; ++s) {
      bf16x8 a = lda(s);
      const int so = 16 * s + 8 * (s >> 1);
      bf16x8 b0h = *(const bf16x8*)(krh + bofs0 + so);
      bf16x8 b0l = *(const bf16x8*)(krl + bofs0 + so);
      bf16x8 b1h = *(const bf16x8*)(krh + bofs1 + so);
      bf16x8 b1l = *(const bf16x8*)(krl + bofs1 + so);
      acc0 = __builtin_amdgcn_mfma_f32_32x32x16_bf16(a, b0h, acc0, 0, 0, 0);
      acc1 = __builtin_amdgcn_mfma_f32_32x32x16_bf16(a, b1h, acc1, 0, 0, 0);
      acc0 = __builtin_amdgcn_mfma_f32_32x32x16_bf16(a, b0l, acc0, 0, 0, 0);
      acc1 = __builtin_amdgcn_mfma_f32_32x32x16_bf16(a, b1l, acc1, 0, 0, 0);
    }
    if (w2) {
#pragma unroll 4
      for (int s = 64; s < 128; ++s) {
        bf16x8 a = lda(s);
        const int so = 16 * s + 8 * (s >> 1);
        bf16x8 b1h = *(const bf16x8*)(krh + bofs1 + so);
        bf16x8 b1l = *(const bf16x8*)(krl + bofs1 + so);
        acc1 = __builtin_amdgcn_mfma_f32_32x32x16_bf16(a, b1h, acc1, 0, 0, 0);
        acc1 = __builtin_amdgcn_mfma_f32_32x32x16_bf16(a, b1l, acc1, 0, 0, 0);
      }
    }
    __syncthreads();
    float* red = (float*)cbase;
#pragma unroll
    for (int g = 0; g < 4; ++g) {
      f32x4 v0 = {acc0[4 * g], acc0[4 * g + 1], acc0[4 * g + 2], acc0[4 * g + 3]};
      f32x4 v1 = {acc1[4 * g], acc1[4 * g + 1], acc1[4 * g + 2], acc1[4 * g + 3]};
      *(f32x4*)(red + ((w2 * 2 + 0) * 32 + r) * 36 + 8 * g + 4 * h) = v0;
      *(f32x4*)(red + ((w2 * 2 + 1) * 32 + r) * 36 + 8 * g + 4 * h) = v1;
    }
    __syncthreads();
    const float dsk = bf2f(dskip[d]);
    u16* at = (u16*)(ws + WS_AT) + (size_t)d * L_;
#pragma unroll
    for (int it = 0; it < 2; ++it) {
      const int c = t2 + it * 128;
      const int Ta = c >> 2, b0 = (c & 3) * 8;
      const float* p0 = red + (0 * 64 + Ta) * 36 + b0;
      const float* p1 = red + (1 * 64 + Ta) * 36 + b0;
      u32x4 uv = *(const u32x4*)(ug + c * 8);
      u32x4 o;
#pragma unroll
      for (int j = 0; j < 4; ++j) {
        float y0 = rbf(p0[2 * j] + p1[2 * j]), y1 = rbf(p0[2 * j + 1] + p1[2 * j + 1]);
        float u0 = __uint_as_float(uv[j] << 16), u1 = __uint_as_float(uv[j] & 0xffff0000u);
        o[j] = pack2(y0 + rbf(u0 * dsk), y1 + rbf(u1 * dsk));
      }
      *(u32x4*)(at + c * 8) = o;
    }
  }
}

__global__ __launch_bounds__(256, 2) void k3_kernel(const u16* __restrict__ x, const char* __restrict__ ws,
                                                    u16* __restrict__ out) {
  __shared__ __attribute__((aligned(16))) float red[4 * 64 * 68];
  __shared__ __attribute__((aligned(16))) u16 atile[64 * 72];
  const int tid = threadIdx.x, lane = tid & 63, wid = tid >> 6;
  const int bid = blockIdx.x;
  const int xcd = bid & 7, loc = bid >> 3;
  const int mt = xcd * 4 + (loc & 3);
  const int nt = loc >> 2;
  const u16* hT = (const u16*)(ws + WS_H);
  const u16* wdt = (const u16*)(ws + WS_WD);
  const u16* at = (const u16*)(ws + WS_AT);
  u32x4 av[2];
#pragma unroll
  for (int it = 0; it < 2; ++it) {
    const int c = tid + it * 256;
    av[it] = *(const u32x4*)(at + (size_t)(nt * 64 + (c >> 3)) * L_ + mt * 64 + (c & 7) * 8);
  }
  f32x4 acc[4][4] = {};
  const int ks0 = wid * 14;
  const u16* hp = hT + ((size_t)(mt * 4) * 56 + ks0) * 512 + lane * 8;
  const u16* wp = wdt + ((size_t)(nt * 4) * 56 + ks0) * 512 + lane * 8;
#pragma unroll 2
  for (int k = 0; k < 14; ++k) {
    bf16x8 hb[4], wa[4];
#pragma unroll
    for (int i = 0; i < 4; ++i) {
      hb[i] = *(const bf16x8*)(hp + (size_t)(i * 56 + k) * 512);
      wa[i] = *(const bf16x8*)(wp + (size_t)(i * 56 + k) * 512);
    }
#pragma unroll
    for (int j = 0; j < 4; ++j)
#pragma unroll
      for (int i = 0; i < 4; ++i) acc[j][i] = __builtin_amdgcn_mfma_f32_16x16x32_bf16(wa[j], hb[i], acc[j][i], 0, 0, 0);
  }
#pragma unroll
  for (int j = 0; j < 4; ++j)
#pragma unroll
    for (int i = 0; i < 4; ++i)
      *(f32x4*)(red + (wid * 64 + i * 16 + (lane & 15)) * 68 + j * 16 + 4 * (lane >> 4)) = acc[j][i];
#pragma unroll
  for (int it = 0; it < 2; ++it) {
    const int c = tid + it * 256;
    *(u32x4*)(atile + (c >> 3) * 72 + (c & 7) * 8) = av[it];
  }
  __syncthreads();
#pragma unroll
  for (int it = 0; it < 2; ++it) {
    const int c = tid + it * 256;
    const int m = c >> 3, n0 = (c & 7) * 8;
    float s[8];
#pragma unroll
    for (int j = 0; j < 8; ++j) s[j] = 0.f;
#pragma unroll
    for (int w = 0; w < 4; ++w) {
      f32x4 p0 = *(const f32x4*)(red + (w * 64 + m) * 68 + n0);
      f32x4 p1 = *(const f32x4*)(red + (w * 64 + m) * 68 + n0 + 4);
#pragma unroll
      for (int j = 0; j < 4; ++j) { s[j] += p0[j]; s[4 + j] += p1[j]; }
    }
    const size_t go = (size_t)(mt * 64 + m) * D_ + nt * 64 + n0;
    u32x4 xv = *(const u32x4*)(x + go);
    u32x4 o;
#pragma unroll
    for (int j = 0; j < 4; ++j) {
      float x0 = __uint_as_float(xv[j] << 16), x1 = __uint_as_float(xv[j] & 0xffff0000u);
      float a0 = bf2f(atile[(n0 + 2 * j) * 72 + m]), a1 = bf2f(atile[(n0 + 2 * j + 1) * 72 + m]);
      o[j] = pack2(rbf(x0 + a0) + rbf(s[2 * j]), rbf(x1 + a1) + rbf(s[2 * j + 1]));
    }
    *(u32x4*)(out + go) = o;
  }
}

extern "C" void kernel_launch(void* const* d_in, const int* in_sizes, int n_in, void* d_out, int out_size,
                              void* d_ws, size_t ws_size, hipStream_t stream) {
  const u16* x = (const u16*)d_in[0];
  const u16* logA = (const u16*)d_in[1];
  const u16* Aim = (const u16*)d_in[2];
  const u16* Cre = (const u16*)d_in[3];
  const u16* Cim = (const u16*)d_in[4];
  const u16* logdt = (const u16*)d_in[5];
  const u16* dskip = (const u16*)d_in[6];
  const u16* wg = (const u16*)d_in[8];
  const u16* wu = (const u16*)d_in[9];
  const u16* wd = (const u16*)d_in[10];
  char* ws = (char*)d_ws;
  hipFuncSetAttribute((const void*)k2_kernel, hipFuncAttributeMaxDynamicSharedMemorySize, K2_LDS);
  k1_kernel<<<K1_KG_BLOCKS + K1_LN_BLOCKS + K1_WT_BLOCKS, 256, 0, stream>>>(x, logA, Aim, Cre, Cim, logdt, wg, wu, wd, ws);
  k2_kernel<<<512, 256, K2_LDS, stream>>>(dskip, ws);
  k3_kernel<<<256, 256, 0, stream>>>(x, ws, (u16*)d_out);
}
```

```cpp
#include <hip/hip_runtime.h>
#include <hip/hip_bf16.h>

typedef unsigned short u16;
typedef unsigned int u32;
using bf16x8 = __attribute__((ext_vector_type(8))) short;
using bf16x4 = __attribute__((ext_vector_type(4))) short;
using f32x4 = __attribute__((ext_vector_type(4))) float;
using f32x16 = __attribute__((ext_vector_type(16))) float;
using u32x4 = __attribute__((ext_vector_type(4))) unsigned int;
using u32x2 = __attribute__((ext_vector_type(2))) unsigned int;

constexpr int L_ = 2048, D_ = 512, N_ = 128, F_ = 1792;
constexpr int KC_STRIDE = 648;

constexpr size_t WS_XNT = 0;
constexpr size_t WS_XNTR = WS_XNT + (size_t)L_ * D_ * 2;
constexpr size_t WS_KC = WS_XNTR + (size_t)L_ * D_ * 2;
constexpr size_t WS_H = WS_KC + (size_t)D_ * KC_STRIDE * 4;
constexpr size_t WS_WG = WS_H + (size_t)L_ * F_ * 2;
constexpr size_t WS_WU = WS_WG + (size_t)D_ * F_ * 2;
constexpr size_t WS_WD = WS_WU + (size_t)D_ * F_ * 2;
constexpr size_t WS_AT = WS_WD + (size_t)D_ * F_ * 2;

#define DIAG_START(k)
#define DIAG_STAMP(i)
#define DIAG_END(wsp)

__device__ __forceinline__ float bf2f(u16 v) { return __uint_as_float(((u32)v) << 16); }
__device__ __forceinline__ u16 f2bf(float f) {
  __bf16 b = (__bf16)f;
  return __builtin_bit_cast(u16, b);
}
__device__ __forceinline__ float rbf(float f) { return bf2f(f2bf(f)); }
__device__ __forceinline__ u32 pack2(float lo, float hi) { return (u32)f2bf(lo) | ((u32)f2bf(hi) << 16); }

__device__ __forceinline__ void st16(void* p, u32x4 v) { *(u32x4*)p = v; }
__device__ __forceinline__ void st8(void* p, u32x2 v) { *(u32x2*)p = v; }
__device__ __forceinline__ void st4(void* p, float v) { *(float*)p = v; }

__device__ __forceinline__ float wave_sum(float v) {
  v += __builtin_bit_cast(float, __builtin_amdgcn_update_dpp(0, __builtin_bit_cast(int, v), 0xB1, 0xf, 0xf, true));
  v += __builtin_bit_cast(float, __builtin_amdgcn_update_dpp(0, __builtin_bit_cast(int, v), 0x4E, 0xf, 0xf, true));
  v += __builtin_bit_cast(float, __builtin_amdgcn_update_dpp(0, __builtin_bit_cast(int, v), 0x141, 0xf, 0xf, true));
  v += __builtin_bit_cast(float, __builtin_amdgcn_update_dpp(0, __builtin_bit_cast(int, v), 0x140, 0xf, 0xf, true));
  const int iv = __builtin_bit_cast(int, v);
  return (__builtin_bit_cast(float, __builtin_amdgcn_readlane(iv, 0)) + __builtin_bit_cast(float, __builtin_amdgcn_readlane(iv, 16))) +
         (__builtin_bit_cast(float, __builtin_amdgcn_readlane(iv, 32)) + __builtin_bit_cast(float, __builtin_amdgcn_readlane(iv, 48)));
}

constexpr int K1_LN_BLOCKS = 128;
constexpr int K1_KG_BLOCKS = 512;
constexpr int K1_WT_BLOCKS = 336;

typedef float f2 __attribute__((ext_vector_type(2)));
__device__ __forceinline__ f2 cmul(f2 a, f2 b) {
  f2 t, r;
  asm("v_pk_mul_f32 %0, %1, %2 op_sel:[1,1] op_sel_hi:[1,0] neg_lo:[0,1]" : "=v"(t) : "v"(a), "v"(b));
  asm("v_pk_fma_f32 %0, %1, %2, %3 op_sel:[0,0,0] op_sel_hi:[0,1,1]" : "=v"(r) : "v"(a), "v"(b), "v"(t));
  return r;
}
__device__ __forceinline__ f2 csq(f2 a) { return cmul(a, a); }
__device__ __forceinline__ f2 cexp_(float zr, float zi, float t) {
  const float m = __builtin_amdgcn_exp2f((zr * t) * 1.44269504088896341f);
  float r = (zi * t) * 0.15915494309189535f;
  r -= rintf(r);
  return f2{m * __builtin_amdgcn_cosf(r), m * __builtin_amdgcn_sinf(r)};
}

__global__ __launch_bounds__(256) void k1_kernel(const u16* __restrict__ x, const u16* __restrict__ logA,
                                                 const u16* __restrict__ Aim, const u16* __restrict__ Cre,
                                                 const u16* __restrict__ Cim, const u16* __restrict__ logdt,
                                                 const u16* __restrict__ wg, const u16* __restrict__ wu,
                                                 const u16* __restrict__ wd, char* __restrict__ ws) {
  __shared__ __attribute__((aligned(16))) char smem[32 * 520 * 2];
  const int tid = threadIdx.x, lane = tid & 63, wid = tid >> 6;
  DIAG_START(0)
  int bid = blockIdx.x;
  if (bid < K1_KG_BLOCKS) {
    const int d = bid;
    float4* prm = (float4*)(smem + 16384);
    if (tid < 128) {
      const u16 la = logA[d * N_ + tid], ai = Aim[d * N_ + tid], cr = Cre[d * N_ + tid], ci = Cim[d * N_ + tid];
      const float dt = rbf(expf(bf2f(logdt[d])));
      const float ar = -rbf(expf(bf2f(la)));
      prm[tid] = make_float4(ar * dt, bf2f(ai) * dt, bf2f(cr) * dt, bf2f(ci) * dt);
    }
    __syncthreads();
    DIAG_STAMP(1);
    const int kq = lane >> 4, idx = lane & 15;
    f32x4 acc[4] = {};
    const float ty = (float)idx, te = (float)(idx == 0 ? 256 : 16 * idx);
#pragma unroll 2
    for (int s = 0; s < 8; ++s) {
      const float4 pr = prm[wid * 32 + s * 4 + kq];
      const f2 C = f2{pr.z, -pr.w};
      f2 Y[4], X[4];
      Y[0] = cexp_(pr.x, pr.y, ty);
      f2 E = cexp_(pr.x, -pr.y, te);
      X[0] = (idx == 0) ? C : cmul(C, E);
#pragma unroll
      for (int e = 1; e < 4; ++e) { Y[e] = csq(Y[e - 1]); E = csq(E); X[e] = cmul(C, E); }
#pragma unroll
      for (int e = 0; e < 4; ++e) acc[e] = __builtin_amdgcn_mfma_f32_16x16x4f32(X[e].x, Y[e].x, acc[e], 0, 0, 0);
#pragma unroll
      for (int e = 0; e < 4; ++e) acc[e] = __builtin_amdgcn_mfma_f32_16x16x4f32(X[e].y, Y[e].y, acc[e], 0, 0, 0);
    }
    DIAG_STAMP(2);
    float* red = (float*)smem;
#pragma unroll
    for (int e = 0; e < 4; ++e)
#pragma unroll
      for (int r = 0; r < 4; ++r) red[(wid * 4 + e) * 256 + ((lane >> 4) * 4 + r) * 16 + (lane & 15)] = acc[e][r];
    __syncthreads();
    float* kc = (float*)(ws + WS_KC) + (size_t)d * KC_STRIDE;
#pragma unroll
    for (int e = 0; e < 4; ++e) {
      float v = 2.f * (red[(0 * 4 + e) * 256 + tid] + red[(1 * 4 + e) * 256 + tid] + red[(2 * 4 + e) * 256 + tid] +
                       red[(3 * 4 + e) * 256 + tid]);
      if (e == 0) st4(kc + tid, v);
      else if (tid >= 128) st4(kc + 128 * e + tid, v);
      else if (e == 3 && tid == 0) st4(kc + 640, v);
    }
    DIAG_END(ws);
    return;
  }
  bid -= K1_KG_BLOCKS;
  if (bid < K1_LN_BLOCKS) {
    u16* tile = (u16*)smem;
    u16* xnt = (u16*)(ws + WS_XNT);
    const int r0 = bid * 16;
    u32x4 v[4];
#pragma unroll
    for (int i = 0; i < 4; ++i) v[i] = *(const u32x4*)(x + (size_t)(r0 + wid * 4 + i) * D_ + lane * 8);
#pragma unroll
    for (int i = 0; i < 4; ++i) {
      const int rl = wid * 4 + i, row = r0 + rl;
      float f[8];
#pragma unroll
      for (int j = 0; j < 4; ++j) { f[2 * j] = __uint_as_float(v[i][j] << 16); f[2 * j + 1] = __uint_as_float(v[i][j] & 0xffff0000u); }
      float s = 0.f;
#pragma unroll
      for (int j = 0; j < 8; ++j) s += f[j];
      s = wave_sum(s);
      const float mean = s * (1.f / D_);
      float q = 0.f;
#pragma unroll
      for (int j = 0; j < 8; ++j) { float t = f[j] - mean; q += t * t; }
      q = wave_sum(q);
      const float mb = rbf(mean);
      const float vb = rbf(q * (1.f / D_));
      const float sd = rbf(sqrtf(rbf(vb + 1e-5f)));
      u32x4 o;
#pragma unroll
      for (int j = 0; j < 4; ++j) o[j] = pack2(rbf(f[2 * j] - mb) / sd, rbf(f[2 * j + 1] - mb) / sd);
      st16(xnt + ((size_t)((row >> 4) * 16 + (lane >> 2)) * 64 + (lane & 3) * 16 + (row & 15)) * 8, o);
      *(u32x4*)(tile + rl * 520 + lane * 8) = o;
    }
    DIAG_STAMP(1);
    __syncthreads();
    u16* xtr = (u16*)(ws + WS_XNTR);
#pragma unroll
    for (int h = 0; h < 2; ++h) {
      const int d = tid + h * 256;
#pragma unroll
      for (int g = 0; g < 2; ++g) {
        u32x4 o;
#pragma unroll
        for (int j = 0; j < 4; ++j)
          o[j] = (u32)tile[(g * 8 + 2 * j) * 520 + d] | ((u32)tile[(g * 8 + 2 * j + 1) * 520 + d] << 16);
        st16(xtr + (size_t)d * L_ + r0 + g * 8, o);
      }
    }
    DIAG_END(ws);
    return;
  }
  bid -= K1_LN_BLOCKS;
  {
    int task = bid * 4 + wid;
    const u16* W; u16* Wt; int Kd, Nd;
    if (task < 448) { W = wg; Wt = (u16*)(ws + WS_WG); Kd = D_; Nd = F_; }
    else if (task < 896) { task -= 448; W = wu; Wt = (u16*)(ws + WS_WU); Kd = D_; Nd = F_; }
    else { task -= 896; W = wd; Wt = (u16*)(ws + WS_WD); Kd = F_; Nd = D_; }
    const int nks = Kd / 32;
    const int ks = task % nks, nb = task / nks;
    const int col = nb * 64 + lane;
    u16 v[32];
#pragma unroll
    for (int k = 0; k < 32; ++k) v[k] = W[(size_t)(ks * 32 + k) * Nd + col];
#pragma unroll
    for (int q = 0; q < 4; ++q) {
      u32x4 o;
#pragma unroll
      for (int j = 0; j < 4; ++j) o[j] = (u32)v[q * 8 + 2 * j] | ((u32)v[q * 8 + 2 * j + 1] << 16);
      st16(Wt + ((size_t)((col >> 4) * nks + ks) * 64 + q * 16 + (col & 15)) * 8, o);
    }
    DIAG_END(ws);
  }
}

constexpr int G1_STAGE = 22 * 1024;
constexpr int G1_NBUF = 3;
constexpr int K2_LDS = 77824;

constexpr int UC_LEN = 2080;
constexpr int KR_LEN = 3840;
constexpr int CV_CH_BYTES = 4 * UC_LEN * 2 + 2 * KR_LEN * 2;
constexpr int CV_CH_STRIDE = CV_CH_BYTES + 4224 + 2592;

__device__ __forceinline__ float gelu_tanh(float g) {
  const float c0 = -2.f * 0.7978845608028654f * 1.44269504088896341f;
  const float c1 = c0 * 0.044715f;
  const float e = __builtin_amdgcn_exp2f(g * fmaf(g * g, c1, c0));
  return g * __builtin_amdgcn_rcpf(1.f + e);
}

__device__ __forceinline__ void glds16(const void* g, void* l) {
  __builtin_amdgcn_global_load_lds((const __attribute__((address_space(1))) u32*)g,
                                   (__attribute__((address_space(3))) u32*)l, 16, 0, 0);
}

__global__ __launch_bounds__(256, 2) void k2_kernel(const u16* __restrict__ dskip, char* __restrict__ ws) {
  extern __shared__ __attribute__((aligned(16))) char smem[];
  const int tid = threadIdx.x, lane = tid & 63, wid = tid >> 6;
  DIAG_START(1)
  const int bid = blockIdx.x;
  if (bid < 256) {
    const int xcd = bid & 7, loc = bid >> 3;
    const int mt = (xcd >> 1) * 4 + (loc & 3);
    const int nt = (xcd & 1) * 8 + (loc >> 2);
    const u16* xnt = (const u16*)(ws + WS_XNT);
    const u16* wgt = (const u16*)(ws + WS_WG);
    const u16* wut = (const u16*)(ws + WS_WU);
    auto wsrc = [&](int slot) -> const u16* {
      const int t = slot >> 1, isup = slot & 1;
      return (isup ? wut : wgt) + (size_t)(nt * 7 + t) * 16 * 512;
    };
    auto stage = [&](int ks, int buf) {
      char* base = smem + buf * G1_STAGE;
#pragma unroll
      for (int i = 0; i < 6; ++i) {
        const int f = wid + i * 4;
        if (f < 8) glds16(xnt + ((size_t)(mt * 8 + f) * 16 + ks) * 512 + lane * 8, base + f * 1024 + lane * 16);
        else if (f < 22) glds16(wsrc(f - 8) + (size_t)ks * 512 + lane * 8, base + f * 1024 + lane * 16);
      }
    };
    const int wm = wid >> 1, wn = wid & 1;
    f32x4 acc[7][4] = {};
    stage(0, 0);
    stage(1, 1);
    for (int ks = 0; ks < 16; ++ks) {
      if (ks < 15) {
        if (wid < 2) asm volatile("s_waitcnt vmcnt(6)" ::: "memory");
        else asm volatile("s_waitcnt vmcnt(5)" ::: "memory");
      } else {
        asm volatile("s_waitcnt vmcnt(0)" ::: "memory");
      }
      asm volatile("s_waitcnt lgkmcnt(0)" ::: "memory");
      __builtin_amdgcn_s_barrier();
      if (ks == 0) DIAG_STAMP(1);
      if (ks == 4) DIAG_STAMP(2);
      if (ks == 8) DIAG_STAMP(3);
      if (ks == 12) DIAG_STAMP(4);
      if (ks + 2 < 16) stage(ks + 2, (ks + 2) % 3);
      const char* base = smem + (ks % 3) * G1_STAGE;
      bf16x8 xb[4], wa[7];
#pragma unroll
      for (int i = 0; i < 4; ++i) xb[i] = *(const bf16x8*)(base + (wm * 4 + i) * 1024 + lane * 16);
#pragma unroll
      for (int j = 0; j < 7; ++j) wa[j] = *(const bf16x8*)(base + (8 + wn * 7 + j) * 1024 + lane * 16);
#pragma unroll
      for (int j = 0; j < 7; ++j)
#pragma unroll
        for (int i = 0; i < 4; ++i) acc[j][i] = __builtin_amdgcn_mfma_f32_16x16x32_bf16(wa[j], xb[i], acc[j][i], 0, 0, 0);
    }
    DIAG_STAMP(5);
    __syncthreads();
    float* ex = (float*)smem;
    if (wn == 0) {
#pragma unroll
      for (int i = 0; i < 4; ++i)
#pragma unroll
        for (int r = 0; r < 4; ++r) ex[((wm * 4 + i) * 4 + r) * 64 + lane] = acc[6][i][r];
    }
    __syncthreads();
    u16* hT = (u16*)(ws + WS_H);
    auto emit = [&](int t, const f32x4& g, const f32x4& u, int i) {
      float hv[4];
#pragma unroll
      for (int r = 0; r < 4; ++r) hv[r] = rbf(gelu_tanh(rbf(g[r]))) * rbf(u[r]);
      const int T = nt * 7 + t;
      const int ks2 = T >> 1;
      const int q = 2 * (T & 1) + (lane >> 5);
      const int m16 = mt * 8 + wm * 4 + i;
      u32x2 o;
      o[0] = pack2(hv[0], hv[1]);
      o[1] = pack2(hv[2], hv[3]);
      st8(hT + ((size_t)(m16 * 56 + ks2) * 64 + q * 16 + (lane & 15)) * 8 + ((lane >> 4) & 1) * 4, o);
    };
    if (wn == 0) {
#pragma unroll
      for (int t = 0; t < 3; ++t)
#pragma unroll
        for (int i = 0; i < 4; ++i) emit(t, acc[2 * t][i], acc[2 * t + 1][i], i);
    } else {
#pragma unroll
      for (int i = 0; i < 4; ++i) {
        f32x4 g;
#pragma unroll
        for (int r = 0; r < 4; ++r) g[r] = ex[((wm * 4 + i) * 4 + r) * 64 + lane];
        emit(3, g, acc[0][i], i);
      }
#pragma unroll
      for (int t = 4; t < 7; ++t)
#pragma unroll
        for (int i = 0; i < 4; ++i) emit(t, acc[2 * t - 7][i], acc[2 * t - 6][i], i);
    }
    DIAG_END(ws);
    return;
  }
  {
    const int cb = bid - 256;
    const int ch = wid >> 1, w2 = wid & 1;
    const int d = cb * 2 + ch;
    const int t2 = tid & 127;
    char* cbase = smem + ch * CV_CH_STRIDE;
    u16* ucp = (u16*)cbase;
    u16* krh = (u16*)(cbase + 4 * UC_LEN * 2);
    u16* krl = krh + KR_LEN;
    const u16* ug = (const u16*)(ws + WS_XNTR) + (size_t)d * L_;
    const float* kc = (const float*)(ws + WS_KC) + (size_t)d * KC_STRIDE;
    u32x4 uv0 = *(const u32x4*)(ug + t2 * 8), uv1 = *(const u32x4*)(ug + (t2 + 128) * 8);
    float kv[6];
#pragma unroll
    for (int j = 0; j < 6; ++j) { const int i = t2 + j * 128; kv[j] = (i < 641) ? kc[i] : 0.f; }
    const float dsk = bf2f(dskip[d]);
    u32x4* uraw = (u32x4*)(cbase + CV_CH_BYTES);
    u32* kraw = (u32*)(cbase + CV_CH_BYTES + 4224);
    if (t2 < 4) { uraw[t2] = u32x4{0, 0, 0, 0}; uraw[260 + t2] = u32x4{0, 0, 0, 0}; }
    uraw[4 + t2] = uv0;
    uraw[4 + 128 + t2] = uv1;
#pragma unroll
    for (int j = 0; j < 6; ++j) {
      const int i = t2 + j * 128;
      const u16 hi = f2bf(kv[j]);
      const u16 lo = f2bf(kv[j] - bf2f(hi));
      if (i < 648) kraw[i] = (u32)hi | ((u32)lo << 16);
    }
    __syncthreads();
    DIAG_STAMP(1);
    for (int tb = t2; tb < UC_LEN / 8; tb += 128) {
      u32 w[8];
      u32x4 a = uraw[tb], b = uraw[tb + 1];
#pragma unroll
      for (int j = 0; j < 4; ++j) { w[j] = a[j]; w[4 + j] = b[j]; }
      u32x4 o0, o1, o2, o3;
#pragma unroll
      for (int j = 0; j < 4; ++j) {
        o0[j] = (w[j] >> 16) | (w[j + 1] << 16);
        o1[j] = w[j + 1];
        o2[j] = (w[j + 1] >> 16) | (w[j + 2] << 16);
        o3[j] = w[j + 2];
      }
      *(u32x4*)(ucp + 0 * UC_LEN + tb * 8) = o0;
      *(u32x4*)(ucp + 1 * UC_LEN + tb * 8) = o1;
      *(u32x4*)(ucp + 2 * UC_LEN + tb * 8) = o2;
      *(u32x4*)(ucp + 3 * UC_LEN + tb * 8) = o3;
    }
#pragma unroll
    for (int it = 0; it < 3; ++it) {
      const int tb = t2 + it * 128;
      u32x4 oh = {0, 0, 0, 0}, ol = {0, 0, 0, 0};
      if (it < 2) {
        u32 kk[8];
#pragma unroll
        for (int j = 0; j < 8; ++j) {
          const int jj = 2047 - (tb * 8 + j);
          const int cb = (int)f2bf((float)jj) - 17024;
          kk[j] = kraw[jj < 128 ? jj : cb];
        }
#pragma unroll
        for (int j = 0; j < 4; ++j) {
          oh[j] = __builtin_amdgcn_perm(kk[2 * j + 1], kk[2 * j], 0x05040100u);
          ol[j] = __builtin_amdgcn_perm(kk[2 * j + 1], kk[2 * j], 0x07060302u);
        }
      }
      const int pa = tb * 8 + 8 * (tb >> 2);
      *(u32x4*)(krh + pa) = oh;
      *(u32x4*)(krl + pa) = ol;
    }
    __syncthreads();
    DIAG_STAMP(2);
    const int r = lane & 31, h = lane >> 5;
    const u16* ap = ucp + (r & 3) * UC_LEN + 8 * h + 4 * (r >> 2);
    const int bofs0 = 2048 - 32 * (r + 1) + 8 * h + 8 * (63 - r);
    const int bofs1 = bofs0 - 1024 - 256;
    f32x16 acc0 = {}, acc1 = {};
    int ao2 = (int)((const char*)ap - smem) + 8;
    asm volatile("" : "+v"(ao2));
    auto lda = [&](int s) -> bf16x8 {
      bf16x4 lo = *(const bf16x4*)(ap + 16 * s);
      bf16x4 hi = *(const bf16x4*)(smem + ao2 + 32 * s);
      return __builtin_shufflevector(lo, hi, 0, 1, 2, 3, 4, 5, 6, 7);
    };
    auto both = [&](int sb, int se) {
      bf16x8 a = lda(sb);
      int so = 16 * sb + 8 * (sb >> 1);
      bf16x8 b0h = *(const bf16x8*)(krh + bofs0 + so), b0l = *(const bf16x8*)(krl + bofs0 + so);
      bf16x8 b1h = *(const bf16x8*)(krh + bofs1 + so), b1l = *(const bf16x8*)(krl + bofs1 + so);
#pragma unroll 4
      for (int s = sb; s < se; ++s) {
        const int sn = s + 1;
        const int son = 16 * sn + 8 * (sn >> 1);
        bf16x8 an = lda(sn);
        bf16x8 n0h = *(const bf16x8*)(krh + bofs0 + son), n0l = *(const bf16x8*)(krl + bofs0 + son);
        bf16x8 n1h = *(const bf16x8*)(krh + bofs1 + son), n1l = *(const bf16x8*)(krl + bofs1 + son);
        __builtin_amdgcn_sched_barrier(0);
        acc0 = __builtin_amdgcn_mfma_f32_32x32x16_bf16(a, b0h, acc0, 0, 0, 0);
        acc1 = __builtin_amdgcn_mfma_f32_32x32x16_bf16(a, b1h, acc1, 0, 0, 0);
        acc0 = __builtin_amdgcn_mfma_f32_32x32x16_bf16(a, b0l, acc0, 0, 0, 0);
        acc1 = __builtin_amdgcn_mfma_f32_32x32x16_bf16(a, b1l, acc1, 0, 0, 0);
        __builtin_amdgcn_sched_barrier(0);
        a = an; b0h = n0h; b0l = n0l; b1h = n1h; b1l = n1l;
      }
    };
    if (__builtin_amdgcn_readfirstlane(w2) == 0) {
      both(0, 48);
    } else {
      both(48, 64);
      bf16x8 a0 = lda(64), a1 = lda(65);
      bf16x8 h0 = *(const bf16x8*)(krh + bofs1 + 16 * 64 + 8 * 32), l0 = *(const bf16x8*)(krl + bofs1 + 16 * 64 + 8 * 32);
      bf16x8 h1 = *(const bf16x8*)(krh + bofs1 + 16 * 65 + 8 * 32), l1 = *(const bf16x8*)(krl + bofs1 + 16 * 65 + 8 * 32);
#pragma unroll 4
      for (int s = 64; s < 128; s += 2) {
        const int sn = (s + 2) & 127;
        const int son = 16 * sn + 8 * (sn >> 1);
        bf16x8 na0 = lda(sn), na1 = lda(sn + 1);
        bf16x8 nh0 = *(const bf16x8*)(krh + bofs1 + son), nl0 = *(const bf16x8*)(krl + bofs1 + son);
        bf16x8 nh1 = *(const bf16x8*)(krh + bofs1 + son + 16), nl1 = *(const bf16x8*)(krl + bofs1 + son + 16);
        __builtin_amdgcn_sched_barrier(0);
        acc1 = __builtin_amdgcn_mfma_f32_32x32x16_bf16(a0, h0, acc1, 0, 0, 0);
        acc1 = __builtin_amdgcn_mfma_f32_32x32x16_bf16(a0, l0, acc1, 0, 0, 0);
        acc1 = __builtin_amdgcn_mfma_f32_32x32x16_bf16(a1, h1, acc1, 0, 0, 0);
        acc1 = __builtin_amdgcn_mfma_f32_32x32x16_bf16(a1, l1, acc1, 0, 0, 0);
        __builtin_amdgcn_sched_barrier(0);
        a0 = na0; a1 = na1; h0 = nh0; l0 = nl0; h1 = nh1; l1 = nl1;
      }
    }
    DIAG_STAMP(3);
    __syncthreads();
    DIAG_STAMP(4);
    float* red = (float*)cbase;
#pragma unroll
    for (int g = 0; g < 4; ++g) {
      f32x4 v0 = {acc0[4 * g], acc0[4 * g + 1], acc0[4 * g + 2], acc0[4 * g + 3]};
      f32x4 v1 = {acc1[4 * g], acc1[4 * g + 1], acc1[4 * g + 2], acc1[4 * g + 3]};
      *(f32x4*)(red + ((w2 * 2 + 0) * 32 + r) * 36 + 8 * g + 4 * h) = v0;
      *(f32x4*)(red + ((w2 * 2 + 1) * 32 + r) * 36 + 8 * g + 4 * h) = v1;
    }
    __syncthreads();
    u16* at = (u16*)(ws + WS_AT) + (size_t)d * L_;
#pragma unroll
    for (int it = 0; it < 2; ++it) {
      const int c = t2 + it * 128;
      const int Ta = c >> 2, b0 = (c & 3) * 8;
      const float* p0 = red + (0 * 64 + Ta) * 36 + b0;
      const float* p1 = red + (1 * 64 + Ta) * 36 + b0;
      const u32x4 uv = it ? uv1 : uv0;
      u32x4 o;
#pragma unroll
      for (int j = 0; j < 4; ++j) {
        float y0 = rbf(p0[2 * j] + p1[2 * j]), y1 = rbf(p0[2 * j + 1] + p1[2 * j + 1]);
        float u0 = __uint_as_float(uv[j] << 16), u1 = __uint_as_float(uv[j] & 0xffff0000u);
        o[j] = pack2(y0 + rbf(u0 * dsk), y1 + rbf(u1 * dsk));
      }
      st16(at + c * 8, o);
    }
    DIAG_END(ws);
  }
}

__global__ __launch_bounds__(256, 1) void k3_kernel(const u16* __restrict__ x, const char* __restrict__ ws,
                                                    u16* __restrict__ out) {
  __shared__ __attribute__((aligned(16))) float red[4 * 64 * 68];
  __shared__ __attribute__((aligned(16))) u16 atile[64 * 72];
  const int tid = threadIdx.x, lane = tid & 63, wid = tid >> 6;
  DIAG_START(2)
  const int bid = blockIdx.x;
  const int xcd = bid & 7, loc = bid >> 3;
  const int mt = xcd * 4 + (loc & 3);
  const int nt = loc >> 2;
  const u16* hT = (const u16*)(ws + WS_H);
  const u16* wdt = (const u16*)(ws + WS_WD);
  const u16* at = (const u16*)(ws + WS_AT);
  u32x4 av[2];
#pragma unroll
  for (int it = 0; it < 2; ++it) {
    const int c = tid + it * 256;
    av[it] = *(const u32x4*)(at + (size_t)(nt * 64 + (c >> 3)) * L_ + mt * 64 + (c & 7) * 8);
  }
  f32x4 acc[4][4] = {};
  const int ks0 = wid * 14;
  const u16* hp = hT + ((size_t)(mt * 4) * 56 + ks0) * 512 + lane * 8;
  const u16* wp = wdt + ((size_t)(nt * 4) * 56 + ks0) * 512 + lane * 8;
  bf16x8 hb[7][4], wa[7][4];
#pragma unroll
  for (int k = 0; k < 7; ++k)
#pragma unroll
    for (int i = 0; i < 4; ++i) {
      hb[k][i] = *(const bf16x8*)(hp + (size_t)(i * 56 + k) * 512);
      wa[k][i] = *(const bf16x8*)(wp + (size_t)(i * 56 + k) * 512);
    }
  __builtin_amdgcn_sched_barrier(0);
#pragma unroll
  for (int k = 0; k < 7; ++k) {
#pragma unroll
    for (int j = 0; j < 4; ++j)
#pragma unroll
      for (int i = 0; i < 4; ++i) acc[j][i] = __builtin_amdgcn_mfma_f32_16x16x32_bf16(wa[k][j], hb[k][i], acc[j][i], 0, 0, 0);
#pragma unroll
    for (int i = 0; i < 4; ++i) {
      hb[k][i] = *(const bf16x8*)(hp + (size_t)(i * 56 + k + 7) * 512);
      wa[k][i] = *(const bf16x8*)(wp + (size_t)(i * 56 + k + 7) * 512);
    }
    __builtin_amdgcn_sched_barrier(0);
  }
#pragma unroll
  for (int k = 0; k < 7; ++k)
#pragma unroll
    for (int j = 0; j < 4; ++j)
#pragma unroll
      for (int i = 0; i < 4; ++i) acc[j][i] = __builtin_amdgcn_mfma_f32_16x16x32_bf16(wa[k][j], hb[k][i], acc[j][i], 0, 0, 0);
  DIAG_STAMP(1);
#pragma unroll
  for (int j = 0; j < 4; ++j)
#pragma unroll
    for (int i = 0; i < 4; ++i)
      *(f32x4*)(red + (wid * 64 + i * 16 + (lane & 15)) * 68 + j * 16 + 4 * (lane >> 4)) = acc[j][i];
#pragma unroll
  for (int it = 0; it < 2; ++it) {
    const int c = tid + it * 256;
    *(u32x4*)(atile + (c >> 3) * 72 + (c & 7) * 8) = av[it];
  }
  __syncthreads();
#pragma unroll
  for (int it = 0; it < 2; ++it) {
    const int c = tid + it * 256;
    const int m = c >> 3, n0 = (c & 7) * 8;
    float s[8];
#pragma unroll
    for (int j = 0; j < 8; ++j) s[j] = 0.f;
#pragma unroll
    for (int w = 0; w < 4; ++w) {
      f32x4 p0 = *(const f32x4*)(red + (w * 64 + m) * 68 + n0);
      f32x4 p1 = *(const f32x4*)(red + (w * 64 + m) * 68 + n0 + 4);
#pragma unroll
      for (int j = 0; j < 4; ++j) { s[j] += p0[j]; s[4 + j] += p1[j]; }
    }
    const size_t go = (size_t)(mt * 64 + m) * D_ + nt * 64 + n0;
    u32x4 xv = *(const u32x4*)(x + go);
    u32x4 o;
#pragma unroll
    for (int j = 0; j < 4; ++j) {
      float x0 = __uint_as_float(xv[j] << 16), x1 = __uint_as_float(xv[j] & 0xffff0000u);
      float a0 = bf2f(atile[(n0 + 2 * j) * 72 + m]), a1 = bf2f(atile[(n0 + 2 * j + 1) * 72 + m]);
      o[j] = pack2(rbf(x0 + a0) + rbf(s[2 * j]), rbf(x1 + a1) + rbf(s[2 * j + 1]));
    }
    *(u32x4*)(out + go) = o;
  }
  DIAG_END(ws);
}

extern "C" void kernel_launch(void* const* d_in, const int* in_sizes, int n_in, void* d_out, int out_size,
                              void* d_ws, size_t ws_size, hipStream_t stream) {
  const u16* x = (const u16*)d_in[0];
  const u16* logA = (const u16*)d_in[1];
  const u16* Aim = (const u16*)d_in[2];
  const u16* Cre = (const u16*)d_in[3];
  const u16* Cim = (const u16*)d_in[4];
  const u16* logdt = (const u16*)d_in[5];
  const u16* dskip = (const u16*)d_in[6];
  const u16* wg = (const u16*)d_in[8];
  const u16* wu = (const u16*)d_in[9];
  const u16* wd = (const u16*)d_in[10];
  char* ws = (char*)d_ws;
  hipFuncSetAttribute((const void*)k2_kernel, hipFuncAttributeMaxDynamicSharedMemorySize, K2_LDS);
  k1_kernel<<<K1_KG_BLOCKS + K1_LN_BLOCKS + K1_WT_BLOCKS, 256, 0, stream>>>(x, logA, Aim, Cre, Cim, logdt, wg, wu, wd, ws);
  k2_kernel<<<512, 256, K2_LDS, stream>>>(dskip, ws);
  k3_kernel<<<256, 256, 0, stream>>>(x, ws, (u16*)d_out);
}
```

```cpp
#include <hip/hip_runtime.h>
#include <hip/hip_bf16.h>

typedef unsigned short u16;
typedef unsigned int u32;
using bf16x8 = __attribute__((ext_vector_type(8))) short;
using f16x8 = __attribute__((ext_vector_type(8))) _Float16;
using f16x4 = __attribute__((ext_vector_type(4))) _Float16;
using f32x4 = __attribute__((ext_vector_type(4))) float;
using f32x16 = __attribute__((ext_vector_type(16))) float;
using u32x4 = __attribute__((ext_vector_type(4))) unsigned int;
using u32x2 = __attribute__((ext_vector_type(2))) unsigned int;
typedef float f2 __attribute__((ext_vector_type(2)));
typedef __attribute__((address_space(1))) unsigned gu32;

constexpr int L_ = 2048, D_ = 512, N_ = 128, F_ = 1792;
constexpr int KC_STRIDE = 648;

constexpr size_t WS_CNT = 0;
constexpr size_t WS_XNT = 4096;
constexpr size_t WS_XNTR = WS_XNT + (size_t)L_ * D_ * 2;
constexpr size_t WS_KC = WS_XNTR + (size_t)L_ * D_ * 2;
constexpr size_t WS_H = WS_KC + (size_t)D_ * KC_STRIDE * 4;
constexpr size_t WS_WG = WS_H + (size_t)L_ * F_ * 2;
constexpr size_t WS_WU = WS_WG + (size_t)D_ * F_ * 2;
constexpr size_t WS_WD = WS_WU + (size_t)D_ * F_ * 2;
constexpr size_t WS_AT = WS_WD + (size_t)D_ * F_ * 2;

#define DIAG_START(k)
#define DIAG_STAMP(i)
#define DIAG_END(wsp)

__device__ __forceinline__ float bf2f(u16 v) { return __uint_as_float(((u32)v) << 16); }
__device__ __forceinline__ u16 f2bf(float f) {
  __bf16 b = (__bf16)f;
  return __builtin_bit_cast(u16, b);
}
__device__ __forceinline__ float rbf(float f) { return bf2f(f2bf(f)); }
__device__ __forceinline__ u32 pack2(float lo, float hi) {
  typedef __bf16 b2 __attribute__((ext_vector_type(2)));
  b2 r = __builtin_convertvector(f2{lo, hi}, b2);
  return __builtin_bit_cast(u32, r);
}
__device__ __forceinline__ f2 unpack2(u32 w) { return f2{__uint_as_float(w << 16), __uint_as_float(w & 0xffff0000u)}; }

__device__ __forceinline__ void st16_wt(void* p, u32x4 v) { asm volatile("global_store_dwordx4 %0, %1, off sc1\n\ts_nop 1" ::"v"(p), "v"(v) : "memory"); }
__device__ __forceinline__ void st16(void* p, u32x4 v) { *(u32x4*)p = v; }
__device__ __forceinline__ void st8(void* p, u32x2 v) { *(u32x2*)p = v; }

#define DPP_F(v, ctrl) __builtin_bit_cast(float, __builtin_amdgcn_update_dpp(0, __builtin_bit_cast(int, v), ctrl, 0xf, 0xf, true))
__device__ __forceinline__ float wave_sum(float v) {
  v += DPP_F(v, 0xB1);
  v += DPP_F(v, 0x4E);
  v += DPP_F(v, 0x141);
  v += DPP_F(v, 0x140);
  const int iv = __builtin_bit_cast(int, v);
  return (__builtin_bit_cast(float, __builtin_amdgcn_readlane(iv, 0)) + __builtin_bit_cast(float, __builtin_amdgcn_readlane(iv, 16))) +
         (__builtin_bit_cast(float, __builtin_amdgcn_readlane(iv, 32)) + __builtin_bit_cast(float, __builtin_amdgcn_readlane(iv, 48)));
}
__device__ __forceinline__ float wave_max(float v) {
  v = fmaxf(v, DPP_F(v, 0xB1));
  v = fmaxf(v, DPP_F(v, 0x4E));
  v = fmaxf(v, DPP_F(v, 0x141));
  v = fmaxf(v, DPP_F(v, 0x140));
  const int iv = __builtin_bit_cast(int, v);
  return fmaxf(fmaxf(__builtin_bit_cast(float, __builtin_amdgcn_readlane(iv, 0)), __builtin_bit_cast(float, __builtin_amdgcn_readlane(iv, 16))),
               fmaxf(__builtin_bit_cast(float, __builtin_amdgcn_readlane(iv, 32)), __builtin_bit_cast(float, __builtin_amdgcn_readlane(iv, 48))));
}

constexpr int K1_LN_BLOCKS = 128;
constexpr int K1_WT_BLOCKS = 336;
constexpr int K1_KG_BLOCKS = 512;

__device__ __forceinline__ void cmul6(f2& r0, f2& r1, f2& r2, f2& r3, f2& r4, f2& r5, f2 a0, f2 b0, f2 a1, f2 b1,
                                      f2 a2, f2 b2, f2 a3, f2 b3, f2 a4, f2 b4, f2 a5, f2 b5) {
  asm("v_pk_mul_f32 %0, %6, %7 op_sel:[1,1] op_sel_hi:[1,0] neg_lo:[0,1]\n\t"
      "v_pk_mul_f32 %1, %8, %9 op_sel:[1,1] op_sel_hi:[1,0] neg_lo:[0,1]\n\t"
      "v_pk_mul_f32 %2, %10, %11 op_sel:[1,1] op_sel_hi:[1,0] neg_lo:[0,1]\n\t"
      "v_pk_mul_f32 %3, %12, %13 op_sel:[1,1] op_sel_hi:[1,0] neg_lo:[0,1]\n\t"
      "v_pk_mul_f32 %4, %14, %15 op_sel:[1,1] op_sel_hi:[1,0] neg_lo:[0,1]\n\t"
      "v_pk_mul_f32 %5, %16, %17 op_sel:[1,1] op_sel_hi:[1,0] neg_lo:[0,1]\n\t"
      "v_pk_fma_f32 %0, %6, %7, %0 op_sel:[0,0,0] op_sel_hi:[0,1,1]\n\t"
      "v_pk_fma_f32 %1, %8, %9, %1 op_sel:[0,0,0] op_sel_hi:[0,1,1]\n\t"
      "v_pk_fma_f32 %2, %10, %11, %2 op_sel:[0,0,0] op_sel_hi:[0,1,1]\n\t"
      "v_pk_fma_f32 %3, %12, %13, %3 op_sel:[0,0,0] op_sel_hi:[0,1,1]\n\t"
      "v_pk_fma_f32 %4, %14, %15, %4 op_sel:[0,0,0] op_sel_hi:[0,1,1]\n\t"
      "v_pk_fma_f32 %5, %16, %17, %5 op_sel:[0,0,0] op_sel_hi:[0,1,1]\n\t"
      "s_nop 1"
      : "=&v"(r0), "=&v"(r1), "=&v"(r2), "=&v"(r3), "=&v"(r4), "=&v"(r5)
      : "v"(a0), "v"(b0), "v"(a1), "v"(b1), "v"(a2), "v"(b2), "v"(a3), "v"(b3), "v"(a4), "v"(b4), "v"(a5), "v"(b5));
}
__device__ __forceinline__ void cmul2(f2& r0, f2& r1, f2 a0, f2 b0, f2 a1, f2 b1) {
  asm("v_pk_mul_f32 %0, %2, %3 op_sel:[1,1] op_sel_hi:[1,0] neg_lo:[0,1]\n\t"
      "v_pk_mul_f32 %1, %4, %5 op_sel:[1,1] op_sel_hi:[1,0] neg_lo:[0,1]\n\t"
      "s_nop 0\n\t"
      "v_pk_fma_f32 %0, %2, %3, %0 op_sel:[0,0,0] op_sel_hi:[0,1,1]\n\t"
      "v_pk_fma_f32 %1, %4, %5, %1 op_sel:[0,0,0] op_sel_hi:[0,1,1]\n\t"
      "s_nop 1"
      : "=&v"(r0), "=&v"(r1)
      : "v"(a0), "v"(b0), "v"(a1), "v"(b1));
}
__device__ __forceinline__ f2 cexp_(float zr, float zi, float t) {
  const float m = __builtin_amdgcn_exp2f((zr * t) * 1.44269504088896341f);
  float r = (zi * t) * 0.15915494309189535f;
  r -= rintf(r);
  return f2{m * __builtin_amdgcn_cosf(r), m * __builtin_amdgcn_sinf(r)};
}

__global__ __launch_bounds__(256) void k1_kernel(const u16* __restrict__ x, const u16* __restrict__ logA,
                                                 const u16* __restrict__ Aim, const u16* __restrict__ Cre,
                                                 const u16* __restrict__ Cim, const u16* __restrict__ logdt,
                                                 const u16* __restrict__ wg, const u16* __restrict__ wu,
                                                 const u16* __restrict__ wd, char* __restrict__ ws) {
  __shared__ __attribute__((aligned(16))) char smem[18432];
  const int tid = threadIdx.x, lane = tid & 63, wid = tid >> 6;
  DIAG_START(0)
  int bid = blockIdx.x - K1_KG_BLOCKS;
  if (bid >= 0 && bid < K1_LN_BLOCKS) {
    __builtin_amdgcn_s_setprio(3);
    if (bid == 0 && tid < 8) *(u32*)(ws + WS_CNT + tid * 256) = 0u;
    u16* tile = (u16*)smem;
    u16* xnt = (u16*)(ws + WS_XNT);
    const int r0 = bid * 16;
    u32x4 v[4];
#pragma unroll
    for (int i = 0; i < 4; ++i) v[i] = *(const u32x4*)(x + (size_t)(r0 + wid * 4 + i) * D_ + lane * 8);
#pragma unroll
    for (int i = 0; i < 4; ++i) {
      const int rl = wid * 4 + i, row = r0 + rl;
      f2 f[4];
#pragma unroll
      for (int j = 0; j < 4; ++j) f[j] = unpack2(v[i][j]);
      const f2 s2 = (f[0] + f[1]) + (f[2] + f[3]);
      const float mean = wave_sum(s2.x + s2.y) * (1.f / D_);
      f2 q2 = {0.f, 0.f};
#pragma unroll
      for (int j = 0; j < 4; ++j) { const f2 t = f[j] - mean; q2 = __builtin_elementwise_fma(t, t, q2); }
      const float q = wave_sum(q2.x + q2.y);
      const float mb = rbf(mean);
      const float vb = rbf(q * (1.f / D_));
      const float sd = rbf(sqrtf(rbf(vb + 1e-5f)));
      const float rs = __builtin_amdgcn_rcpf(sd);
      u32x4 o;
#pragma unroll
      for (int j = 0; j < 4; ++j) {
        const f2 t = f[j] - mb;
        const f2 a = unpack2(pack2(t.x, t.y));
        f2 qq = a * rs;
        const f2 rem = __builtin_elementwise_fma(-qq, f2{sd, sd}, a);
        qq = __builtin_elementwise_fma(rem, f2{rs, rs}, qq);
        o[j] = pack2(qq.x, qq.y);
      }
      st16(xnt + ((size_t)((row >> 4) * 16 + (lane >> 2)) * 64 + (lane & 3) * 16 + (row & 15)) * 8, o);
      *(u32x4*)(tile + rl * 520 + lane * 8) = o;
    }
    DIAG_STAMP(1);
    __syncthreads();
    u16* xtr = (u16*)(ws + WS_XNTR);
#pragma unroll
    for (int h = 0; h < 2; ++h) {
      const int d = tid + h * 256;
#pragma unroll
      for (int g = 0; g < 2; ++g) {
        u32x4 o;
#pragma unroll
        for (int j = 0; j < 4; ++j)
          o[j] = (u32)tile[(g * 8 + 2 * j) * 520 + d] | ((u32)tile[(g * 8 + 2 * j + 1) * 520 + d] << 16);
        st16(xtr + (size_t)d * L_ + r0 + g * 8, o);
      }
    }
    DIAG_END(ws);
    return;
  }
  bid -= K1_LN_BLOCKS;
  if (bid >= 0) {
    __builtin_amdgcn_s_setprio(2);
    int task = bid * 4 + wid;
    const u16* W; u16* Wt; int Kd, Nd;
    if (task < 448) { W = wg; Wt = (u16*)(ws + WS_WG); Kd = D_; Nd = F_; }
    else if (task < 896) { task -= 448; W = wu; Wt = (u16*)(ws + WS_WU); Kd = D_; Nd = F_; }
    else { task -= 896; W = wd; Wt = (u16*)(ws + WS_WD); Kd = F_; Nd = D_; }
    const int nks = Kd / 32;
    const int ks = task % nks, nb = task / nks;
    const int col = nb * 64 + lane;
    u16 v[32];
#pragma unroll
    for (int k = 0; k < 32; ++k) v[k] = W[(size_t)(ks * 32 + k) * Nd + col];
#pragma unroll
    for (int q = 0; q < 4; ++q) {
      u32x4 o;
#pragma unroll
      for (int j = 0; j < 4; ++j) o[j] = (u32)v[q * 8 + 2 * j] | ((u32)v[q * 8 + 2 * j + 1] << 16);
      st16(Wt + ((size_t)((col >> 4) * nks + ks) * 64 + q * 16 + (col & 15)) * 8, o);
    }
    DIAG_END(ws);
    return;
  }
  bid = blockIdx.x;
  {
    const int d = bid;
    float4* prm = (float4*)(smem + 16384);
    if (tid < 128) {
      const u16 la = logA[d * N_ + tid], ai = Aim[d * N_ + tid], cr = Cre[d * N_ + tid], ci = Cim[d * N_ + tid];
      const float dt = rbf(expf(bf2f(logdt[d])));
      const float ar = -rbf(expf(bf2f(la)));
      prm[tid] = make_float4(ar * dt, bf2f(ai) * dt, bf2f(cr) * dt, bf2f(ci) * dt);
    }
    __syncthreads();
    DIAG_STAMP(1);
    const int kq = lane >> 4, idx = lane & 15;
    f32x4 acc[4] = {};
    const float ty = (float)idx, te = (float)(idx == 0 ? 256 : 16 * idx);
    float4 pr[8];
#pragma unroll
    for (int s = 0; s < 8; ++s) pr[s] = prm[wid * 32 + s * 4 + kq];
#pragma unroll
    for (int s = 0; s < 8; s += 2) {
      const float4 pa = pr[s], pb = pr[s + 1];
      const f2 Ca = f2{pa.z, -pa.w}, Cb = f2{pb.z, -pb.w};
      f2 Ya = cexp_(pa.x, pa.y, ty), Yb = cexp_(pb.x, pb.y, ty);
      f2 Ea = cexp_(pa.x, -pa.y, te), Eb = cexp_(pb.x, -pb.y, te);
      f2 Fa = (idx == 0) ? f2{1.f, 0.f} : Ea, Fb = (idx == 0) ? f2{1.f, 0.f} : Eb;
#pragma unroll
      for (int e = 0; e < 3; ++e) {
        f2 nYa, nYb, nEa, nEb, Xa, Xb;
        cmul6(Xa, Xb, nYa, nYb, nEa, nEb, Ca, Fa, Cb, Fb, Ya, Ya, Yb, Yb, Ea, Ea, Eb, Eb);
        acc[e] = __builtin_amdgcn_mfma_f32_16x16x4f32(Xa.x, Ya.x, acc[e], 0, 0, 0);
        acc[(e + 1) & 3] = acc[(e + 1) & 3];
        acc[e] = __builtin_amdgcn_mfma_f32_16x16x4f32(Xa.y, Ya.y, acc[e], 0, 0, 0);
        acc[e] = __builtin_amdgcn_mfma_f32_16x16x4f32(Xb.x, Yb.x, acc[e], 0, 0, 0);
        acc[e] = __builtin_amdgcn_mfma_f32_16x16x4f32(Xb.y, Yb.y, acc[e], 0, 0, 0);
        Ya = nYa; Yb = nYb; Ea = nEa; Eb = nEb; Fa = nEa; Fb = nEb;
      }
      f2 Xa, Xb;
      cmul2(Xa, Xb, Ca, Ea, Cb, Eb);
      acc[3] = __builtin_amdgcn_mfma_f32_16x16x4f32(Xa.x, Ya.x, acc[3], 0, 0, 0);
      acc[3] = __builtin_amdgcn_mfma_f32_16x16x4f32(Xa.y, Ya.y, acc[3], 0, 0, 0);
      acc[3] = __builtin_amdgcn_mfma_f32_16x16x4f32(Xb.x, Yb.x, acc[3], 0, 0, 0);
      acc[3] = __builtin_amdgcn_mfma_f32_16x16x4f32(Xb.y, Yb.y, acc[3], 0, 0, 0);
    }
    DIAG_STAMP(2);
    float* red = (float*)smem;
#pragma unroll
    for (int e = 0; e < 4; ++e)
#pragma unroll
      for (int r = 0; r < 4; ++r) red[(wid * 4 + e) * 256 + ((lane >> 4) * 4 + r) * 16 + (lane & 15)] = acc[e][r];
    __syncthreads();
    float* kc = (float*)(ws + WS_KC) + (size_t)d * KC_STRIDE;
#pragma unroll
    for (int e = 0; e < 4; ++e) {
      const float v = 2.f * ((red[(0 * 4 + e) * 256 + tid] + red[(1 * 4 + e) * 256 + tid]) +
                             (red[(2 * 4 + e) * 256 + tid] + red[(3 * 4 + e) * 256 + tid]));
      if (e == 0) kc[tid] = v;
      else if (tid >= 128) kc[128 * e + tid] = v;
      else if (e == 3 && tid == 0) kc[640] = v;
    }
    DIAG_END(ws);
  }
}

constexpr int G1_STAGE = 22 * 1024;
constexpr int G1_NBUF = 7;
constexpr int K2_LDS = G1_STAGE * G1_NBUF;

__device__ __forceinline__ f2 gelu2(f2 g) {
  const float c0 = -2.f * 0.7978845608028654f * 1.44269504088896341f;
  const float c1 = c0 * 0.044715f;
  const f2 t = g * __builtin_elementwise_fma(g * g, f2{c1, c1}, f2{c0, c0});
  const f2 dn = f2{__builtin_amdgcn_exp2f(t.x), __builtin_amdgcn_exp2f(t.y)} + 1.f;
  return g * f2{__builtin_amdgcn_rcpf(dn.x), __builtin_amdgcn_rcpf(dn.y)};
}

__device__ __forceinline__ void glds16(const void* g, void* l) {
  __builtin_amdgcn_global_load_lds((const __attribute__((address_space(1))) u32*)g,
                                   (__attribute__((address_space(3))) u32*)l, 16, 0, 0);
}

__global__ __launch_bounds__(512, 2) void k2_kernel(char* __restrict__ ws) {
  extern __shared__ __attribute__((aligned(16))) char smem[];
  const int tid = threadIdx.x, lane = tid & 63;
  const int wid = __builtin_amdgcn_readfirstlane(tid >> 6);
  DIAG_START(1)
  const int bid = blockIdx.x;
  const int xcd = bid & 7, loc = bid >> 3;
  const int mt = (xcd >> 1) * 4 + (loc & 3);
  const int nt = (xcd & 1) * 8 + (loc >> 2);
  const u16* xnt = (const u16*)(ws + WS_XNT);
  const u16* wgt = (const u16*)(ws + WS_WG);
  const u16* wut = (const u16*)(ws + WS_WU);
  auto src = [&](int f) -> const u16* {
    if (f < 8) return xnt + (size_t)(mt * 8 + f) * 16 * 512;
    const int s = f - 8;
    return ((s & 1) ? wut : wgt) + (size_t)(nt * 7 + (s >> 1)) * 16 * 512;
  };
  const u16* s0 = src(wid) + lane * 8;
  const u16* s1 = src(wid + 8) + lane * 8;
  const u16* s2 = src(wid < 6 ? wid + 16 : 21) + lane * 8;
  auto stage = [&](int ks) {
    char* base = smem + (ks % G1_NBUF) * G1_STAGE + wid * 1024 + lane * 16;
    glds16(s0 + ks * 512, base);
    glds16(s1 + ks * 512, base + 8192);
    if (wid < 6) glds16(s2 + ks * 512, base + 16384);
  };
  const int wm = wid >> 1, wn = wid & 1;
  f32x4 acc[7][2] = {};
  bf16x8 xa[2], wa[7], xb[2], wb[7];
  auto rd = [&](int ks, bf16x8(&xf)[2], bf16x8(&wf)[7]) {
    const char* base = smem + (ks % G1_NBUF) * G1_STAGE + lane * 16;
#pragma unroll
    for (int i = 0; i < 2; ++i) xf[i] = *(const bf16x8*)(base + (wm * 2 + i) * 1024);
#pragma unroll
    for (int j = 0; j < 7; ++j) wf[j] = *(const bf16x8*)(base + (8 + wn * 7 + j) * 1024);
  };
  auto mm = [&](bf16x8(&xf)[2], bf16x8(&wf)[7]) {
#pragma unroll
    for (int j = 0; j < 7; ++j)
#pragma unroll
      for (int i = 0; i < 2; ++i) acc[j][i] = __builtin_amdgcn_mfma_f32_16x16x32_bf16(wf[j], xf[i], acc[j][i], 0, 0, 0);
  };
#define G1_WAIT(n) do { if (wid < 6) asm volatile("s_waitcnt vmcnt(%0)" ::"n"(3 * (n)) : "memory"); \
                        else asm volatile("s_waitcnt vmcnt(%0)" ::"n"(2 * (n)) : "memory"); } while (0)
#define G1_BAR() do { asm volatile("s_waitcnt lgkmcnt(0)" ::: "memory"); __builtin_amdgcn_s_barrier(); } while (0)
#pragma unroll
  for (int k = 0; k < 7; ++k) stage(k);
  G1_WAIT(6);
  G1_BAR();
  DIAG_STAMP(1);
  rd(0, xa, wa);
#define G1_ITER(k, XC, WC, XN, WN, NLEFT)                         \
  do {                                                            \
    G1_WAIT(NLEFT);                                               \
    G1_BAR();                                                     \
    rd((k) + 1, XN, WN);                                          \
    if ((k) + 7 < 16) stage((k) + 7);                             \
    __builtin_amdgcn_sched_barrier(0);                            \
    mm(XC, WC);                                                   \
    __builtin_amdgcn_sched_barrier(0);                            \
  } while (0)
  G1_ITER(0, xa, wa, xb, wb, 5);
  G1_ITER(1, xb, wb, xa, wa, 5);
  G1_ITER(2, xa, wa, xb, wb, 5);
  G1_ITER(3, xb, wb, xa, wa, 5);
  G1_ITER(4, xa, wa, xb, wb, 5);
  G1_ITER(5, xb, wb, xa, wa, 5);
  G1_ITER(6, xa, wa, xb, wb, 5);
  G1_ITER(7, xb, wb, xa, wa, 5);
  DIAG_STAMP(2);
  G1_ITER(8, xa, wa, xb, wb, 5);
  G1_ITER(9, xb, wb, xa, wa, 5);
  G1_ITER(10, xa, wa, xb, wb, 4);
  G1_ITER(11, xb, wb, xa, wa, 3);
  G1_ITER(12, xa, wa, xb, wb, 2);
  G1_ITER(13, xb, wb, xa, wa, 1);
  G1_ITER(14, xa, wa, xb, wb, 0);
  mm(xb, wb);
  DIAG_STAMP(3);
  __syncthreads();
  f32x4* ex = (f32x4*)smem;
  if (wn == 0) {
#pragma unroll
    for (int i = 0; i < 2; ++i) ex[(wm * 2 + i) * 64 + lane] = acc[6][i];
  }
  __syncthreads();
  u16* hT = (u16*)(ws + WS_H);
  auto emit = [&](int t, const f32x4& g, const f32x4& u, int i) {
    u32x2 o;
#pragma unroll
    for (int p = 0; p < 2; ++p) {
      const f2 gb = unpack2(pack2(g[2 * p], g[2 * p + 1]));
      const f2 ub = unpack2(pack2(u[2 * p], u[2 * p + 1]));
      const f2 ge = gelu2(gb);
      const f2 hv = unpack2(pack2(ge.x, ge.y)) * ub;
      o[p] = pack2(hv.x, hv.y);
    }
    const int T = nt * 7 + t;
    const int ks2 = T >> 1;
    const int q = 2 * (T & 1) + (lane >> 5);
    const int m16 = mt * 8 + wm * 2 + i;
    st8(hT + ((size_t)(m16 * 56 + ks2) * 64 + q * 16 + (lane & 15)) * 8 + ((lane >> 4) & 1) * 4, o);
  };
  if (wn == 0) {
#pragma unroll
    for (int t = 0; t < 3; ++t)
#pragma unroll
      for (int i = 0; i < 2; ++i) emit(t, acc[2 * t][i], acc[2 * t + 1][i], i);
  } else {
#pragma unroll
    for (int i = 0; i < 2; ++i) emit(3, ex[(wm * 2 + i) * 64 + lane], acc[0][i], i);
#pragma unroll
    for (int t = 4; t < 7; ++t)
#pragma unroll
      for (int i = 0; i < 2; ++i) emit(t, acc[2 * t - 7][i], acc[2 * t - 6][i], i);
  }
  DIAG_END(ws);
}

constexpr int UC_LEN = 2080;
constexpr int KR_LEN = 3840;
constexpr int CV_UCP = 0;
constexpr int CV_KR = CV_UCP + 4 * UC_LEN * 2;
constexpr int CV_URAW = CV_KR + KR_LEN * 2;
constexpr int CV_KRAW = CV_URAW + 4224;
constexpr int CV_MAX = CV_KRAW + 2592;
constexpr int CV_CH_STRIDE = 33280;
constexpr int G2_RED = 4 * 64 * 68 * 4;
constexpr int K3_LDS = G2_RED + 64 * 72 * 2;

__device__ __forceinline__ u32 bf2_to_h2(u32 w) {
  auto r = __builtin_amdgcn_cvt_pkrtz(__uint_as_float(w << 16), __uint_as_float(w & 0xffff0000u));
  return __builtin_bit_cast(u32, r);
}

__global__ __launch_bounds__(512, 4) void k3_kernel(const u16* __restrict__ x, const u16* __restrict__ dskip,
                                                    char* __restrict__ ws, u16* __restrict__ out) {
  extern __shared__ __attribute__((aligned(16))) char smem[];
  const int tid = threadIdx.x, lane = tid & 63;
  const int wid = __builtin_amdgcn_readfirstlane(tid >> 6);
  DIAG_START(2)
  const int bid = blockIdx.x;
  if (bid < 256) {
    const int ch = wid >> 2, w4 = wid & 3;
    const int d = bid * 2 + ch;
    const int t2 = tid & 255;
    char* cbase = smem + ch * CV_CH_STRIDE;
    u16* ucp = (u16*)(cbase + CV_UCP);
    u16* kr = (u16*)(cbase + CV_KR);
    u32x4* uraw = (u32x4*)(cbase + CV_URAW);
    float* kraw = (float*)(cbase + CV_KRAW);
    float* kmax = (float*)(cbase + CV_MAX);
    const u16* ug = (const u16*)(ws + WS_XNTR) + (size_t)d * L_;
    const float* kc = (const float*)(ws + WS_KC) + (size_t)d * KC_STRIDE;
    const u32x4 uv0 = *(const u32x4*)(ug + t2 * 8);
    const float kv0 = kc[t2], kv1 = kc[t2 + 256], kv2 = (t2 < 129) ? kc[t2 + 512] : 0.f;
    const float dsk = bf2f(dskip[d]);
    if (t2 < 4) { uraw[t2] = u32x4{0, 0, 0, 0}; uraw[260 + t2] = u32x4{0, 0, 0, 0}; }
    {
      u32x4 h;
#pragma unroll
      for (int j = 0; j < 4; ++j) h[j] = bf2_to_h2(uv0[j]);
      uraw[4 + t2] = h;
    }
    kraw[t2] = kv0;
    kraw[t2 + 256] = kv1;
    if (t2 < 136) kraw[t2 + 512] = kv2;
    {
      const float mx = wave_max(fmaxf(fmaxf(fabsf(kv0), fabsf(kv1)), fabsf(kv2)));
      if (lane == 0) kmax[w4] = mx;
    }
    __syncthreads();
    DIAG_STAMP(1);
    float kscale, kinv;
    {
      const float mx = fmaxf(fmaxf(kmax[0], kmax[1]), fmaxf(kmax[2], kmax[3]));
      int e = (int)((__float_as_uint(mx) >> 23) & 0xff);
      e = min(max(e, 20), 240);
      kscale = __uint_as_float((u32)(127 + 13 + 127 - e) << 23);
      kinv = __uint_as_float((u32)(e - 13) << 23);
    }
    for (int tb = t2; tb < UC_LEN / 8; tb += 256) {
      u32 w[8];
      u32x4 a = uraw[tb], b = uraw[tb + 1];
#pragma unroll
      for (int j = 0; j < 4; ++j) { w[j] = a[j]; w[4 + j] = b[j]; }
      u32x4 o0, o1, o2, o3;
#pragma unroll
      for (int j = 0; j < 4; ++j) {
        o0[j] = __builtin_amdgcn_alignbit(w[j + 1], w[j], 16);
        o1[j] = w[j + 1];
        o2[j] = __builtin_amdgcn_alignbit(w[j + 2], w[j + 1], 16);
        o3[j] = w[j + 2];
      }
      *(u32x4*)(ucp + 0 * UC_LEN + tb * 8) = o0;
      *(u32x4*)(ucp + 1 * UC_LEN + tb * 8) = o1;
      *(u32x4*)(ucp + 2 * UC_LEN + tb * 8) = o2;
      *(u32x4*)(ucp + 3 * UC_LEN + tb * 8) = o3;
    }
    {
      float kk[8];
#pragma unroll
      for (int j = 0; j < 8; ++j) {
        const int jj = 2047 - (t2 * 8 + j);
        const int cbi = (int)f2bf((float)jj) - 17024;
        kk[j] = kraw[jj < 128 ? jj : cbi] * kscale;
      }
      f16x8 o;
#pragma unroll
      for (int j = 0; j < 8; ++j) o[j] = (_Float16)kk[j];
      *(f16x8*)(kr + t2 * 8 + 8 * (t2 >> 2)) = o;
      if (t2 < 128) { const int tb = t2 + 256; *(u32x4*)(kr + tb * 8 + 8 * (tb >> 2)) = u32x4{0, 0, 0, 0}; }
    }
    __syncthreads();
    DIAG_STAMP(2);
    const int r = lane & 31, h = lane >> 5;
    const u16* ap = ucp + (r & 3) * UC_LEN + 8 * h + 4 * (r >> 2);
    const int bofs0 = 2048 - 32 * (r + 1) + 8 * h + 8 * (63 - r);
    const int bofs1 = bofs0 - 1024 - 256;
    f32x16 acc0 = {}, acc1 = {};
    int ao2 = (int)((const char*)ap - smem) + 8;
    asm volatile("" : "+v"(ao2));
    auto lda = [&](int s) -> f16x8 {
      f16x4 lo = *(const f16x4*)(ap + 16 * s);
      f16x4 hi = *(const f16x4*)(smem + ao2 + 32 * s);
      return __builtin_shufflevector(lo, hi, 0, 1, 2, 3, 4, 5, 6, 7);
    };
    auto ldb = [&](int ofs, int s) -> f16x8 { return *(const f16x8*)(kr + ofs + 16 * s + 8 * (s >> 1)); };
    auto both = [&](int sb, int se) {
      f16x8 a0 = lda(sb), a1 = lda(sb + 1);
      f16x8 p0 = ldb(bofs0, sb), p1 = ldb(bofs0, sb + 1), q0 = ldb(bofs1, sb), q1 = ldb(bofs1, sb + 1);
#pragma unroll 2
      for (int s = sb; s < se; s += 2) {
        const int sn = s + 2;
        f16x8 na0 = lda(sn), na1 = lda(sn + 1);
        f16x8 np0 = ldb(bofs0, sn), np1 = ldb(bofs0, sn + 1), nq0 = ldb(bofs1, sn), nq1 = ldb(bofs1, sn + 1);
        __builtin_amdgcn_sched_barrier(0);
        acc0 = __builtin_amdgcn_mfma_f32_32x32x16_f16(a0, p0, acc0, 0, 0, 0);
        acc1 = __builtin_amdgcn_mfma_f32_32x32x16_f16(a0, q0, acc1, 0, 0, 0);
        acc0 = __builtin_amdgcn_mfma_f32_32x32x16_f16(a1, p1, acc0, 0, 0, 0);
        acc1 = __builtin_amdgcn_mfma_f32_32x32x16_f16(a1, q1, acc1, 0, 0, 0);
        __builtin_amdgcn_sched_barrier(0);
        a0 = na0; a1 = na1; p0 = np0; p1 = np1; q0 = nq0; q1 = nq1;
      }
    };
    auto one = [&](int sb, int se) {
      f16x8 a0 = lda(sb), a1 = lda(sb + 1), q0 = ldb(bofs1, sb), q1 = ldb(bofs1, sb + 1);
#pragma unroll 2
      for (int s = sb; s < se; s += 2) {
        const int sn = (s + 2) & 127;
        f16x8 na0 = lda(sn), na1 = lda(sn + 1), nq0 = ldb(bofs1, sn), nq1 = ldb(bofs1, sn + 1);
        __builtin_amdgcn_sched_barrier(0);
        acc1 = __builtin_amdgcn_mfma_f32_32x32x16_f16(a0, q0, acc1, 0, 0, 0);
        acc1 = __builtin_amdgcn_mfma_f32_32x32x16_f16(a1, q1, acc1, 0, 0, 0);
        __builtin_amdgcn_sched_barrier(0);
        a0 = na0; a1 = na1; q0 = nq0; q1 = nq1;
      }
    };
    if (w4 == 0) both(0, 24);
    else if (w4 == 1) both(24, 48);
    else if (w4 == 2) { both(48, 64); one(64, 80); }
    else one(80, 128);
    DIAG_STAMP(3);
    __syncthreads();
    DIAG_STAMP(4);
    float* red = (float*)cbase;
#pragma unroll
    for (int g = 0; g < 4; ++g) {
      f32x4 v0 = {acc0[4 * g], acc0[4 * g + 1], acc0[4 * g + 2], acc0[4 * g + 3]};
      f32x4 v1 = {acc1[4 * g], acc1[4 * g + 1], acc1[4 * g + 2], acc1[4 * g + 3]};
      if (w4 < 3) *(f32x4*)(red + (w4 * 32 + r) * 36 + 8 * g + 4 * h) = v0;
      *(f32x4*)(red + ((3 + w4) * 32 + r) * 36 + 8 * g + 4 * h) = v1;
    }
    __syncthreads();
    u16* at = (u16*)(ws + WS_AT) + (size_t)d * L_;
    {
      const int c = t2;
      const int T = c >> 7, a = (c >> 2) & 31, b0 = (c & 3) * 8;
      f32x4 s0 = {}, s1 = {};
      const float* p = red + ((T ? 3 : 0) * 32 + a) * 36 + b0;
#pragma unroll
      for (int w = 0; w < 3; ++w) { s0 += *(const f32x4*)(p + w * 32 * 36); s1 += *(const f32x4*)(p + w * 32 * 36 + 4); }
      if (T) { s0 += *(const f32x4*)(p + 3 * 32 * 36); s1 += *(const f32x4*)(p + 3 * 32 * 36 + 4); }
      u32x4 o;
#pragma unroll
      for (int j = 0; j < 4; ++j) {
        const f2 y = unpack2((j < 2) ? pack2(s0[2 * j] * kinv, s0[2 * j + 1] * kinv) : pack2(s1[2 * j - 4] * kinv, s1[2 * j - 3] * kinv));
        const f2 us = unpack2(uv0[j]) * dsk;
        const f2 t = unpack2(pack2(us.x, us.y)) + y;
        o[j] = pack2(t.x, t.y);
      }
      st16_wt(at + c * 8, o);
    }
    asm volatile("s_waitcnt vmcnt(0)" ::: "memory");
    __syncthreads();
    if (tid == 0) __hip_atomic_fetch_add((gu32*)(ws + WS_CNT + (bid >> 5) * 256), 1u, __ATOMIC_RELAXED, __HIP_MEMORY_SCOPE_AGENT);
    DIAG_END(ws);
    return;
  }
  {
    const int gb = bid - 256;
    const int xcd = gb & 7, loc = gb >> 3;
    const int mt = xcd * 4 + (loc & 3);
    const int nt = loc >> 2;
    const u16* hT = (const u16*)(ws + WS_H);
    const u16* wdt = (const u16*)(ws + WS_WD);
    float* red = (float*)smem;
    u16* atile = (u16*)(smem + G2_RED);
    f32x4 acc[4][4] = {};
    const int ks0 = wid * 7;
    const __amdgpu_buffer_rsrc_t hr = __builtin_amdgcn_make_buffer_rsrc((void*)(hT + ((size_t)(mt * 4) * 56 + ks0) * 512), 0, 4 * 56 * 1024, 0x00020000);
    const __amdgpu_buffer_rsrc_t wr = __builtin_amdgcn_make_buffer_rsrc((void*)(wdt + ((size_t)(nt * 4) * 56 + ks0) * 512), 0, 4 * 56 * 1024, 0x00020000);
    const int vo = lane * 16;
#define G2_LD(rs, i, k) __builtin_bit_cast(bf16x8, __builtin_amdgcn_raw_buffer_load_b128(rs, vo, ((i) * 56 + (k)) * 1024, 0))
    bf16x8 ha[4], hb[4], wa[4];
#pragma unroll
    for (int i = 0; i < 4; ++i) { ha[i] = G2_LD(hr, i, 0); wa[i] = G2_LD(wr, i, 0); }
#define G2_STEP(k, HC, HN)                                                                             \
  do {                                                                                                 \
    if ((k) + 1 < 7) {                                                                                 \
      _Pragma("unroll") for (int i = 0; i < 4; ++i) HN[i] = G2_LD(hr, i, (k) + 1);                    \
    }                                                                                                  \
    _Pragma("unroll") for (int j = 0; j < 4; ++j) {                                                    \
      __builtin_amdgcn_sched_barrier(0);                                                               \
      _Pragma("unroll") for (int i = 0; i < 4; ++i)                                                    \
        acc[j][i] = __builtin_amdgcn_mfma_f32_16x16x32_bf16(wa[j], HC[i], acc[j][i], 0, 0, 0);         \
      __builtin_amdgcn_sched_barrier(0);                                                               \
      if ((k) + 1 < 7) wa[j] = G2_LD(wr, j, (k) + 1);                                                  \
    }                                                                                                  \
  } while (0)
    G2_STEP(0, ha, hb);
    G2_STEP(1, hb, ha);
    G2_STEP(2, ha, hb);
    G2_STEP(3, hb, ha);
    G2_STEP(4, ha, hb);
    G2_STEP(5, hb, ha);
    G2_STEP(6, ha, hb);
    DIAG_STAMP(1);
    const int w3 = wid & 3;
    float* rp = red + (w3 * 64 + (lane & 15)) * 68 + 4 * (lane >> 4);
    if (wid >= 4) {
#pragma unroll
      for (int j = 0; j < 4; ++j)
#pragma unroll
        for (int i = 0; i < 4; ++i) *(f32x4*)(rp + i * 16 * 68 + j * 16) = acc[j][i];
    }
    __syncthreads();
    if (wid < 4) {
#pragma unroll
      for (int j = 0; j < 4; ++j)
#pragma unroll
        for (int i = 0; i < 4; ++i) { f32x4* q = (f32x4*)(rp + i * 16 * 68 + j * 16); *q = *q + acc[j][i]; }
    }
    if (tid == 511) {
      gu32* cnt = (gu32*)(ws + WS_CNT + nt * 256);
      for (unsigned spins = 0; spins < (1u << 22); ++spins) {
        if (__hip_atomic_load(cnt, __ATOMIC_RELAXED, __HIP_MEMORY_SCOPE_AGENT) >= 32u) break;
        __builtin_amdgcn_s_sleep(1);
      }
    }
    __syncthreads();
    DIAG_STAMP(2);
    {
      const u16* at = (const u16*)(ws + WS_AT) + (size_t)(nt * 64 + (tid >> 3)) * L_ + mt * 64 + (tid & 7) * 8;
      u32x4 av;
      asm volatile("global_load_dwordx4 %0, %1, off sc1\n\ts_waitcnt vmcnt(0)" : "=&v"(av) : "v"(at) : "memory");
      *(u32x4*)(atile + (tid >> 3) * 72 + (tid & 7) * 8) = av;
    }
    const int m = tid >> 3, n0 = (tid & 7) * 8;
    const size_t go = (size_t)(mt * 64 + m) * D_ + nt * 64 + n0;
    const u32x4 xv = *(const u32x4*)(x + go);
    f32x4 p0 = {}, p1 = {};
#pragma unroll
    for (int w = 0; w < 4; ++w) {
      p0 += *(const f32x4*)(red + (w * 64 + m) * 68 + n0);
      p1 += *(const f32x4*)(red + (w * 64 + m) * 68 + n0 + 4);
    }
    __syncthreads();
    u32x4 o;
#pragma unroll
    for (int j = 0; j < 4; ++j) {
      const f2 xx = unpack2(xv[j]);
      const f2 aa = f2{bf2f(atile[(n0 + 2 * j) * 72 + m]), bf2f(atile[(n0 + 2 * j + 1) * 72 + m])};
      const f2 r1 = xx + aa;
      const f2 bb = unpack2((j < 2) ? pack2(p0[2 * j], p0[2 * j + 1]) : pack2(p1[2 * j - 4], p1[2 * j - 3]));
      const f2 r2 = unpack2(pack2(r1.x, r1.y)) + bb;
      o[j] = pack2(r2.x, r2.y);
    }
    *(u32x4*)(out + go) = o;
    DIAG_END(ws);
  }
}

extern "C" void kernel_launch(void* const* d_in, const int* in_sizes, int n_in, void* d_out, int out_size,
                              void* d_ws, size_t ws_size, hipStream_t stream) {
  const u16* x = (const u16*)d_in[0];
  const u16* logA = (const u16*)d_in[1];
  const u16* Aim = (const u16*)d_in[2];
  const u16* Cre = (const u16*)d_in[3];
  const u16* Cim = (const u16*)d_in[4];
  const u16* logdt = (const u16*)d_in[5];
  const u16* dskip = (const u16*)d_in[6];
  const u16* wg = (const u16*)d_in[8];
  const u16* wu = (const u16*)d_in[9];
  const u16* wd = (const u16*)d_in[10];
  char* ws = (char*)d_ws;
  (void)hipFuncSetAttribute((const void*)k2_kernel, hipFuncAttributeMaxDynamicSharedMemorySize, K2_LDS);
  (void)hipFuncSetAttribute((const void*)k3_kernel, hipFuncAttributeMaxDynamicSharedMemorySize, K3_LDS);
  k1_kernel<<<K1_LN_BLOCKS + K1_WT_BLOCKS + K1_KG_BLOCKS, 256, 0, stream>>>(x, logA, Aim, Cre, Cim, logdt, wg, wu, wd, ws);
  k2_kernel<<<256, 512, K2_LDS, stream>>>(ws);
  k3_kernel<<<512, 512, K3_LDS, stream>>>(x, dskip, ws, (u16*)d_out);
}
```

```cpp
#include <hip/hip_runtime.h>
#include <hip/hip_bf16.h>

typedef unsigned short u16;
typedef unsigned int u32;
using bf16x8 = __attribute__((ext_vector_type(8))) short;
using f16x8 = __attribute__((ext_vector_type(8))) _Float16;
using f16x4 = __attribute__((ext_vector_type(4))) _Float16;
using f32x4 = __attribute__((ext_vector_type(4))) float;
using f32x16 = __attribute__((ext_vector_type(16))) float;
using u32x4 = __attribute__((ext_vector_type(4))) unsigned int;
using u32x2 = __attribute__((ext_vector_type(2))) unsigned int;
typedef float f2 __attribute__((ext_vector_type(2)));
typedef __attribute__((address_space(1))) unsigned gu32;

constexpr int L_ = 2048, D_ = 512, N_ = 128, F_ = 1792;
constexpr int KC_STRIDE = 648;

constexpr size_t WS_CNT = 0;
constexpr size_t WS_XNT = 8192;
constexpr size_t WS_XNTR = WS_XNT + (size_t)L_ * D_ * 2;
constexpr size_t WS_KC = WS_XNTR + (size_t)L_ * D_ * 2;
constexpr size_t WS_H = WS_KC + (size_t)D_ * KC_STRIDE * 4;
constexpr size_t WS_WG = WS_H + (size_t)L_ * F_ * 2;
constexpr size_t WS_WU = WS_WG + (size_t)D_ * F_ * 2;
constexpr size_t WS_WD = WS_WU + (size_t)D_ * F_ * 2;
constexpr size_t WS_AT = WS_WD + (size_t)D_ * F_ * 2;

#define DIAG_START(k)
#define DIAG_STAMP(i)
#define DIAG_END(wsp)
#define DIAG_STAMP2(i)

__device__ __forceinline__ float bf2f(u16 v) { return __uint_as_float(((u32)v) << 16); }
__device__ __forceinline__ u16 f2bf(float f) {
  __bf16 b = (__bf16)f;
  return __builtin_bit_cast(u16, b);
}
__device__ __forceinline__ float rbf(float f) { return bf2f(f2bf(f)); }
__device__ __forceinline__ u32 pack2(float lo, float hi) {
  typedef __bf16 b2 __attribute__((ext_vector_type(2)));
  b2 r = __builtin_convertvector(f2{lo, hi}, b2);
  return __builtin_bit_cast(u32, r);
}
__device__ __forceinline__ f2 unpack2(u32 w) { return f2{__uint_as_float(w << 16), __uint_as_float(w & 0xffff0000u)}; }

__device__ __forceinline__ void st16_wt(void* p, u32x4 v) { asm volatile("global_store_dwordx4 %0, %1, off sc1\n\ts_nop 1" ::"v"(p), "v"(v) : "memory"); }
__device__ __forceinline__ void st16(void* p, u32x4 v) { *(u32x4*)p = v; }
__device__ __forceinline__ void st8(void* p, u32x2 v) { *(u32x2*)p = v; }

#define DPP_F(v, ctrl) __builtin_bit_cast(float, __builtin_amdgcn_update_dpp(0, __builtin_bit_cast(int, v), ctrl, 0xf, 0xf, true))
__device__ __forceinline__ float wave_sum(float v) {
  v += DPP_F(v, 0xB1);
  v += DPP_F(v, 0x4E);
  v += DPP_F(v, 0x141);
  v += DPP_F(v, 0x140);
  const int iv = __builtin_bit_cast(int, v);
  return (__builtin_bit_cast(float, __builtin_amdgcn_readlane(iv, 0)) + __builtin_bit_cast(float, __builtin_amdgcn_readlane(iv, 16))) +
         (__builtin_bit_cast(float, __builtin_amdgcn_readlane(iv, 32)) + __builtin_bit_cast(float, __builtin_amdgcn_readlane(iv, 48)));
}
__device__ __forceinline__ float wave_max(float v) {
  v = fmaxf(v, DPP_F(v, 0xB1));
  v = fmaxf(v, DPP_F(v, 0x4E));
  v = fmaxf(v, DPP_F(v, 0x141));
  v = fmaxf(v, DPP_F(v, 0x140));
  const int iv = __builtin_bit_cast(int, v);
  return fmaxf(fmaxf(__builtin_bit_cast(float, __builtin_amdgcn_readlane(iv, 0)), __builtin_bit_cast(float, __builtin_amdgcn_readlane(iv, 16))),
               fmaxf(__builtin_bit_cast(float, __builtin_amdgcn_readlane(iv, 32)), __builtin_bit_cast(float, __builtin_amdgcn_readlane(iv, 48))));
}

constexpr int K1_LN_BLOCKS = 128;
constexpr int K1_WT_BLOCKS = 336;

__global__ __launch_bounds__(256) void k1_kernel(const u16* __restrict__ x, const u16* __restrict__ wg,
                                                 const u16* __restrict__ wu, const u16* __restrict__ wd,
                                                 char* __restrict__ ws) {
  __shared__ __attribute__((aligned(16))) char smem[16 * 520 * 2];
  const int tid = threadIdx.x, lane = tid & 63, wid = tid >> 6;
  DIAG_START(0)
  int bid = blockIdx.x;
  if (bid < K1_LN_BLOCKS) {
    if (bid == 0 && tid < 24) *(u32*)(ws + WS_CNT + tid * 256) = 0u;
    u16* tile = (u16*)smem;
    u16* xnt = (u16*)(ws + WS_XNT);
    const int r0 = bid * 16;
    u32x4 v[4];
#pragma unroll
    for (int i = 0; i < 4; ++i) v[i] = *(const u32x4*)(x + (size_t)(r0 + wid * 4 + i) * D_ + lane * 8);
#pragma unroll
    for (int i = 0; i < 4; ++i) {
      const int rl = wid * 4 + i, row = r0 + rl;
      f2 f[4];
#pragma unroll
      for (int j = 0; j < 4; ++j) f[j] = unpack2(v[i][j]);
      const f2 s2 = (f[0] + f[1]) + (f[2] + f[3]);
      const float mean = wave_sum(s2.x + s2.y) * (1.f / D_);
      f2 q2 = {0.f, 0.f};
#pragma unroll
      for (int j = 0; j < 4; ++j) { const f2 t = f[j] - mean; q2 = __builtin_elementwise_fma(t, t, q2); }
      const float q = wave_sum(q2.x + q2.y);
      const float mb = rbf(mean);
      const float vb = rbf(q * (1.f / D_));
      const float sd = rbf(sqrtf(rbf(vb + 1e-5f)));
      const float rs = __builtin_amdgcn_rcpf(sd);
      u32x4 o;
#pragma unroll
      for (int j = 0; j < 4; ++j) {
        const f2 t = f[j] - mb;
        const f2 a = unpack2(pack2(t.x, t.y));
        f2 qq = a * rs;
        const f2 rem = __builtin_elementwise_fma(-qq, f2{sd, sd}, a);
        qq = __builtin_elementwise_fma(rem, f2{rs, rs}, qq);
        o[j] = pack2(qq.x, qq.y);
      }
      st16(xnt + ((size_t)((row >> 4) * 16 + (lane >> 2)) * 64 + (lane & 3) * 16 + (row & 15)) * 8, o);
      *(u32x4*)(tile + rl * 520 + lane * 8) = o;
    }
    DIAG_STAMP(1);
    __syncthreads();
    u16* xtr = (u16*)(ws + WS_XNTR);
#pragma unroll
    for (int h = 0; h < 2; ++h) {
      const int d = tid + h * 256;
#pragma unroll
      for (int g = 0; g < 2; ++g) {
        u32x4 o;
#pragma unroll
        for (int j = 0; j < 4; ++j)
          o[j] = (u32)tile[(g * 8 + 2 * j) * 520 + d] | ((u32)tile[(g * 8 + 2 * j + 1) * 520 + d] << 16);
        st16(xtr + (size_t)d * L_ + r0 + g * 8, o);
      }
    }
    DIAG_END(ws);
    return;
  }
  bid -= K1_LN_BLOCKS;
  {
    int task = bid * 4 + wid;
    const u16* W; u16* Wt; int Kd, Nd;
    if (task < 448) { W = wg; Wt = (u16*)(ws + WS_WG); Kd = D_; Nd = F_; }
    else if (task < 896) { task -= 448; W = wu; Wt = (u16*)(ws + WS_WU); Kd = D_; Nd = F_; }
    else { task -= 896; W = wd; Wt = (u16*)(ws + WS_WD); Kd = F_; Nd = D_; }
    const int nks = Kd / 32;
    const int ks = task % nks, nb = task / nks;
    const int col = nb * 64 + lane;
    u16 v[32];
#pragma unroll
    for (int k = 0; k < 32; ++k) v[k] = W[(size_t)(ks * 32 + k) * Nd + col];
#pragma unroll
    for (int q = 0; q < 4; ++q) {
      u32x4 o;
#pragma unroll
      for (int j = 0; j < 4; ++j) o[j] = (u32)v[q * 8 + 2 * j] | ((u32)v[q * 8 + 2 * j + 1] << 16);
      st16(Wt + ((size_t)((col >> 4) * nks + ks) * 64 + q * 16 + (col & 15)) * 8, o);
    }
    DIAG_END(ws);
  }
}

constexpr int G1_STAGE = 22 * 1024;
constexpr int G1_NBUF = 5;
constexpr int KG_RED = G1_STAGE * G1_NBUF;
constexpr int KG_PRM = KG_RED + 2 * 16384;
constexpr int KG_KRAW = KG_PRM + 4096;
constexpr int KG_MAX = KG_KRAW + 5184;
constexpr int K2_LDS = KG_MAX + 64;

constexpr int UC_LEN = 2080;
constexpr int KR_LEN = 3840;
constexpr int CV_UCP = 0;
constexpr int CV_KR = CV_UCP + 4 * UC_LEN * 2;
constexpr int CV_URAW = CV_KR + KR_LEN * 2;
constexpr int CV_CH_STRIDE = 36864;
constexpr int G2_RED = 8 * 64 * 68 * 4;
constexpr int G2_AT = G2_RED;

__device__ __forceinline__ void cmul6(f2& r0, f2& r1, f2& r2, f2& r3, f2& r4, f2& r5, f2 a0, f2 b0, f2 a1, f2 b1,
                                      f2 a2, f2 b2, f2 a3, f2 b3, f2 a4, f2 b4, f2 a5, f2 b5) {
  asm("v_pk_mul_f32 %0, %6, %7 op_sel:[1,1] op_sel_hi:[1,0] neg_lo:[0,1]\n\t"
      "v_pk_mul_f32 %1, %8, %9 op_sel:[1,1] op_sel_hi:[1,0] neg_lo:[0,1]\n\t"
      "v_pk_mul_f32 %2, %10, %11 op_sel:[1,1] op_sel_hi:[1,0] neg_lo:[0,1]\n\t"
      "v_pk_mul_f32 %3, %12, %13 op_sel:[1,1] op_sel_hi:[1,0] neg_lo:[0,1]\n\t"
      "v_pk_mul_f32 %4, %14, %15 op_sel:[1,1] op_sel_hi:[1,0] neg_lo:[0,1]\n\t"
      "v_pk_mul_f32 %5, %16, %17 op_sel:[1,1] op_sel_hi:[1,0] neg_lo:[0,1]\n\t"
      "v_pk_fma_f32 %0, %6, %7, %0 op_sel:[0,0,0] op_sel_hi:[0,1,1]\n\t"
      "v_pk_fma_f32 %1, %8, %9, %1 op_sel:[0,0,0] op_sel_hi:[0,1,1]\n\t"
      "v_pk_fma_f32 %2, %10, %11, %2 op_sel:[0,0,0] op_sel_hi:[0,1,1]\n\t"
      "v_pk_fma_f32 %3, %12, %13, %3 op_sel:[0,0,0] op_sel_hi:[0,1,1]\n\t"
      "v_pk_fma_f32 %4, %14, %15, %4 op_sel:[0,0,0] op_sel_hi:[0,1,1]\n\t"
      "v_pk_fma_f32 %5, %16, %17, %5 op_sel:[0,0,0] op_sel_hi:[0,1,1]\n\t"
      "s_nop 1"
      : "=&v"(r0), "=&v"(r1), "=&v"(r2), "=&v"(r3), "=&v"(r4), "=&v"(r5)
      : "v"(a0), "v"(b0), "v"(a1), "v"(b1), "v"(a2), "v"(b2), "v"(a3), "v"(b3), "v"(a4), "v"(b4), "v"(a5), "v"(b5));
}
__device__ __forceinline__ void cmul2(f2& r0, f2& r1, f2 a0, f2 b0, f2 a1, f2 b1) {
  asm("v_pk_mul_f32 %0, %2, %3 op_sel:[1,1] op_sel_hi:[1,0] neg_lo:[0,1]\n\t"
      "v_pk_mul_f32 %1, %4, %5 op_sel:[1,1] op_sel_hi:[1,0] neg_lo:[0,1]\n\t"
      "s_nop 0\n\t"
      "v_pk_fma_f32 %0, %2, %3, %0 op_sel:[0,0,0] op_sel_hi:[0,1,1]\n\t"
      "v_pk_fma_f32 %1, %4, %5, %1 op_sel:[0,0,0] op_sel_hi:[0,1,1]\n\t"
      "s_nop 1"
      : "=&v"(r0), "=&v"(r1)
      : "v"(a0), "v"(b0), "v"(a1), "v"(b1));
}
__device__ __forceinline__ f2 cexp_(float zr, float zi, float t) {
  const float m = __builtin_amdgcn_exp2f((zr * t) * 1.44269504088896341f);
  float r = (zi * t) * 0.15915494309189535f;
  r -= rintf(r);
  return f2{m * __builtin_amdgcn_cosf(r), m * __builtin_amdgcn_sinf(r)};
}
__device__ __forceinline__ f2 gelu2(f2 g) {
  const float c0 = -2.f * 0.7978845608028654f * 1.44269504088896341f;
  const float c1 = c0 * 0.044715f;
  const f2 t = g * __builtin_elementwise_fma(g * g, f2{c1, c1}, f2{c0, c0});
  const f2 dn = f2{__builtin_amdgcn_exp2f(t.x), __builtin_amdgcn_exp2f(t.y)} + 1.f;
  return g * f2{__builtin_amdgcn_rcpf(dn.x), __builtin_amdgcn_rcpf(dn.y)};
}
__device__ __forceinline__ void glds16(const void* g, void* l) {
  __builtin_amdgcn_global_load_lds((const __attribute__((address_space(1))) u32*)g,
                                   (__attribute__((address_space(3))) u32*)l, 16, 0, 0);
}
__device__ __forceinline__ void st8_wt(void* p, u32x2 v) { asm volatile("global_store_dwordx2 %0, %1, off sc1\n\ts_nop 1" ::"v"(p), "v"(v) : "memory"); }
__device__ __forceinline__ u32 bf2_to_h2(u32 w) {
  auto r = __builtin_amdgcn_cvt_pkrtz(__uint_as_float(w << 16), __uint_as_float(w & 0xffff0000u));
  return __builtin_bit_cast(u32, r);
}
__device__ __forceinline__ void wait_count(const char* ws, int idx, unsigned want) {
  gu32* cnt = (gu32*)(ws + WS_CNT + idx * 256);
  for (unsigned spins = 0; spins < (1u << 22); ++spins) {
    if (__hip_atomic_load(cnt, __ATOMIC_RELAXED, __HIP_MEMORY_SCOPE_AGENT) >= want) break;
    __builtin_amdgcn_s_sleep(1);
  }
}
#define RAW_BAR() do { asm volatile("s_waitcnt lgkmcnt(0)" ::: "memory"); __builtin_amdgcn_s_barrier(); } while (0)

__global__ __launch_bounds__(512, 2) void k2_kernel(const u16* __restrict__ x, const u16* __restrict__ logA,
                                                    const u16* __restrict__ Aim, const u16* __restrict__ Cre,
                                                    const u16* __restrict__ Cim, const u16* __restrict__ logdt,
                                                    const u16* __restrict__ dskip, char* __restrict__ ws,
                                                    u16* __restrict__ out) {
  extern __shared__ __attribute__((aligned(16))) char smem[];
  const int tid = threadIdx.x, lane = tid & 63;
  const int wid = __builtin_amdgcn_readfirstlane(tid >> 6);
  DIAG_START(1)
  const int bid = blockIdx.x;
  const int xcd = bid & 7, loc = bid >> 3;
  const int ch = wid >> 2, w4 = wid & 3;
  const int d = bid * 2 + ch;
  const int t2 = tid & 255;
  const int mt = (xcd >> 1) * 4 + (loc & 3);
  const int nt = (xcd & 1) * 8 + (loc >> 2);

  u32x4 uv0; u32 pla, pai, pcr, pci, pdt, pds;
  {
    const u16* ug = (const u16*)(ws + WS_XNTR) + (size_t)d * L_ + t2 * 8;
    const int pi = d * N_ + (t2 & 127);
    asm volatile(
        "global_load_dwordx4 %0, %7, off\n\t"
        "global_load_ushort %1, %8, off\n\t"
        "global_load_ushort %2, %9, off\n\t"
        "global_load_ushort %3, %10, off\n\t"
        "global_load_ushort %4, %11, off\n\t"
        "global_load_ushort %5, %12, off\n\t"
        "global_load_ushort %6, %13, off"
        : "=&v"(uv0), "=&v"(pla), "=&v"(pai), "=&v"(pcr), "=&v"(pci), "=&v"(pdt), "=&v"(pds)
        : "v"(ug), "v"(logA + pi), "v"(Aim + pi), "v"(Cre + pi), "v"(Cim + pi), "v"(logdt + d), "v"(dskip + d)
        : "memory");
  }
  const u16* xnt = (const u16*)(ws + WS_XNT);
  const u16* wgt = (const u16*)(ws + WS_WG);
  const u16* wut = (const u16*)(ws + WS_WU);
  auto src = [&](int f) -> const u16* {
    if (f < 8) return xnt + (size_t)(mt * 8 + f) * 16 * 512;
    const int s = f - 8;
    return ((s & 1) ? wut : wgt) + (size_t)(nt * 7 + (s >> 1)) * 16 * 512;
  };
  const u16* s0 = src(wid) + lane * 8;
  const u16* s1 = src(wid + 8) + lane * 8;
  const u16* s2 = src(wid < 6 ? wid + 16 : 21) + lane * 8;
  auto stage = [&](int ks) {
    char* base = smem + (ks % G1_NBUF) * G1_STAGE + wid * 1024 + lane * 16;
    glds16(s0 + ks * 512, base);
    glds16(s1 + ks * 512, base + 8192);
    if (wid < 6) glds16(s2 + ks * 512, base + 16384);
  };
#define G1_WAIT(n) do { if (wid < 6) asm volatile("s_waitcnt vmcnt(%0)" ::"n"(3 * (n)) : "memory"); \
                        else asm volatile("s_waitcnt vmcnt(%0)" ::"n"(2 * (n)) : "memory"); } while (0)
#pragma unroll
  for (int k = 0; k < G1_NBUF; ++k) stage(k);
  G1_WAIT(G1_NBUF);
  asm volatile("" : "+v"(uv0), "+v"(pla), "+v"(pai), "+v"(pcr), "+v"(pci), "+v"(pdt), "+v"(pds));
  const float dsk = bf2f((u16)pds);

  float* kraw = (float*)(smem + KG_KRAW) + ch * KC_STRIDE;
  float* kmax = (float*)(smem + KG_MAX) + ch * 4;
  {
    float4* prm = (float4*)(smem + KG_PRM) + ch * 128;
    if (t2 < 128) {
      const float dt = rbf(expf(bf2f((u16)pdt)));
      const float ar = -rbf(expf(bf2f((u16)pla)));
      prm[t2] = make_float4(ar * dt, bf2f((u16)pai) * dt, bf2f((u16)pcr) * dt, bf2f((u16)pci) * dt);
    }
    RAW_BAR();
    DIAG_STAMP(1);
    const int kq = lane >> 4, idx = lane & 15;
    f32x4 acc[4] = {};
    const float ty = (float)idx, te = (float)(idx == 0 ? 256 : 16 * idx);
    float4 pr[8];
#pragma unroll
    for (int s = 0; s < 8; ++s) pr[s] = prm[w4 * 32 + s * 4 + kq];
#pragma unroll
    for (int s = 0; s < 8; s += 2) {
      const float4 pa = pr[s], pb = pr[s + 1];
      const f2 Ca = f2{pa.z, -pa.w}, Cb = f2{pb.z, -pb.w};
      f2 Ya[4], Yb[4], Xa[4], Xb[4];
      Ya[0] = cexp_(pa.x, pa.y, ty); Yb[0] = cexp_(pb.x, pb.y, ty);
      f2 Ea = cexp_(pa.x, -pa.y, te), Eb = cexp_(pb.x, -pb.y, te);
      f2 Fa = (idx == 0) ? f2{1.f, 0.f} : Ea, Fb = (idx == 0) ? f2{1.f, 0.f} : Eb;
#pragma unroll
      for (int e = 0; e < 3; ++e) {
        f2 nEa, nEb;
        cmul6(Xa[e], Xb[e], Ya[e + 1], Yb[e + 1], nEa, nEb, Ca, Fa, Cb, Fb, Ya[e], Ya[e], Yb[e], Yb[e], Ea, Ea, Eb, Eb);
        Ea = nEa; Eb = nEb; Fa = nEa; Fb = nEb;
      }
      cmul2(Xa[3], Xb[3], Ca, Ea, Cb, Eb);
#pragma unroll
      for (int e = 0; e < 4; ++e) acc[e] = __builtin_amdgcn_mfma_f32_16x16x4f32(Xa[e].x, Ya[e].x, acc[e], 0, 0, 0);
#pragma unroll
      for (int e = 0; e < 4; ++e) acc[e] = __builtin_amdgcn_mfma_f32_16x16x4f32(Xa[e].y, Ya[e].y, acc[e], 0, 0, 0);
#pragma unroll
      for (int e = 0; e < 4; ++e) acc[e] = __builtin_amdgcn_mfma_f32_16x16x4f32(Xb[e].x, Yb[e].x, acc[e], 0, 0, 0);
#pragma unroll
      for (int e = 0; e < 4; ++e) acc[e] = __builtin_amdgcn_mfma_f32_16x16x4f32(Xb[e].y, Yb[e].y, acc[e], 0, 0, 0);
    }
    float* red = (float*)(smem + KG_RED) + ch * 4096;
#pragma unroll
    for (int e = 0; e < 4; ++e)
#pragma unroll
      for (int r = 0; r < 4; ++r) red[(w4 * 4 + e) * 256 + ((lane >> 4) * 4 + r) * 16 + (lane & 15)] = acc[e][r];
    RAW_BAR();
    float mx = 0.f;
#pragma unroll
    for (int e = 0; e < 4; ++e) {
      const float v = 2.f * ((red[(0 * 4 + e) * 256 + t2] + red[(1 * 4 + e) * 256 + t2]) +
                             (red[(2 * 4 + e) * 256 + t2] + red[(3 * 4 + e) * 256 + t2]));
      if (e == 0) { kraw[t2] = v; mx = fabsf(v); }
      else if (t2 >= 128) { kraw[128 * e + t2] = v; mx = fmaxf(mx, fabsf(v)); }
      else if (e == 3 && t2 == 0) { kraw[640] = v; mx = fmaxf(mx, fabsf(v)); }
    }
    mx = wave_max(mx);
    if (lane == 0) kmax[w4] = mx;
  }
  DIAG_STAMP(2);

  {
    const int wm = wid >> 1, wn = wid & 1;
    f32x4 acc[7][2] = {};
    bf16x8 xa[2], wa[7], xb[2], wb[7];
    G1_WAIT(G1_NBUF - 1);
    RAW_BAR();
    {
      const char* base = smem + lane * 16;
#pragma unroll
      for (int i = 0; i < 2; ++i) xa[i] = *(const bf16x8*)(base + (wm * 2 + i) * 1024);
#pragma unroll
      for (int j = 0; j < 7; ++j) wa[j] = *(const bf16x8*)(base + (8 + wn * 7 + j) * 1024);
    }
#define G1_ITER(k, XC, WC, XN, WN, NLEFT)                                                          \
  do {                                                                                             \
    G1_WAIT(NLEFT);                                                                                \
    RAW_BAR();                                                                                     \
    const char* rb_ = smem + (((k) + 1) % G1_NBUF) * G1_STAGE + lane * 16;                         \
    _Pragma("unroll") for (int j = 0; j < 7; ++j) {                                                \
      acc[j][0] = __builtin_amdgcn_mfma_f32_16x16x32_bf16(WC[j], XC[0], acc[j][0], 0, 0, 0);       \
      if (j < 2) XN[j] = *(const bf16x8*)(rb_ + (wm * 2 + j) * 1024);                              \
      acc[j][1] = __builtin_amdgcn_mfma_f32_16x16x32_bf16(WC[j], XC[1], acc[j][1], 0, 0, 0);       \
      WN[j] = *(const bf16x8*)(rb_ + (8 + wn * 7 + j) * 1024);                                     \
      __builtin_amdgcn_sched_barrier(0);                                                           \
      if (j == 3 && (k) + G1_NBUF < 16) { stage((k) + G1_NBUF); __builtin_amdgcn_sched_barrier(0); } \
    }                                                                                              \
  } while (0)
    G1_ITER(0, xa, wa, xb, wb, 3);
    G1_ITER(1, xb, wb, xa, wa, 3);
    G1_ITER(2, xa, wa, xb, wb, 3);
    G1_ITER(3, xb, wb, xa, wa, 3);
    G1_ITER(4, xa, wa, xb, wb, 3);
    G1_ITER(5, xb, wb, xa, wa, 3);
    G1_ITER(6, xa, wa, xb, wb, 3);
    G1_ITER(7, xb, wb, xa, wa, 3);
    G1_ITER(8, xa, wa, xb, wb, 3);
    G1_ITER(9, xb, wb, xa, wa, 3);
    G1_ITER(10, xa, wa, xb, wb, 3);
    G1_ITER(11, xb, wb, xa, wa, 3);
    G1_ITER(12, xa, wa, xb, wb, 2);
    G1_ITER(13, xb, wb, xa, wa, 1);
    G1_ITER(14, xa, wa, xb, wb, 0);
#pragma unroll
    for (int j = 0; j < 7; ++j)
#pragma unroll
      for (int i = 0; i < 2; ++i) acc[j][i] = __builtin_amdgcn_mfma_f32_16x16x32_bf16(wb[j], xb[i], acc[j][i], 0, 0, 0);
    DIAG_STAMP(3);
    f32x4* ex = (f32x4*)(smem + KG_RED);
    if (wn == 0) {
#pragma unroll
      for (int i = 0; i < 2; ++i) ex[(wm * 2 + i) * 64 + lane] = acc[6][i];
    }
    __syncthreads();
    u16* hT = (u16*)(ws + WS_H);
    auto emit = [&](int t, const f32x4& g, const f32x4& u, int i) {
      u32x2 o;
#pragma unroll
      for (int p = 0; p < 2; ++p) {
        const f2 gb = unpack2(pack2(g[2 * p], g[2 * p + 1]));
        const f2 ub = unpack2(pack2(u[2 * p], u[2 * p + 1]));
        const f2 ge = gelu2(gb);
        const f2 hv = unpack2(pack2(ge.x, ge.y)) * ub;
        o[p] = pack2(hv.x, hv.y);
      }
      const int T = nt * 7 + t;
      const int ks2 = T >> 1;
      const int q = 2 * (T & 1) + (lane >> 5);
      const int m16 = mt * 8 + wm * 2 + i;
      st8_wt(hT + ((size_t)(m16 * 56 + ks2) * 64 + q * 16 + (lane & 15)) * 8 + ((lane >> 4) & 1) * 4, o);
    };
    if (wn == 0) {
#pragma unroll
      for (int t = 0; t < 3; ++t)
#pragma unroll
        for (int i = 0; i < 2; ++i) emit(t, acc[2 * t][i], acc[2 * t + 1][i], i);
    } else {
#pragma unroll
      for (int i = 0; i < 2; ++i) emit(3, ex[(wm * 2 + i) * 64 + lane], acc[0][i], i);
#pragma unroll
      for (int t = 4; t < 7; ++t)
#pragma unroll
        for (int i = 0; i < 2; ++i) emit(t, acc[2 * t - 7][i], acc[2 * t - 6][i], i);
    }
    asm volatile("s_waitcnt vmcnt(0)" ::: "memory");
    __syncthreads();
    if (tid == 0) __hip_atomic_fetch_add((gu32*)(ws + WS_CNT + (8 + mt) * 256), 1u, __ATOMIC_RELAXED, __HIP_MEMORY_SCOPE_AGENT);
  }
  DIAG_STAMP(4);

  const int mt2 = (xcd >> 1) * 8 + (xcd & 1) * 4 + (loc & 3);
  const int nt2 = loc >> 2;
  const int vo = lane * 16;
  const __amdgpu_buffer_rsrc_t hr = __builtin_amdgcn_make_buffer_rsrc((void*)((const u16*)(ws + WS_H) + ((size_t)(mt2 * 4) * 56 + wid * 7) * 512), 0, 4 * 56 * 1024, 0x00020000);
  const __amdgpu_buffer_rsrc_t wr = __builtin_amdgcn_make_buffer_rsrc((void*)((const u16*)(ws + WS_WD) + ((size_t)(nt2 * 4) * 56 + wid * 7) * 512), 0, 4 * 56 * 1024, 0x00020000);
#define G2_LD(rs, i, k, aux) __builtin_bit_cast(bf16x8, __builtin_amdgcn_raw_buffer_load_b128(rs, vo, ((i) * 56 + (k)) * 1024, aux))
  bf16x8 wd[7][4];
#pragma unroll
  for (int k = 0; k < 7; ++k)
#pragma unroll
    for (int i = 0; i < 4; ++i) wd[k][i] = G2_LD(wr, i, k, 0);
  __builtin_amdgcn_sched_barrier(0);

  {
    char* cbase = smem + ch * CV_CH_STRIDE;
    u16* ucp = (u16*)(cbase + CV_UCP);
    u16* kr = (u16*)(cbase + CV_KR);
    u32x4* uraw = (u32x4*)(cbase + CV_URAW);
    if (t2 < 4) { uraw[t2] = u32x4{0, 0, 0, 0}; uraw[260 + t2] = u32x4{0, 0, 0, 0}; }
    {
      u32x4 h;
#pragma unroll
      for (int j = 0; j < 4; ++j) h[j] = bf2_to_h2(uv0[j]);
      uraw[4 + t2] = h;
    }
    float kscale, kinv;
    {
      const float mx = fmaxf(fmaxf(kmax[0], kmax[1]), fmaxf(kmax[2], kmax[3]));
      int e = (int)((__float_as_uint(mx) >> 23) & 0xff);
      e = min(max(e, 20), 240);
      kscale = __uint_as_float((u32)(127 + 13 + 127 - e) << 23);
      kinv = __uint_as_float((u32)(e - 13) << 23);
    }
    {
      float kk[8];
#pragma unroll
      for (int j = 0; j < 8; ++j) {
        const int jj = 2047 - (t2 * 8 + j);
        const int cbi = (int)f2bf((float)jj) - 17024;
        kk[j] = kraw[jj < 128 ? jj : cbi] * kscale;
      }
      f16x8 o;
#pragma unroll
      for (int j = 0; j < 8; ++j) o[j] = (_Float16)kk[j];
      *(f16x8*)(kr + t2 * 8 + 8 * (t2 >> 2)) = o;
      if (t2 < 128) { const int tb = t2 + 256; *(u32x4*)(kr + tb * 8 + 8 * (tb >> 2)) = u32x4{0, 0, 0, 0}; }
    }
    RAW_BAR();
    for (int tb = t2; tb < UC_LEN / 8; tb += 256) {
      u32 w[8];
      u32x4 a = uraw[tb], b = uraw[tb + 1];
#pragma unroll
      for (int j = 0; j < 4; ++j) { w[j] = a[j]; w[4 + j] = b[j]; }
      u32x4 o0, o1, o2, o3;
#pragma unroll
      for (int j = 0; j < 4; ++j) {
        o0[j] = __builtin_amdgcn_alignbit(w[j + 1], w[j], 16);
        o1[j] = w[j + 1];
        o2[j] = __builtin_amdgcn_alignbit(w[j + 2], w[j + 1], 16);
        o3[j] = w[j + 2];
      }
      *(u32x4*)(ucp + 0 * UC_LEN + tb * 8) = o0;
      *(u32x4*)(ucp + 1 * UC_LEN + tb * 8) = o1;
      *(u32x4*)(ucp + 2 * UC_LEN + tb * 8) = o2;
      *(u32x4*)(ucp + 3 * UC_LEN + tb * 8) = o3;
    }
    RAW_BAR();
    DIAG_STAMP(5);
    const int r = lane & 31, h = lane >> 5;
    const u16* ap = ucp + (r & 3) * UC_LEN + 8 * h + 4 * (r >> 2);
    const int bofs0 = 2048 - 32 * (r + 1) + 8 * h + 8 * (63 - r);
    const int bofs1 = bofs0 - 1024 - 256;
    f32x16 acc0 = {}, acc1 = {};
    int ao2 = (int)((const char*)ap - smem) + 8;
    asm volatile("" : "+v"(ao2));
    auto lda = [&](int s) -> f16x8 {
      f16x4 lo = *(const f16x4*)(ap + 16 * s);
      f16x4 hi = *(const f16x4*)(smem + ao2 + 32 * s);
      return __builtin_shufflevector(lo, hi, 0, 1, 2, 3, 4, 5, 6, 7);
    };
    auto ldb = [&](int ofs, int s) -> f16x8 { return *(const f16x8*)(kr + ofs + 16 * s + 8 * (s >> 1)); };
    {
      const int sb = w4 * 16;
      f16x8 a0 = lda(sb), a1 = lda(sb + 1), c0 = lda(sb + 64), c1 = lda(sb + 65);
      f16x8 p0 = ldb(bofs0, sb), p1 = ldb(bofs0, sb + 1), q0 = ldb(bofs1, sb), q1 = ldb(bofs1, sb + 1);
#pragma unroll 2
      for (int it = 0; it < 8; ++it) {
        const int sn = sb + ((2 * it + 2) & 15);
        f16x8 na0 = lda(sn), na1 = lda(sn + 1), nc0 = lda(sn + 64), nc1 = lda(sn + 65);
        f16x8 np0 = ldb(bofs0, sn), np1 = ldb(bofs0, sn + 1), nq0 = ldb(bofs1, sn), nq1 = ldb(bofs1, sn + 1);
        __builtin_amdgcn_sched_barrier(0);
        acc0 = __builtin_amdgcn_mfma_f32_32x32x16_f16(a0, p0, acc0, 0, 0, 0);
        acc1 = __builtin_amdgcn_mfma_f32_32x32x16_f16(a0, q0, acc1, 0, 0, 0);
        acc0 = __builtin_amdgcn_mfma_f32_32x32x16_f16(a1, p1, acc0, 0, 0, 0);
        acc1 = __builtin_amdgcn_mfma_f32_32x32x16_f16(c0, p0, acc1, 0, 0, 0);
        acc1 = __builtin_amdgcn_mfma_f32_32x32x16_f16(a1, q1, acc1, 0, 0, 0);
        acc1 = __builtin_amdgcn_mfma_f32_32x32x16_f16(c1, p1, acc1, 0, 0, 0);
        __builtin_amdgcn_sched_barrier(0);
        a0 = na0; a1 = na1; c0 = nc0; c1 = nc1; p0 = np0; p1 = np1; q0 = nq0; q1 = nq1;
      }
    }
    DIAG_STAMP2(1);
    RAW_BAR();
    float* red = (float*)cbase;
#pragma unroll
    for (int g = 0; g < 4; ++g) {
      f32x4 v0 = {acc0[4 * g], acc0[4 * g + 1], acc0[4 * g + 2], acc0[4 * g + 3]};
      f32x4 v1 = {acc1[4 * g], acc1[4 * g + 1], acc1[4 * g + 2], acc1[4 * g + 3]};
      *(f32x4*)(red + (w4 * 32 + r) * 36 + 8 * g + 4 * h) = v0;
      *(f32x4*)(red + ((4 + w4) * 32 + r) * 36 + 8 * g + 4 * h) = v1;
    }
    if (tid == 511) wait_count(ws, 8 + (mt2 >> 1), 16u);
    RAW_BAR();
    u16* at = (u16*)(ws + WS_AT) + (size_t)d * L_;
    {
      const int c = t2;
      const int T = c >> 7, a = (c >> 2) & 31, b0 = (c & 3) * 8;
      f32x4 s0 = {}, s1 = {};
      const float* p = red + (T * 4 * 32 + a) * 36 + b0;
#pragma unroll
      for (int w = 0; w < 4; ++w) { s0 += *(const f32x4*)(p + w * 32 * 36); s1 += *(const f32x4*)(p + w * 32 * 36 + 4); }
      u32x4 o;
#pragma unroll
      for (int j = 0; j < 4; ++j) {
        const f2 y = unpack2((j < 2) ? pack2(s0[2 * j] * kinv, s0[2 * j + 1] * kinv) : pack2(s1[2 * j - 4] * kinv, s1[2 * j - 3] * kinv));
        const f2 us = unpack2(uv0[j]) * dsk;
        const f2 t = unpack2(pack2(us.x, us.y)) + y;
        o[j] = pack2(t.x, t.y);
      }
      st16_wt(at + c * 8, o);
    }
  }
  DIAG_STAMP2(2);

  {
    f32x4 acc[4][4] = {};
    bf16x8 hb[4][4];
#pragma unroll
    for (int k = 0; k < 4; ++k)
#pragma unroll
      for (int i = 0; i < 4; ++i) hb[k][i] = G2_LD(hr, i, k, 16);
    asm volatile("s_waitcnt vmcnt(16)" ::: "memory");
    RAW_BAR();
    if (tid == 0) __hip_atomic_fetch_add((gu32*)(ws + WS_CNT + (bid >> 5) * 256), 1u, __ATOMIC_RELAXED, __HIP_MEMORY_SCOPE_AGENT);
    __builtin_amdgcn_sched_barrier(0);
#pragma unroll
    for (int k = 0; k < 7; ++k) {
#pragma unroll
      for (int j = 0; j < 4; ++j)
#pragma unroll
        for (int i = 0; i < 4; ++i) acc[j][i] = __builtin_amdgcn_mfma_f32_16x16x32_bf16(wd[k][j], hb[k & 3][i], acc[j][i], 0, 0, 0);
      if (k < 3) {
#pragma unroll
        for (int i = 0; i < 4; ++i) hb[k][i] = G2_LD(hr, i, k + 4, 16);
      }
      __builtin_amdgcn_sched_barrier(0);
    }
    DIAG_STAMP2(3);
    float* red = (float*)smem;
    u16* atile = (u16*)(smem + G2_AT);
    float* rp = red + (wid * 64 + (lane & 15)) * 68 + 4 * (lane >> 4);
#pragma unroll
    for (int j = 0; j < 4; ++j)
#pragma unroll
      for (int i = 0; i < 4; ++i) *(f32x4*)(rp + i * 16 * 68 + j * 16) = acc[j][i];
    const int m = tid >> 3, n0 = (tid & 7) * 8;
    const size_t go = (size_t)(mt2 * 64 + m) * D_ + nt2 * 64 + n0;
    const u32x4 xv = *(const u32x4*)(x + go);
    if (tid == 511) wait_count(ws, nt2, 32u);
    __syncthreads();
    DIAG_STAMP2(4);
    {
      const u16* at = (const u16*)(ws + WS_AT) + (size_t)(nt2 * 64 + (tid >> 3)) * L_ + mt2 * 64 + (tid & 7) * 8;
      u32x4 av;
      asm volatile("global_load_dwordx4 %0, %1, off sc1\n\ts_waitcnt vmcnt(0)" : "=&v"(av) : "v"(at) : "memory");
      *(u32x4*)(atile + (tid >> 3) * 72 + (tid & 7) * 8) = av;
    }
    f32x4 p0 = {}, p1 = {};
#pragma unroll
    for (int w = 0; w < 8; ++w) {
      p0 += *(const f32x4*)(red + (w * 64 + m) * 68 + n0);
      p1 += *(const f32x4*)(red + (w * 64 + m) * 68 + n0 + 4);
    }
    __syncthreads();
    u32x4 o;
#pragma unroll
    for (int j = 0; j < 4; ++j) {
      const f2 xx = unpack2(xv[j]);
      const f2 aa = f2{bf2f(atile[(n0 + 2 * j) * 72 + m]), bf2f(atile[(n0 + 2 * j + 1) * 72 + m])};
      const f2 r1 = xx + aa;
      const f2 bb = unpack2((j < 2) ? pack2(p0[2 * j], p0[2 * j + 1]) : pack2(p1[2 * j - 4], p1[2 * j - 3]));
      const f2 r2 = unpack2(pack2(r1.x, r1.y)) + bb;
      o[j] = pack2(r2.x, r2.y);
    }
    *(u32x4*)(out + go) = o;
  }
}

extern "C" void kernel_launch(void* const* d_in, const int* in_sizes, int n_in, void* d_out, int out_size,
                              void* d_ws, size_t ws_size, hipStream_t stream) {
  const u16* x = (const u16*)d_in[0];
  const u16* logA = (const u16*)d_in[1];
  const u16* Aim = (const u16*)d_in[2];
  const u16* Cre = (const u16*)d_in[3];
  const u16* Cim = (const u16*)d_in[4];
  const u16* logdt = (const u16*)d_in[5];
  const u16* dskip = (const u16*)d_in[6];
  const u16* wg = (const u16*)d_in[8];
  const u16* wu = (const u16*)d_in[9];
  const u16* wd = (const u16*)d_in[10];
  char* ws = (char*)d_ws;
  (void)hipFuncSetAttribute((const void*)k2_kernel, hipFuncAttributeMaxDynamicSharedMemorySize, K2_LDS);
  k1_kernel<<<K1_LN_BLOCKS + K1_WT_BLOCKS, 256, 0, stream>>>(x, wg, wu, wd, ws);
  k2_kernel<<<256, 512, K2_LDS, stream>>>(x, logA, Aim, Cre, Cim, logdt, dskip, ws, (u16*)d_out);
}
```

```cpp
#include <hip/hip_runtime.h>
#include <hip/hip_bf16.h>

typedef unsigned short u16;
typedef unsigned int u32;
using bf16x8 = __attribute__((ext_vector_type(8))) short;
using f16x8 = __attribute__((ext_vector_type(8))) _Float16;
using f16x4 = __attribute__((ext_vector_type(4))) _Float16;
using f32x4 = __attribute__((ext_vector_type(4))) float;
using f32x16 = __attribute__((ext_vector_type(16))) float;
using u32x4 = __attribute__((ext_vector_type(4))) unsigned int;
using u32x2 = __attribute__((ext_vector_type(2))) unsigned int;
typedef float f2 __attribute__((ext_vector_type(2)));
typedef __attribute__((address_space(1))) unsigned gu32;

constexpr int L_ = 2048, D_ = 512, N_ = 128, F_ = 1792;
constexpr int KC_STRIDE = 648;

constexpr size_t WS_CNT = 0;
constexpr size_t WS_XNT = 4096;
constexpr size_t WS_XNTR = WS_XNT + (size_t)L_ * D_ * 2;
constexpr size_t WS_KC = WS_XNTR + (size_t)L_ * D_ * 2;
constexpr size_t WS_H = WS_KC + (size_t)D_ * KC_STRIDE * 4;
constexpr size_t WS_WG = WS_H + (size_t)L_ * F_ * 2;
constexpr size_t WS_WU = WS_WG + (size_t)D_ * F_ * 2;
constexpr size_t WS_WD = WS_WU + (size_t)D_ * F_ * 2;
constexpr size_t WS_AT = WS_WD + (size_t)D_ * F_ * 2;

#define DIAG_START(k)
#define DIAG_STAMP(i)
#define DIAG_END(wsp)

__device__ __forceinline__ float bf2f(u16 v) { return __uint_as_float(((u32)v) << 16); }
__device__ __forceinline__ u16 f2bf(float f) {
  __bf16 b = (__bf16)f;
  return __builtin_bit_cast(u16, b);
}
__device__ __forceinline__ float rbf(float f) { return bf2f(f2bf(f)); }
__device__ __forceinline__ u32 pack2(float lo, float hi) {
  typedef __bf16 b2 __attribute__((ext_vector_type(2)));
  b2 r = __builtin_convertvector(f2{lo, hi}, b2);
  return __builtin_bit_cast(u32, r);
}
__device__ __forceinline__ f2 unpack2(u32 w) { return f2{__uint_as_float(w << 16), __uint_as_float(w & 0xffff0000u)}; }

__device__ __forceinline__ void st16_wt(void* p, u32x4 v) { asm volatile("global_store_dwordx4 %0, %1, off sc1\n\ts_nop 1" ::"v"(p), "v"(v) : "memory"); }
__device__ __forceinline__ void st16(void* p, u32x4 v) { *(u32x4*)p = v; }
__device__ __forceinline__ void st8(void* p, u32x2 v) { *(u32x2*)p = v; }

#define DPP_F(v, ctrl) __builtin_bit_cast(float, __builtin_amdgcn_update_dpp(0, __builtin_bit_cast(int, v), ctrl, 0xf, 0xf, true))
__device__ __forceinline__ float wave_sum(float v) {
  v += DPP_F(v, 0xB1);
  v += DPP_F(v, 0x4E);
  v += DPP_F(v, 0x141);
  v += DPP_F(v, 0x140);
  const int iv = __builtin_bit_cast(int, v);
  return (__builtin_bit_cast(float, __builtin_amdgcn_readlane(iv, 0)) + __builtin_bit_cast(float, __builtin_amdgcn_readlane(iv, 16))) +
         (__builtin_bit_cast(float, __builtin_amdgcn_readlane(iv, 32)) + __builtin_bit_cast(float, __builtin_amdgcn_readlane(iv, 48)));
}
__device__ __forceinline__ float wave_max(float v) {
  v = fmaxf(v, DPP_F(v, 0xB1));
  v = fmaxf(v, DPP_F(v, 0x4E));
  v = fmaxf(v, DPP_F(v, 0x141));
  v = fmaxf(v, DPP_F(v, 0x140));
  const int iv = __builtin_bit_cast(int, v);
  return fmaxf(fmaxf(__builtin_bit_cast(float, __builtin_amdgcn_readlane(iv, 0)), __builtin_bit_cast(float, __builtin_amdgcn_readlane(iv, 16))),
               fmaxf(__builtin_bit_cast(float, __builtin_amdgcn_readlane(iv, 32)), __builtin_bit_cast(float, __builtin_amdgcn_readlane(iv, 48))));
}

constexpr int K1_LN_BLOCKS = 128;
constexpr int K1_WT_BLOCKS = 336;
constexpr int K1_KG_BLOCKS = 512;

__device__ __forceinline__ void cmul6(f2& r0, f2& r1, f2& r2, f2& r3, f2& r4, f2& r5, f2 a0, f2 b0, f2 a1, f2 b1,
                                      f2 a2, f2 b2, f2 a3, f2 b3, f2 a4, f2 b4, f2 a5, f2 b5) {
  asm("v_pk_mul_f32 %0, %6, %7 op_sel:[1,1] op_sel_hi:[1,0] neg_lo:[0,1]\n\t"
      "v_pk_mul_f32 %1, %8, %9 op_sel:[1,1] op_sel_hi:[1,0] neg_lo:[0,1]\n\t"
      "v_pk_mul_f32 %2, %10, %11 op_sel:[1,1] op_sel_hi:[1,0] neg_lo:[0,1]\n\t"
      "v_pk_mul_f32 %3, %12, %13 op_sel:[1,1] op_sel_hi:[1,0] neg_lo:[0,1]\n\t"
      "v_pk_mul_f32 %4, %14, %15 op_sel:[1,1] op_sel_hi:[1,0] neg_lo:[0,1]\n\t"
      "v_pk_mul_f32 %5, %16, %17 op_sel:[1,1] op_sel_hi:[1,0] neg_lo:[0,1]\n\t"
      "v_pk_fma_f32 %0, %6, %7, %0 op_sel:[0,0,0] op_sel_hi:[0,1,1]\n\t"
      "v_pk_fma_f32 %1, %8, %9, %1 op_sel:[0,0,0] op_sel_hi:[0,1,1]\n\t"
      "v_pk_fma_f32 %2, %10, %11, %2 op_sel:[0,0,0] op_sel_hi:[0,1,1]\n\t"
      "v_pk_fma_f32 %3, %12, %13, %3 op_sel:[0,0,0] op_sel_hi:[0,1,1]\n\t"
      "v_pk_fma_f32 %4, %14, %15, %4 op_sel:[0,0,0] op_sel_hi:[0,1,1]\n\t"
      "v_pk_fma_f32 %5, %16, %17, %5 op_sel:[0,0,0] op_sel_hi:[0,1,1]\n\t"
      "s_nop 1"
      : "=&v"(r0), "=&v"(r1), "=&v"(r2), "=&v"(r3), "=&v"(r4), "=&v"(r5)
      : "v"(a0), "v"(b0), "v"(a1), "v"(b1), "v"(a2), "v"(b2), "v"(a3), "v"(b3), "v"(a4), "v"(b4), "v"(a5), "v"(b5));
}
__device__ __forceinline__ void cmul2(f2& r0, f2& r1, f2 a0, f2 b0, f2 a1, f2 b1) {
  asm("v_pk_mul_f32 %0, %2, %3 op_sel:[1,1] op_sel_hi:[1,0] neg_lo:[0,1]\n\t"
      "v_pk_mul_f32 %1, %4, %5 op_sel:[1,1] op_sel_hi:[1,0] neg_lo:[0,1]\n\t"
      "s_nop 0\n\t"
      "v_pk_fma_f32 %0, %2, %3, %0 op_sel:[0,0,0] op_sel_hi:[0,1,1]\n\t"
      "v_pk_fma_f32 %1, %4, %5, %1 op_sel:[0,0,0] op_sel_hi:[0,1,1]\n\t"
      "s_nop 1"
      : "=&v"(r0), "=&v"(r1)
      : "v"(a0), "v"(b0), "v"(a1), "v"(b1));
}
__device__ __forceinline__ f2 cexp_(float zr, float zi, float t) {
  const float m = __builtin_amdgcn_exp2f((zr * t) * 1.44269504088896341f);
  float r = (zi * t) * 0.15915494309189535f;
  r -= rintf(r);
  return f2{m * __builtin_amdgcn_cosf(r), m * __builtin_amdgcn_sinf(r)};
}

__global__ __launch_bounds__(256) void k1_kernel(const u16* __restrict__ x, const u16* __restrict__ logA,
                                                 const u16* __restrict__ Aim, const u16* __restrict__ Cre,
                                                 const u16* __restrict__ Cim, const u16* __restrict__ logdt,
                                                 const u16* __restrict__ wg, const u16* __restrict__ wu,
                                                 const u16* __restrict__ wd, char* __restrict__ ws) {
  __shared__ __attribute__((aligned(16))) char smem[18432];
  const int tid = threadIdx.x, lane = tid & 63, wid = tid >> 6;
  DIAG_START(0)
  int bid = blockIdx.x - K1_KG_BLOCKS;
  if (bid >= 0 && bid < K1_LN_BLOCKS) {
    __builtin_amdgcn_s_setprio(3);
    if (bid == 0 && tid < 8) *(u32*)(ws + WS_CNT + tid * 256) = 0u;
    u16* tile = (u16*)smem;
    u16* xnt = (u16*)(ws + WS_XNT);
    const int r0 = bid * 16;
    u32x4 v[4];
#pragma unroll
    for (int i = 0; i < 4; ++i) v[i] = *(const u32x4*)(x + (size_t)(r0 + wid * 4 + i) * D_ + lane * 8);
#pragma unroll
    for (int i = 0; i < 4; ++i) {
      const int rl = wid * 4 + i, row = r0 + rl;
      f2 f[4];
#pragma unroll
      for (int j = 0; j < 4; ++j) f[j] = unpack2(v[i][j]);
      const f2 s2 = (f[0] + f[1]) + (f[2] + f[3]);
      const float mean = wave_sum(s2.x + s2.y) * (1.f / D_);
      f2 q2 = {0.f, 0.f};
#pragma unroll
      for (int j = 0; j < 4; ++j) { const f2 t = f[j] - mean; q2 = __builtin_elementwise_fma(t, t, q2); }
      const float q = wave_sum(q2.x + q2.y);
      const float mb = rbf(mean);
      const float vb = rbf(q * (1.f / D_));
      const float sd = rbf(sqrtf(rbf(vb + 1e-5f)));
      const float rs = __builtin_amdgcn_rcpf(sd);
      u32x4 o;
#pragma unroll
      for (int j = 0; j < 4; ++j) {
        const f2 t = f[j] - mb;
        const f2 a = unpack2(pack2(t.x, t.y));
        f2 qq = a * rs;
        const f2 rem = __builtin_elementwise_fma(-qq, f2{sd, sd}, a);
        qq = __builtin_elementwise_fma(rem, f2{rs, rs}, qq);
        o[j] = pack2(qq.x, qq.y);
      }
      st16(xnt + ((size_t)((row >> 4) * 16 + (lane >> 2)) * 64 + (lane & 3) * 16 + (row & 15)) * 8, o);
      *(u32x4*)(tile + rl * 520 + lane * 8) = o;
    }
    DIAG_STAMP(1);
    __syncthreads();
    u16* xtr = (u16*)(ws + WS_XNTR);
#pragma unroll
    for (int h = 0; h < 2; ++h) {
      const int d = tid + h * 256;
#pragma unroll
      for (int g = 0; g < 2; ++g) {
        u32x4 o;
#pragma unroll
        for (int j = 0; j < 4; ++j)
          o[j] = (u32)tile[(g * 8 + 2 * j) * 520 + d] | ((u32)tile[(g * 8 + 2 * j + 1) * 520 + d] << 16);
        st16(xtr + (size_t)d * L_ + r0 + g * 8, o);
      }
    }
    DIAG_END(ws);
    return;
  }
  bid -= K1_LN_BLOCKS;
  if (bid >= 0) {
    __builtin_amdgcn_s_setprio(2);
    int task = bid * 4 + wid;
    const u16* W; u16* Wt; int Kd, Nd;
    if (task < 448) { W = wg; Wt = (u16*)(ws + WS_WG); Kd = D_; Nd = F_; }
    else if (task < 896) { task -= 448; W = wu; Wt = (u16*)(ws + WS_WU); Kd = D_; Nd = F_; }
    else { task -= 896; W = wd; Wt = (u16*)(ws + WS_WD); Kd = F_; Nd = D_; }
    const int nks = Kd / 32;
    const int ks = task % nks, nb = task / nks;
    const int col = nb * 64 + lane;
    u16 v[32];
#pragma unroll
    for (int k = 0; k < 32; ++k) v[k] = W[(size_t)(ks * 32 + k) * Nd + col];
#pragma unroll
    for (int q = 0; q < 4; ++q) {
      u32x4 o;
#pragma unroll
      for (int j = 0; j < 4; ++j) o[j] = (u32)v[q * 8 + 2 * j] | ((u32)v[q * 8 + 2 * j + 1] << 16);
      st16(Wt + ((size_t)((col >> 4) * nks + ks) * 64 + q * 16 + (col & 15)) * 8, o);
    }
    DIAG_END(ws);
    return;
  }
  bid = blockIdx.x;
  {
    const int d = bid;
    float4* prm = (float4*)(smem + 16384);
    if (tid < 128) {
      const u16 la = logA[d * N_ + tid], ai = Aim[d * N_ + tid], cr = Cre[d * N_ + tid], ci = Cim[d * N_ + tid];
      const float dt = rbf(expf(bf2f(logdt[d])));
      const float ar = -rbf(expf(bf2f(la)));
      prm[tid] = make_float4(ar * dt, bf2f(ai) * dt, bf2f(cr) * dt, bf2f(ci) * dt);
    }
    __syncthreads();
    DIAG_STAMP(1);
    const int kq = lane >> 4, idx = lane & 15;
    f32x4 acc[4] = {};
    const float ty = (float)idx, te = (float)(idx == 0 ? 256 : 16 * idx);
    float4 pr[8];
#pragma unroll
    for (int s = 0; s < 8; ++s) pr[s] = prm[wid * 32 + s * 4 + kq];
#pragma unroll
    for (int s = 0; s < 8; s += 2) {
      const float4 pa = pr[s], pb = pr[s + 1];
      const f2 Ca = f2{pa.z, -pa.w}, Cb = f2{pb.z, -pb.w};
      f2 Ya[4], Yb[4], Xa[4], Xb[4];
      Ya[0] = cexp_(pa.x, pa.y, ty); Yb[0] = cexp_(pb.x, pb.y, ty);
      f2 Ea = cexp_(pa.x, -pa.y, te), Eb = cexp_(pb.x, -pb.y, te);
      f2 Fa = (idx == 0) ? f2{1.f, 0.f} : Ea, Fb = (idx == 0) ? f2{1.f, 0.f} : Eb;
#pragma unroll
      for (int e = 0; e < 3; ++e) {
        f2 nEa, nEb;
        cmul6(Xa[e], Xb[e], Ya[e + 1], Yb[e + 1], nEa, nEb, Ca, Fa, Cb, Fb, Ya[e], Ya[e], Yb[e], Yb[e], Ea, Ea, Eb, Eb);
        Ea = nEa; Eb = nEb; Fa = nEa; Fb = nEb;
      }
      cmul2(Xa[3], Xb[3], Ca, Ea, Cb, Eb);
#pragma unroll
      for (int e = 0; e < 4; ++e) acc[e] = __builtin_amdgcn_mfma_f32_16x16x4f32(Xa[e].x, Ya[e].x, acc[e], 0, 0, 0);
#pragma unroll
      for (int e = 0; e < 4; ++e) acc[e] = __builtin_amdgcn_mfma_f32_16x16x4f32(Xa[e].y, Ya[e].y, acc[e], 0, 0, 0);
#pragma unroll
      for (int e = 0; e < 4; ++e) acc[e] = __builtin_amdgcn_mfma_f32_16x16x4f32(Xb[e].x, Yb[e].x, acc[e], 0, 0, 0);
#pragma unroll
      for (int e = 0; e < 4; ++e) acc[e] = __builtin_amdgcn_mfma_f32_16x16x4f32(Xb[e].y, Yb[e].y, acc[e], 0, 0, 0);
    }
    DIAG_STAMP(2);
    float* red = (float*)smem;
#pragma unroll
    for (int e = 0; e < 4; ++e)
#pragma unroll
      for (int r = 0; r < 4; ++r) red[(wid * 4 + e) * 256 + ((lane >> 4) * 4 + r) * 16 + (lane & 15)] = acc[e][r];
    __syncthreads();
    float* kc = (float*)(ws + WS_KC) + (size_t)d * KC_STRIDE;
#pragma unroll
    for (int e = 0; e < 4; ++e) {
      const float v = 2.f * ((red[(0 * 4 + e) * 256 + tid] + red[(1 * 4 + e) * 256 + tid]) +
                             (red[(2 * 4 + e) * 256 + tid] + red[(3 * 4 + e) * 256 + tid]));
      if (e == 0) kc[tid] = v;
      else if (tid >= 128) kc[128 * e + tid] = v;
      else if (e == 3 && tid == 0) kc[640] = v;
    }
    DIAG_END(ws);
  }
}

constexpr int G1_STAGE = 22 * 1024;
constexpr int G1_NBUF = 7;
constexpr int K2_LDS = G1_STAGE * G1_NBUF;

__device__ __forceinline__ f2 gelu2(f2 g) {
  const float c0 = -2.f * 0.7978845608028654f * 1.44269504088896341f;
  const float c1 = c0 * 0.044715f;
  const f2 t = g * __builtin_elementwise_fma(g * g, f2{c1, c1}, f2{c0, c0});
  const f2 dn = f2{__builtin_amdgcn_exp2f(t.x), __builtin_amdgcn_exp2f(t.y)} + 1.f;
  return g * f2{__builtin_amdgcn_rcpf(dn.x), __builtin_amdgcn_rcpf(dn.y)};
}

__device__ __forceinline__ void glds16(const void* g, void* l) {
  __builtin_amdgcn_global_load_lds((const __attribute__((address_space(1))) u32*)g,
                                   (__attribute__((address_space(3))) u32*)l, 16, 0, 0);
}

__global__ __launch_bounds__(512, 2) void k2_kernel(char* __restrict__ ws) {
  extern __shared__ __attribute__((aligned(16))) char smem[];
  const int tid = threadIdx.x, lane = tid & 63;
  const int wid = __builtin_amdgcn_readfirstlane(tid >> 6);
  DIAG_START(1)
  const int bid = blockIdx.x;
  const int xcd = bid & 7, loc = bid >> 3;
  const int mt = (xcd >> 1) * 4 + (loc & 3);
  const int nt = (xcd & 1) * 8 + (loc >> 2);
  const u16* xnt = (const u16*)(ws + WS_XNT);
  const u16* wgt = (const u16*)(ws + WS_WG);
  const u16* wut = (const u16*)(ws + WS_WU);
  auto src = [&](int f) -> const u16* {
    if (f < 8) return xnt + (size_t)(mt * 8 + f) * 16 * 512;
    const int s = f - 8;
    return ((s & 1) ? wut : wgt) + (size_t)(nt * 7 + (s >> 1)) * 16 * 512;
  };
  const u16* s0 = src(wid) + lane * 8;
  const u16* s1 = src(wid + 8) + lane * 8;
  const u16* s2 = src(wid < 6 ? wid + 16 : 21) + lane * 8;
  auto stage = [&](int ks) {
    char* base = smem + (ks % G1_NBUF) * G1_STAGE + wid * 1024 + lane * 16;
    glds16(s0 + ks * 512, base);
    glds16(s1 + ks * 512, base + 8192);
    if (wid < 6) glds16(s2 + ks * 512, base + 16384);
  };
  const int wm = wid >> 1, wn = wid & 1;
  f32x4 acc[7][2] = {};
  bf16x8 xa[2], wa[7], xb[2], wb[7];
  auto rd = [&](int ks, bf16x8(&xf)[2], bf16x8(&wf)[7]) {
    const char* base = smem + (ks % G1_NBUF) * G1_STAGE + lane * 16;
#pragma unroll
    for (int i = 0; i < 2; ++i) xf[i] = *(const bf16x8*)(base + (wm * 2 + i) * 1024);
#pragma unroll
    for (int j = 0; j < 7; ++j) wf[j] = *(const bf16x8*)(base + (8 + wn * 7 + j) * 1024);
  };
  auto mm = [&](bf16x8(&xf)[2], bf16x8(&wf)[7]) {
#pragma unroll
    for (int j = 0; j < 7; ++j)
#pragma unroll
      for (int i = 0; i < 2; ++i) acc[j][i] = __builtin_amdgcn_mfma_f32_16x16x32_bf16(wf[j], xf[i], acc[j][i], 0, 0, 0);
  };
#define G1_WAIT(n) do { if (wid < 6) asm volatile("s_waitcnt vmcnt(%0)" ::"n"(3 * (n)) : "memory"); \
                        else asm volatile("s_waitcnt vmcnt(%0)" ::"n"(2 * (n)) : "memory"); } while (0)
#define G1_BAR() do { asm volatile("s_waitcnt lgkmcnt(0)" ::: "memory"); __builtin_amdgcn_s_barrier(); } while (0)
#pragma unroll
  for (int k = 0; k < 7; ++k) stage(k);
  G1_WAIT(6);
  G1_BAR();
  DIAG_STAMP(1);
  rd(0, xa, wa);
#define G1_ITER(k, XC, WC, XN, WN, NLEFT)                                                          \
  do {                                                                                             \
    G1_WAIT(NLEFT);                                                                                \
    G1_BAR();                                                                                      \
    const char* rb_ = smem + (((k) + 1) % G1_NBUF) * G1_STAGE + lane * 16;                         \
    _Pragma("unroll") for (int j = 0; j < 7; ++j) {                                                \
      acc[j][0] = __builtin_amdgcn_mfma_f32_16x16x32_bf16(WC[j], XC[0], acc[j][0], 0, 0, 0);       \
      if (j < 2) XN[j] = *(const bf16x8*)(rb_ + (wm * 2 + j) * 1024);                              \
      acc[j][1] = __builtin_amdgcn_mfma_f32_16x16x32_bf16(WC[j], XC[1], acc[j][1], 0, 0, 0);       \
      WN[j] = *(const bf16x8*)(rb_ + (8 + wn * 7 + j) * 1024);                                     \
      __builtin_amdgcn_sched_barrier(0);                                                           \
      if (j == 3 && (k) + 7 < 16) { stage((k) + 7); __builtin_amdgcn_sched_barrier(0); }           \
    }                                                                                              \
  } while (0)
  G1_ITER(0, xa, wa, xb, wb, 5);
  G1_ITER(1, xb, wb, xa, wa, 5);
  G1_ITER(2, xa, wa, xb, wb, 5);
  G1_ITER(3, xb, wb, xa, wa, 5);
  G1_ITER(4, xa, wa, xb, wb, 5);
  G1_ITER(5, xb, wb, xa, wa, 5);
  G1_ITER(6, xa, wa, xb, wb, 5);
  G1_ITER(7, xb, wb, xa, wa, 5);
  DIAG_STAMP(2);
  G1_ITER(8, xa, wa, xb, wb, 5);
  G1_ITER(9, xb, wb, xa, wa, 5);
  G1_ITER(10, xa, wa, xb, wb, 4);
  G1_ITER(11, xb, wb, xa, wa, 3);
  G1_ITER(12, xa, wa, xb, wb, 2);
  G1_ITER(13, xb, wb, xa, wa, 1);
  G1_ITER(14, xa, wa, xb, wb, 0);
  mm(xb, wb);
  DIAG_STAMP(3);
  __syncthreads();
  f32x4* ex = (f32x4*)smem;
  if (wn == 0) {
#pragma unroll
    for (int i = 0; i < 2; ++i) ex[(wm * 2 + i) * 64 + lane] = acc[6][i];
  }
  __syncthreads();
  u16* hT = (u16*)(ws + WS_H);
  auto emit = [&](int t, const f32x4& g, const f32x4& u, int i) {
    u32x2 o;
#pragma unroll
    for (int p = 0; p < 2; ++p) {
      const f2 gb = unpack2(pack2(g[2 * p], g[2 * p + 1]));
      const f2 ub = unpack2(pack2(u[2 * p], u[2 * p + 1]));
      const f2 ge = gelu2(gb);
      const f2 hv = unpack2(pack2(ge.x, ge.y)) * ub;
      o[p] = pack2(hv.x, hv.y);
    }
    const int T = nt * 7 + t;
    const int ks2 = T >> 1;
    const int q = 2 * (T & 1) + (lane >> 5);
    const int m16 = mt * 8 + wm * 2 + i;
    st8(hT + ((size_t)(m16 * 56 + ks2) * 64 + q * 16 + (lane & 15)) * 8 + ((lane >> 4) & 1) * 4, o);
  };
  if (wn == 0) {
#pragma unroll
    for (int t = 0; t < 3; ++t)
#pragma unroll
      for (int i = 0; i < 2; ++i) emit(t, acc[2 * t][i], acc[2 * t + 1][i], i);
  } else {
#pragma unroll
    for (int i = 0; i < 2; ++i) emit(3, ex[(wm * 2 + i) * 64 + lane], acc[0][i], i);
#pragma unroll
    for (int t = 4; t < 7; ++t)
#pragma unroll
      for (int i = 0; i < 2; ++i) emit(t, acc[2 * t - 7][i], acc[2 * t - 6][i], i);
  }
  DIAG_END(ws);
}

constexpr int UC_LEN = 2080;
constexpr int KR_LEN = 3840;
constexpr int CV_UCP = 0;
constexpr int CV_KR = CV_UCP + 4 * UC_LEN * 2;
constexpr int CV_URAW = CV_KR + KR_LEN * 2;
constexpr int CV_KRAW = CV_URAW + 4224;
constexpr int CV_MAX = CV_KRAW + 2592;
constexpr int CV_CH_STRIDE = 36864;
constexpr int G2_RED = 4 * 64 * 68 * 4;
constexpr int K3_LDS = G2_RED + 64 * 72 * 2;

__device__ __forceinline__ u32 bf2_to_h2(u32 w) {
  auto r = __builtin_amdgcn_cvt_pkrtz(__uint_as_float(w << 16), __uint_as_float(w & 0xffff0000u));
  return __builtin_bit_cast(u32, r);
}

__global__ __launch_bounds__(512, 4) void k3_kernel(const u16* __restrict__ x, const u16* __restrict__ dskip,
                                                    char* __restrict__ ws, u16* __restrict__ out) {
  extern __shared__ __attribute__((aligned(16))) char smem[];
  const int tid = threadIdx.x, lane = tid & 63;
  const int wid = __builtin_amdgcn_readfirstlane(tid >> 6);
  DIAG_START(2)
  const int bid = blockIdx.x;
  if (bid < 256) {
    const int ch = wid >> 2, w4 = wid & 3;
    const int d = bid * 2 + ch;
    const int t2 = tid & 255;
    char* cbase = smem + ch * CV_CH_STRIDE;
    u16* ucp = (u16*)(cbase + CV_UCP);
    u16* kr = (u16*)(cbase + CV_KR);
    u32x4* uraw = (u32x4*)(cbase + CV_URAW);
    float* kraw = (float*)(cbase + CV_KRAW);
    float* kmax = (float*)(cbase + CV_MAX);
    const u16* ug = (const u16*)(ws + WS_XNTR) + (size_t)d * L_;
    const float* kc = (const float*)(ws + WS_KC) + (size_t)d * KC_STRIDE;
    const u32x4 uv0 = *(const u32x4*)(ug + t2 * 8);
    const float kv0 = kc[t2], kv1 = kc[t2 + 256], kv2 = (t2 < 129) ? kc[t2 + 512] : 0.f;
    const float dsk = bf2f(dskip[d]);
    if (t2 < 4) { uraw[t2] = u32x4{0, 0, 0, 0}; uraw[260 + t2] = u32x4{0, 0, 0, 0}; }
    {
      u32x4 h;
#pragma unroll
      for (int j = 0; j < 4; ++j) h[j] = bf2_to_h2(uv0[j]);
      uraw[4 + t2] = h;
    }
    kraw[t2] = kv0;
    kraw[t2 + 256] = kv1;
    if (t2 < 136) kraw[t2 + 512] = kv2;
    {
      const float mx = wave_max(fmaxf(fmaxf(fabsf(kv0), fabsf(kv1)), fabsf(kv2)));
      if (lane == 0) kmax[w4] = mx;
    }
    __syncthreads();
    DIAG_STAMP(1);
    float kscale, kinv;
    {
      const float mx = fmaxf(fmaxf(kmax[0], kmax[1]), fmaxf(kmax[2], kmax[3]));
      int e = (int)((__float_as_uint(mx) >> 23) & 0xff);
      e = min(max(e, 20), 240);
      kscale = __uint_as_float((u32)(127 + 13 + 127 - e) << 23);
      kinv = __uint_as_float((u32)(e - 13) << 23);
    }
    for (int tb = t2; tb < UC_LEN / 8; tb += 256) {
      u32 w[8];
      u32x4 a = uraw[tb], b = uraw[tb + 1];
#pragma unroll
      for (int j = 0; j < 4; ++j) { w[j] = a[j]; w[4 + j] = b[j]; }
      u32x4 o0, o1, o2, o3;
#pragma unroll
      for (int j = 0; j < 4; ++j) {
        o0[j] = __builtin_amdgcn_alignbit(w[j + 1], w[j], 16);
        o1[j] = w[j + 1];
        o2[j] = __builtin_amdgcn_alignbit(w[j + 2], w[j + 1], 16);
        o3[j] = w[j + 2];
      }
      *(u32x4*)(ucp + 0 * UC_LEN + tb * 8) = o0;
      *(u32x4*)(ucp + 1 * UC_LEN + tb * 8) = o1;
      *(u32x4*)(ucp + 2 * UC_LEN + tb * 8) = o2;
      *(u32x4*)(ucp + 3 * UC_LEN + tb * 8) = o3;
    }
    {
      float kk[8];
#pragma unroll
      for (int j = 0; j < 8; ++j) {
        const int jj = 2047 - (t2 * 8 + j);
        const int cbi = (int)f2bf((float)jj) - 17024;
        kk[j] = kraw[jj < 128 ? jj : cbi] * kscale;
      }
      f16x8 o;
#pragma unroll
      for (int j = 0; j < 8; ++j) o[j] = (_Float16)kk[j];
      *(f16x8*)(kr + t2 * 8 + 8 * (t2 >> 2)) = o;
      if (t2 < 128) { const int tb = t2 + 256; *(u32x4*)(kr + tb * 8 + 8 * (tb >> 2)) = u32x4{0, 0, 0, 0}; }
    }
    __syncthreads();
    DIAG_STAMP(2);
    const int r = lane & 31, h = lane >> 5;
    const u16* ap = ucp + (r & 3) * UC_LEN + 8 * h + 4 * (r >> 2);
    const int bofs0 = 2048 - 32 * (r + 1) + 8 * h + 8 * (63 - r);
    const int bofs1 = bofs0 - 1024 - 256;
    f32x16 acc0 = {}, acc1 = {};
    int ao2 = (int)((const char*)ap - smem) + 8;
    asm volatile("" : "+v"(ao2));
    auto lda = [&](int s) -> f16x8 {
      f16x4 lo = *(const f16x4*)(ap + 16 * s);
      f16x4 hi = *(const f16x4*)(smem + ao2 + 32 * s);
      return __builtin_shufflevector(lo, hi, 0, 1, 2, 3, 4, 5, 6, 7);
    };
    auto ldb = [&](int ofs, int s) -> f16x8 { return *(const f16x8*)(kr + ofs + 16 * s + 8 * (s >> 1)); };
    {
      const int sb = w4 * 16;
      f16x8 a0 = lda(sb), a1 = lda(sb + 1), c0 = lda(sb + 64), c1 = lda(sb + 65);
      f16x8 p0 = ldb(bofs0, sb), p1 = ldb(bofs0, sb + 1), q0 = ldb(bofs1, sb), q1 = ldb(bofs1, sb + 1);
#pragma unroll 2
      for (int it = 0; it < 8; ++it) {
        const int sn = sb + ((2 * it + 2) & 15);
        f16x8 na0 = lda(sn), na1 = lda(sn + 1), nc0 = lda(sn + 64), nc1 = lda(sn + 65);
        f16x8 np0 = ldb(bofs0, sn), np1 = ldb(bofs0, sn + 1), nq0 = ldb(bofs1, sn), nq1 = ldb(bofs1, sn + 1);
        __builtin_amdgcn_sched_barrier(0);
        acc0 = __builtin_amdgcn_mfma_f32_32x32x16_f16(a0, p0, acc0, 0, 0, 0);
        acc1 = __builtin_amdgcn_mfma_f32_32x32x16_f16(a0, q0, acc1, 0, 0, 0);
        acc0 = __builtin_amdgcn_mfma_f32_32x32x16_f16(a1, p1, acc0, 0, 0, 0);
        acc1 = __builtin_amdgcn_mfma_f32_32x32x16_f16(c0, p0, acc1, 0, 0, 0);
        acc1 = __builtin_amdgcn_mfma_f32_32x32x16_f16(a1, q1, acc1, 0, 0, 0);
        acc1 = __builtin_amdgcn_mfma_f32_32x32x16_f16(c1, p1, acc1, 0, 0, 0);
        __builtin_amdgcn_sched_barrier(0);
        a0 = na0; a1 = na1; c0 = nc0; c1 = nc1; p0 = np0; p1 = np1; q0 = nq0; q1 = nq1;
      }
    }
    DIAG_STAMP(3);
    __syncthreads();
    DIAG_STAMP(4);
    float* red = (float*)cbase;
#pragma unroll
    for (int g = 0; g < 4; ++g) {
      f32x4 v0 = {acc0[4 * g], acc0[4 * g + 1], acc0[4 * g + 2], acc0[4 * g + 3]};
      f32x4 v1 = {acc1[4 * g], acc1[4 * g + 1], acc1[4 * g + 2], acc1[4 * g + 3]};
      *(f32x4*)(red + (w4 * 32 + r) * 36 + 8 * g + 4 * h) = v0;
      *(f32x4*)(red + ((4 + w4) * 32 + r) * 36 + 8 * g + 4 * h) = v1;
    }
    __syncthreads();
    u16* at = (u16*)(ws + WS_AT) + (size_t)d * L_;
    {
      const int c = t2;
      const int T = c >> 7, a = (c >> 2) & 31, b0 = (c & 3) * 8;
      f32x4 s0 = {}, s1 = {};
      const float* p = red + (T * 4 * 32 + a) * 36 + b0;
#pragma unroll
      for (int w = 0; w < 4; ++w) { s0 += *(const f32x4*)(p + w * 32 * 36); s1 += *(const f32x4*)(p + w * 32 * 36 + 4); }
      u32x4 o;
#pragma unroll
      for (int j = 0; j < 4; ++j) {
        const f2 y = unpack2((j < 2) ? pack2(s0[2 * j] * kinv, s0[2 * j + 1] * kinv) : pack2(s1[2 * j - 4] * kinv, s1[2 * j - 3] * kinv));
        const f2 us = unpack2(uv0[j]) * dsk;
        const f2 t = unpack2(pack2(us.x, us.y)) + y;
        o[j] = pack2(t.x, t.y);
      }
      st16_wt(at + c * 8, o);
    }
    asm volatile("s_waitcnt vmcnt(0)" ::: "memory");
    __syncthreads();
    if (tid == 0) __hip_atomic_fetch_add((gu32*)(ws + WS_CNT + (bid >> 5) * 256), 1u, __ATOMIC_RELAXED, __HIP_MEMORY_SCOPE_AGENT);
    DIAG_END(ws);
    return;
  }
  {
    const int gb = bid - 256;
    const int xcd = gb & 7, loc = gb >> 3;
    const int mt = xcd * 4 + (loc & 3);
    const int nt = loc >> 2;
    const u16* hT = (const u16*)(ws + WS_H);
    const u16* wdt = (const u16*)(ws + WS_WD);
    float* red = (float*)smem;
    u16* atile = (u16*)(smem + G2_RED);
    f32x4 acc[4][4] = {};
    const int ks0 = wid * 7;
    const __amdgpu_buffer_rsrc_t hr = __builtin_amdgcn_make_buffer_rsrc((void*)(hT + ((size_t)(mt * 4) * 56 + ks0) * 512), 0, 4 * 56 * 1024, 0x00020000);
    const __amdgpu_buffer_rsrc_t wr = __builtin_amdgcn_make_buffer_rsrc((void*)(wdt + ((size_t)(nt * 4) * 56 + ks0) * 512), 0, 4 * 56 * 1024, 0x00020000);
    const int vo = lane * 16;
#define G2_LD(rs, i, k) __builtin_bit_cast(bf16x8, __builtin_amdgcn_raw_buffer_load_b128(rs, vo, ((i) * 56 + (k)) * 1024, 0))
    bf16x8 ha[4], hb[4], wa[4];
#pragma unroll
    for (int i = 0; i < 4; ++i) { ha[i] = G2_LD(hr, i, 0); wa[i] = G2_LD(wr, i, 0); }
#define G2_STEP(k, HC, HN)                                                                             \
  do {                                                                                                 \
    if ((k) + 1 < 7) {                                                                                 \
      _Pragma("unroll") for (int i = 0; i < 4; ++i) HN[i] = G2_LD(hr, i, (k) + 1);                    \
    }                                                                                                  \
    _Pragma("unroll") for (int j = 0; j < 4; ++j) {                                                    \
      __builtin_amdgcn_sched_barrier(0);                                                               \
      _Pragma("unroll") for (int i = 0; i < 4; ++i)                                                    \
        acc[j][i] = __builtin_amdgcn_mfma_f32_16x16x32_bf16(wa[j], HC[i], acc[j][i], 0, 0, 0);         \
      __builtin_amdgcn_sched_barrier(0);                                                               \
      if ((k) + 1 < 7) wa[j] = G2_LD(wr, j, (k) + 1);                                                  \
    }                                                                                                  \
  } while (0)
    G2_STEP(0, ha, hb);
    G2_STEP(1, hb, ha);
    G2_STEP(2, ha, hb);
    G2_STEP(3, hb, ha);
    G2_STEP(4, ha, hb);
    G2_STEP(5, hb, ha);
    G2_STEP(6, ha, hb);
    DIAG_STAMP(1);
    const int w3 = wid & 3;
    float* rp = red + (w3 * 64 + (lane & 15)) * 68 + 4 * (lane >> 4);
    if (wid >= 4) {
#pragma unroll
      for (int j = 0; j < 4; ++j)
#pragma unroll
        for (int i = 0; i < 4; ++i) *(f32x4*)(rp + i * 16 * 68 + j * 16) = acc[j][i];
    }
    __syncthreads();
    if (wid < 4) {
#pragma unroll
      for (int j = 0; j < 4; ++j)
#pragma unroll
        for (int i = 0; i < 4; ++i) { f32x4* q = (f32x4*)(rp + i * 16 * 68 + j * 16); *q = *q + acc[j][i]; }
    }
    if (tid == 511) {
      gu32* cnt = (gu32*)(ws + WS_CNT + nt * 256);
      for (unsigned spins = 0; spins < (1u << 22); ++spins) {
        if (__hip_atomic_load(cnt, __ATOMIC_RELAXED, __HIP_MEMORY_SCOPE_AGENT) >= 32u) break;
        __builtin_amdgcn_s_sleep(1);
      }
    }
    __syncthreads();
    DIAG_STAMP(2);
    {
      const u16* at = (const u16*)(ws + WS_AT) + (size_t)(nt * 64 + (tid >> 3)) * L_ + mt * 64 + (tid & 7) * 8;
      u32x4 av;
      asm volatile("global_load_dwordx4 %0, %1, off sc1\n\ts_waitcnt vmcnt(0)" : "=&v"(av) : "v"(at) : "memory");
      *(u32x4*)(atile + (tid >> 3) * 72 + (tid & 7) * 8) = av;
    }
    const int m = tid >> 3, n0 = (tid & 7) * 8;
    const size_t go = (size_t)(mt * 64 + m) * D_ + nt * 64 + n0;
    const u32x4 xv = *(const u32x4*)(x + go);
    f32x4 p0 = {}, p1 = {};
#pragma unroll
    for (int w = 0; w < 4; ++w) {
      p0 += *(const f32x4*)(red + (w * 64 + m) * 68 + n0);
      p1 += *(const f32x4*)(red + (w * 64 + m) * 68 + n0 + 4);
    }
    __syncthreads();
    u32x4 o;
#pragma unroll
    for (int j = 0; j < 4; ++j) {
      const f2 xx = unpack2(xv[j]);
      const f2 aa = f2{bf2f(atile[(n0 + 2 * j) * 72 + m]), bf2f(atile[(n0 + 2 * j + 1) * 72 + m])};
      const f2 r1 = xx + aa;
      const f2 bb = unpack2((j < 2) ? pack2(p0[2 * j], p0[2 * j + 1]) : pack2(p1[2 * j - 4], p1[2 * j - 3]));
      const f2 r2 = unpack2(pack2(r1.x, r1.y)) + bb;
      o[j] = pack2(r2.x, r2.y);
    }
    *(u32x4*)(out + go) = o;
    DIAG_END(ws);
  }
}

extern "C" void kernel_launch(void* const* d_in, const int* in_sizes, int n_in, void* d_out, int out_size,
                              void* d_ws, size_t ws_size, hipStream_t stream) {
  const u16* x = (const u16*)d_in[0];
  const u16* logA = (const u16*)d_in[1];
  const u16* Aim = (const u16*)d_in[2];
  const u16* Cre = (const u16*)d_in[3];
  const u16* Cim = (const u16*)d_in[4];
  const u16* logdt = (const u16*)d_in[5];
  const u16* dskip = (const u16*)d_in[6];
  const u16* wg = (const u16*)d_in[8];
  const u16* wu = (const u16*)d_in[9];
  const u16* wd = (const u16*)d_in[10];
  char* ws = (char*)d_ws;
  (void)hipFuncSetAttribute((const void*)k2_kernel, hipFuncAttributeMaxDynamicSharedMemorySize, K2_LDS);
  (void)hipFuncSetAttribute((const void*)k3_kernel, hipFuncAttributeMaxDynamicSharedMemorySize, K3_LDS);
  k1_kernel<<<K1_LN_BLOCKS + K1_WT_BLOCKS + K1_KG_BLOCKS, 256, 0, stream>>>(x, logA, Aim, Cre, Cim, logdt, wg, wu, wd, ws);
  k2_kernel<<<256, 512, K2_LDS, stream>>>(ws);
  k3_kernel<<<512, 512, K3_LDS, stream>>>(x, dskip, ws, (u16*)d_out);
}
```

```cpp
#include <hip/hip_runtime.h>
#include <hip/hip_bf16.h>

typedef unsigned short u16;
typedef unsigned int u32;
using bf16x8 = __attribute__((ext_vector_type(8))) short;
using f16x8 = __attribute__((ext_vector_type(8))) _Float16;
using f16x4 = __attribute__((ext_vector_type(4))) _Float16;
using f32x4 = __attribute__((ext_vector_type(4))) float;
using f32x16 = __attribute__((ext_vector_type(16))) float;
using u32x4 = __attribute__((ext_vector_type(4))) unsigned int;
using u32x2 = __attribute__((ext_vector_type(2))) unsigned int;
typedef float f2 __attribute__((ext_vector_type(2)));
typedef __attribute__((address_space(1))) unsigned gu32;

constexpr int L_ = 2048, D_ = 512, N_ = 128, F_ = 1792;
constexpr int KC_STRIDE = 648;

constexpr size_t WS_CNT = 0;
constexpr size_t WS_XNT = 4096;
constexpr size_t WS_XNTR = WS_XNT + (size_t)L_ * D_ * 2;
constexpr size_t WS_KC = WS_XNTR + (size_t)L_ * D_ * 2;
constexpr size_t WS_H = WS_KC + (size_t)D_ * KC_STRIDE * 4;
constexpr size_t WS_WG = WS_H + (size_t)L_ * F_ * 2;
constexpr size_t WS_WU = WS_WG + (size_t)D_ * F_ * 2;
constexpr size_t WS_WD = WS_WU + (size_t)D_ * F_ * 2;
constexpr size_t WS_AT = WS_WD + (size_t)D_ * F_ * 2;

#define DIAG_START(k)
#define DIAG_STAMP(i)
#define DIAG_END(wsp)

__device__ __forceinline__ float bf2f(u16 v) { return __uint_as_float(((u32)v) << 16); }
__device__ __forceinline__ u16 f2bf(float f) {
  __bf16 b = (__bf16)f;
  return __builtin_bit_cast(u16, b);
}
__device__ __forceinline__ float rbf(float f) { return bf2f(f2bf(f)); }
__device__ __forceinline__ u32 pack2(float lo, float hi) {
  typedef __bf16 b2 __attribute__((ext_vector_type(2)));
  b2 r = __builtin_convertvector(f2{lo, hi}, b2);
  return __builtin_bit_cast(u32, r);
}
__device__ __forceinline__ f2 unpack2(u32 w) { return f2{__uint_as_float(w << 16), __uint_as_float(w & 0xffff0000u)}; }

__device__ __forceinline__ void st16_wt(void* p, u32x4 v) { asm volatile("global_store_dwordx4 %0, %1, off sc1\n\ts_nop 1" ::"v"(p), "v"(v) : "memory"); }
__device__ __forceinline__ void st16(void* p, u32x4 v) { *(u32x4*)p = v; }
__device__ __forceinline__ void st8(void* p, u32x2 v) { *(u32x2*)p = v; }

#define DPP_F(v, ctrl) __builtin_bit_cast(float, __builtin_amdgcn_update_dpp(0, __builtin_bit_cast(int, v), ctrl, 0xf, 0xf, true))
__device__ __forceinline__ float wave_sum(float v) {
  v += DPP_F(v, 0xB1);
  v += DPP_F(v, 0x4E);
  v += DPP_F(v, 0x141);
  v += DPP_F(v, 0x140);
  const int iv = __builtin_bit_cast(int, v);
  return (__builtin_bit_cast(float, __builtin_amdgcn_readlane(iv, 0)) + __builtin_bit_cast(float, __builtin_amdgcn_readlane(iv, 16))) +
         (__builtin_bit_cast(float, __builtin_amdgcn_readlane(iv, 32)) + __builtin_bit_cast(float, __builtin_amdgcn_readlane(iv, 48)));
}
__device__ __forceinline__ float wave_max(float v) {
  v = fmaxf(v, DPP_F(v, 0xB1));
  v = fmaxf(v, DPP_F(v, 0x4E));
  v = fmaxf(v, DPP_F(v, 0x141));
  v = fmaxf(v, DPP_F(v, 0x140));
  const int iv = __builtin_bit_cast(int, v);
  return fmaxf(fmaxf(__builtin_bit_cast(float, __builtin_amdgcn_readlane(iv, 0)), __builtin_bit_cast(float, __builtin_amdgcn_readlane(iv, 16))),
               fmaxf(__builtin_bit_cast(float, __builtin_amdgcn_readlane(iv, 32)), __builtin_bit_cast(float, __builtin_amdgcn_readlane(iv, 48))));
}

constexpr int K1_LN_BLOCKS = 128;
constexpr int K1_WT_BLOCKS = 336;
constexpr int K1_KG_BLOCKS = 512;

__device__ __forceinline__ void cmul6(f2& r0, f2& r1, f2& r2, f2& r3, f2& r4, f2& r5, f2 a0, f2 b0, f2 a1, f2 b1,
                                      f2 a2, f2 b2, f2 a3, f2 b3, f2 a4, f2 b4, f2 a5, f2 b5) {
  asm("v_pk_mul_f32 %0, %6, %7 op_sel:[1,1] op_sel_hi:[1,0] neg_lo:[0,1]\n\t"
      "v_pk_mul_f32 %1, %8, %9 op_sel:[1,1] op_sel_hi:[1,0] neg_lo:[0,1]\n\t"
      "v_pk_mul_f32 %2, %10, %11 op_sel:[1,1] op_sel_hi:[1,0] neg_lo:[0,1]\n\t"
      "v_pk_mul_f32 %3, %12, %13 op_sel:[1,1] op_sel_hi:[1,0] neg_lo:[0,1]\n\t"
      "v_pk_mul_f32 %4, %14, %15 op_sel:[1,1] op_sel_hi:[1,0] neg_lo:[0,1]\n\t"
      "v_pk_mul_f32 %5, %16, %17 op_sel:[1,1] op_sel_hi:[1,0] neg_lo:[0,1]\n\t"
      "v_pk_fma_f32 %0, %6, %7, %0 op_sel:[0,0,0] op_sel_hi:[0,1,1]\n\t"
      "v_pk_fma_f32 %1, %8, %9, %1 op_sel:[0,0,0] op_sel_hi:[0,1,1]\n\t"
      "v_pk_fma_f32 %2, %10, %11, %2 op_sel:[0,0,0] op_sel_hi:[0,1,1]\n\t"
      "v_pk_fma_f32 %3, %12, %13, %3 op_sel:[0,0,0] op_sel_hi:[0,1,1]\n\t"
      "v_pk_fma_f32 %4, %14, %15, %4 op_sel:[0,0,0] op_sel_hi:[0,1,1]\n\t"
      "v_pk_fma_f32 %5, %16, %17, %5 op_sel:[0,0,0] op_sel_hi:[0,1,1]\n\t"
      "s_nop 1"
      : "=&v"(r0), "=&v"(r1), "=&v"(r2), "=&v"(r3), "=&v"(r4), "=&v"(r5)
      : "v"(a0), "v"(b0), "v"(a1), "v"(b1), "v"(a2), "v"(b2), "v"(a3), "v"(b3), "v"(a4), "v"(b4), "v"(a5), "v"(b5));
}
__device__ __forceinline__ void cmul2(f2& r0, f2& r1, f2 a0, f2 b0, f2 a1, f2 b1) {
  asm("v_pk_mul_f32 %0, %2, %3 op_sel:[1,1] op_sel_hi:[1,0] neg_lo:[0,1]\n\t"
      "v_pk_mul_f32 %1, %4, %5 op_sel:[1,1] op_sel_hi:[1,0] neg_lo:[0,1]\n\t"
      "s_nop 0\n\t"
      "v_pk_fma_f32 %0, %2, %3, %0 op_sel:[0,0,0] op_sel_hi:[0,1,1]\n\t"
      "v_pk_fma_f32 %1, %4, %5, %1 op_sel:[0,0,0] op_sel_hi:[0,1,1]\n\t"
      "s_nop 1"
      : "=&v"(r0), "=&v"(r1)
      : "v"(a0), "v"(b0), "v"(a1), "v"(b1));
}
__device__ __forceinline__ f2 cexp_(float zr, float zi, float t) {
  const float m = __builtin_amdgcn_exp2f((zr * t) * 1.44269504088896341f);
  float r = (zi * t) * 0.15915494309189535f;
  r -= rintf(r);
  return f2{m * __builtin_amdgcn_cosf(r), m * __builtin_amdgcn_sinf(r)};
}

__global__ __launch_bounds__(256) void k1_kernel(const u16* __restrict__ x, const u16* __restrict__ logA,
                                                 const u16* __restrict__ Aim, const u16* __restrict__ Cre,
                                                 const u16* __restrict__ Cim, const u16* __restrict__ logdt,
                                                 const u16* __restrict__ wg, const u16* __restrict__ wu,
                                                 const u16* __restrict__ wd, char* __restrict__ ws) {
  __shared__ __attribute__((aligned(16))) char smem[18432];
  const int tid = threadIdx.x, lane = tid & 63, wid = tid >> 6;
  DIAG_START(0)
  int bid = blockIdx.x - K1_KG_BLOCKS;
  if (bid >= 0 && bid < K1_LN_BLOCKS) {
    __builtin_amdgcn_s_setprio(3);
    u16* tile = (u16*)smem;
    u16* xnt = (u16*)(ws + WS_XNT);
    const int r0 = bid * 16;
    u32x4 v[4];
#pragma unroll
    for (int i = 0; i < 4; ++i) v[i] = *(const u32x4*)(x + (size_t)(r0 + wid * 4 + i) * D_ + lane * 8);
#pragma unroll
    for (int i = 0; i < 4; ++i) {
      const int rl = wid * 4 + i, row = r0 + rl;
      f2 f[4];
#pragma unroll
      for (int j = 0; j < 4; ++j) f[j] = unpack2(v[i][j]);
      const f2 s2 = (f[0] + f[1]) + (f[2] + f[3]);
      const float mean = wave_sum(s2.x + s2.y) * (1.f / D_);
      f2 q2 = {0.f, 0.f};
#pragma unroll
      for (int j = 0; j < 4; ++j) { const f2 t = f[j] - mean; q2 = __builtin_elementwise_fma(t, t, q2); }
      const float q = wave_sum(q2.x + q2.y);
      const float mb = rbf(mean);
      const float vb = rbf(q * (1.f / D_));
      const float sd = rbf(sqrtf(rbf(vb + 1e-5f)));
      const float rs = __builtin_amdgcn_rcpf(sd);
      u32x4 o;
#pragma unroll
      for (int j = 0; j < 4; ++j) {
        const f2 t = f[j] - mb;
        const f2 a = unpack2(pack2(t.x, t.y));
        f2 qq = a * rs;
        const f2 rem = __builtin_elementwise_fma(-qq, f2{sd, sd}, a);
        qq = __builtin_elementwise_fma(rem, f2{rs, rs}, qq);
        o[j] = pack2(qq.x, qq.y);
      }
      st16(xnt + ((size_t)((row >> 4) * 16 + (lane >> 2)) * 64 + (lane & 3) * 16 + (row & 15)) * 8, o);
      *(u32x4*)(tile + rl * 520 + lane * 8) = o;
    }
    DIAG_STAMP(1);
    __syncthreads();
    u16* xtr = (u16*)(ws + WS_XNTR);
#pragma unroll
    for (int h = 0; h < 2; ++h) {
      const int d = tid + h * 256;
#pragma unroll
      for (int g = 0; g < 2; ++g) {
        u32x4 o;
#pragma unroll
        for (int j = 0; j < 4; ++j)
          o[j] = (u32)tile[(g * 8 + 2 * j) * 520 + d] | ((u32)tile[(g * 8 + 2 * j + 1) * 520 + d] << 16);
        st16(xtr + (size_t)d * L_ + r0 + g * 8, o);
      }
    }
    DIAG_END(ws);
    return;
  }
  bid -= K1_LN_BLOCKS;
  if (bid >= 0) {
    __builtin_amdgcn_s_setprio(2);
    int task = bid * 4 + wid;
    const u16* W; u16* Wt; int Kd, Nd;
    if (task < 448) { W = wg; Wt = (u16*)(ws + WS_WG); Kd = D_; Nd = F_; }
    else if (task < 896) { task -= 448; W = wu; Wt = (u16*)(ws + WS_WU); Kd = D_; Nd = F_; }
    else { task -= 896; W = wd; Wt = (u16*)(ws + WS_WD); Kd = F_; Nd = D_; }
    const int nks = Kd / 32;
    const int ks = task % nks, nb = task / nks;
    const int col = nb * 64 + lane;
    u16 v[32];
#pragma unroll
    for (int k = 0; k < 32; ++k) v[k] = W[(size_t)(ks * 32 + k) * Nd + col];
#pragma unroll
    for (int q = 0; q < 4; ++q) {
      u32x4 o;
#pragma unroll
      for (int j = 0; j < 4; ++j) o[j] = (u32)v[q * 8 + 2 * j] | ((u32)v[q * 8 + 2 * j + 1] << 16);
      st16(Wt + ((size_t)((col >> 4) * nks + ks) * 64 + q * 16 + (col & 15)) * 8, o);
    }
    DIAG_END(ws);
    return;
  }
  bid = blockIdx.x;
  {
    const int d = bid;
    float4* prm = (float4*)(smem + 16384);
    if (tid < 128) {
      const u16 la = logA[d * N_ + tid], ai = Aim[d * N_ + tid], cr = Cre[d * N_ + tid], ci = Cim[d * N_ + tid];
      const float dt = rbf(expf(bf2f(logdt[d])));
      const float ar = -rbf(expf(bf2f(la)));
      prm[tid] = make_float4(ar * dt, bf2f(ai) * dt, bf2f(cr) * dt, bf2f(ci) * dt);
    }
    __syncthreads();
    DIAG_STAMP(1);
    const int kq = lane >> 4, idx = lane & 15;
    f32x4 acc[4] = {};
    const float ty = (float)idx, te = (float)(idx == 0 ? 256 : 16 * idx);
    float4 pr[8];
#pragma unroll
    for (int s = 0; s < 8; ++s) pr[s] = prm[wid * 32 + s * 4 + kq];
#pragma unroll
    for (int s = 0; s < 8; s += 2) {
      const float4 pa = pr[s], pb = pr[s + 1];
      const f2 Ca = f2{pa.z, -pa.w}, Cb = f2{pb.z, -pb.w};
      f2 Ya[4], Yb[4], Xa[4], Xb[4];
      Ya[0] = cexp_(pa.x, pa.y, ty); Yb[0] = cexp_(pb.x, pb.y, ty);
      f2 Ea = cexp_(pa.x, -pa.y, te), Eb = cexp_(pb.x, -pb.y, te);
      f2 Fa = (idx == 0) ? f2{1.f, 0.f} : Ea, Fb = (idx == 0) ? f2{1.f, 0.f} : Eb;
#pragma unroll
      for (int e = 0; e < 3; ++e) {
        f2 nEa, nEb;
        cmul6(Xa[e], Xb[e], Ya[e + 1], Yb[e + 1], nEa, nEb, Ca, Fa, Cb, Fb, Ya[e], Ya[e], Yb[e], Yb[e], Ea, Ea, Eb, Eb);
        Ea = nEa; Eb = nEb; Fa = nEa; Fb = nEb;
      }
      cmul2(Xa[3], Xb[3], Ca, Ea, Cb, Eb);
#pragma unroll
      for (int e = 0; e < 4; ++e) acc[e] = __builtin_amdgcn_mfma_f32_16x16x4f32(Xa[e].x, Ya[e].x, acc[e], 0, 0, 0);
#pragma unroll
      for (int e = 0; e < 4; ++e) acc[e] = __builtin_amdgcn_mfma_f32_16x16x4f32(Xa[e].y, Ya[e].y, acc[e], 0, 0, 0);
#pragma unroll
      for (int e = 0; e < 4; ++e) acc[e] = __builtin_amdgcn_mfma_f32_16x16x4f32(Xb[e].x, Yb[e].x, acc[e], 0, 0, 0);
#pragma unroll
      for (int e = 0; e < 4; ++e) acc[e] = __builtin_amdgcn_mfma_f32_16x16x4f32(Xb[e].y, Yb[e].y, acc[e], 0, 0, 0);
    }
    DIAG_STAMP(2);
    float* red = (float*)smem;
#pragma unroll
    for (int e = 0; e < 4; ++e)
#pragma unroll
      for (int r = 0; r < 4; ++r) red[(wid * 4 + e) * 256 + ((lane >> 4) * 4 + r) * 16 + (lane & 15)] = acc[e][r];
    __syncthreads();
    float* kc = (float*)(ws + WS_KC) + (size_t)d * KC_STRIDE;
#pragma unroll
    for (int e = 0; e < 4; ++e) {
      const float v = 2.f * ((red[(0 * 4 + e) * 256 + tid] + red[(1 * 4 + e) * 256 + tid]) +
                             (red[(2 * 4 + e) * 256 + tid] + red[(3 * 4 + e) * 256 + tid]));
      if (e == 0) kc[tid] = v;
      else if (tid >= 128) kc[128 * e + tid] = v;
      else if (e == 3 && tid == 0) kc[640] = v;
    }
    DIAG_END(ws);
  }
}

constexpr int G1_STAGE = 22 * 1024;
constexpr int G1_NBUF = 7;
constexpr int K2_LDS = G1_STAGE * G1_NBUF;

__device__ __forceinline__ f2 gelu2(f2 g) {
  const float c0 = -2.f * 0.7978845608028654f * 1.44269504088896341f;
  const float c1 = c0 * 0.044715f;
  const f2 t = g * __builtin_elementwise_fma(g * g, f2{c1, c1}, f2{c0, c0});
  const f2 dn = f2{__builtin_amdgcn_exp2f(t.x), __builtin_amdgcn_exp2f(t.y)} + 1.f;
  return g * f2{__builtin_amdgcn_rcpf(dn.x), __builtin_amdgcn_rcpf(dn.y)};
}

__device__ __forceinline__ void glds16(const void* g, void* l) {
  __builtin_amdgcn_global_load_lds((const __attribute__((address_space(1))) u32*)g,
                                   (__attribute__((address_space(3))) u32*)l, 16, 0, 0);
}

__global__ __launch_bounds__(512, 2) void k2_kernel(char* __restrict__ ws) {
  extern __shared__ __attribute__((aligned(16))) char smem[];
  const int tid = threadIdx.x, lane = tid & 63;
  const int wid = __builtin_amdgcn_readfirstlane(tid >> 6);
  DIAG_START(1)
  const int bid = blockIdx.x;
  *(u32x4*)(ws + WS_AT + ((size_t)bid * 512 + tid) * 16) = u32x4{0xffffffffu, 0xffffffffu, 0xffffffffu, 0xffffffffu};
  const int xcd = bid & 7, loc = bid >> 3;
  const int mt = (xcd >> 1) * 4 + (loc & 3);
  const int nt = (xcd & 1) * 8 + (loc >> 2);
  const u16* xnt = (const u16*)(ws + WS_XNT);
  const u16* wgt = (const u16*)(ws + WS_WG);
  const u16* wut = (const u16*)(ws + WS_WU);
  auto src = [&](int f) -> const u16* {
    if (f < 8) return xnt + (size_t)(mt * 8 + f) * 16 * 512;
    const int s = f - 8;
    return ((s & 1) ? wut : wgt) + (size_t)(nt * 7 + (s >> 1)) * 16 * 512;
  };
  const u16* s0 = src(wid) + lane * 8;
  const u16* s1 = src(wid + 8) + lane * 8;
  const u16* s2 = src(wid < 6 ? wid + 16 : 21) + lane * 8;
  auto stage = [&](int ks) {
    char* base = smem + (ks % G1_NBUF) * G1_STAGE + wid * 1024 + lane * 16;
    glds16(s0 + ks * 512, base);
    glds16(s1 + ks * 512, base + 8192);
    if (wid < 6) glds16(s2 + ks * 512, base + 16384);
  };
  const int wm = wid >> 1, wn = wid & 1;
  f32x4 acc[7][2] = {};
  bf16x8 xa[2], wa[7], xb[2], wb[7];
  auto rd = [&](int ks, bf16x8(&xf)[2], bf16x8(&wf)[7]) {
    const char* base = smem + (ks % G1_NBUF) * G1_STAGE + lane * 16;
#pragma unroll
    for (int i = 0; i < 2; ++i) xf[i] = *(const bf16x8*)(base + (wm * 2 + i) * 1024);
#pragma unroll
    for (int j = 0; j < 7; ++j) wf[j] = *(const bf16x8*)(base + (8 + wn * 7 + j) * 1024);
  };
  auto mm = [&](bf16x8(&xf)[2], bf16x8(&wf)[7]) {
#pragma unroll
    for (int j = 0; j < 7; ++j)
#pragma unroll
      for (int i = 0; i < 2; ++i) acc[j][i] = __builtin_amdgcn_mfma_f32_16x16x32_bf16(wf[j], xf[i], acc[j][i], 0, 0, 0);
  };
#define G1_WAIT(n) do { if (wid < 6) asm volatile("s_waitcnt vmcnt(%0)" ::"n"(3 * (n)) : "memory"); \
                        else asm volatile("s_waitcnt vmcnt(%0)" ::"n"(2 * (n)) : "memory"); } while (0)
#define G1_BAR() do { asm volatile("s_waitcnt lgkmcnt(0)" ::: "memory"); __builtin_amdgcn_s_barrier(); } while (0)
#pragma unroll
  for (int k = 0; k < 7; ++k) stage(k);
  G1_WAIT(6);
  G1_BAR();
  DIAG_STAMP(1);
  rd(0, xa, wa);
#define G1_ITER(k, XC, WC, XN, WN, NLEFT)                                                          \
  do {                                                                                             \
    G1_WAIT(NLEFT);                                                                                \
    G1_BAR();                                                                                      \
    const char* rb_ = smem + (((k) + 1) % G1_NBUF) * G1_STAGE + lane * 16;                         \
    _Pragma("unroll") for (int j = 0; j < 7; ++j) {                                                \
      acc[j][0] = __builtin_amdgcn_mfma_f32_16x16x32_bf16(WC[j], XC[0], acc[j][0], 0, 0, 0);       \
      if (j < 2) XN[j] = *(const bf16x8*)(rb_ + (wm * 2 + j) * 1024);                              \
      acc[j][1] = __builtin_amdgcn_mfma_f32_16x16x32_bf16(WC[j], XC[1], acc[j][1], 0, 0, 0);       \
      WN[j] = *(const bf16x8*)(rb_ + (8 + wn * 7 + j) * 1024);                                     \
      __builtin_amdgcn_sched_barrier(0);                                                           \
      if (j == 3 && (k) + 7 < 16) { stage((k) + 7); __builtin_amdgcn_sched_barrier(0); }           \
    }                                                                                              \
  } while (0)
  G1_ITER(0, xa, wa, xb, wb, 5);
  G1_ITER(1, xb, wb, xa, wa, 5);
  G1_ITER(2, xa, wa, xb, wb, 5);
  G1_ITER(3, xb, wb, xa, wa, 5);
  G1_ITER(4, xa, wa, xb, wb, 5);
  G1_ITER(5, xb, wb, xa, wa, 5);
  G1_ITER(6, xa, wa, xb, wb, 5);
  G1_ITER(7, xb, wb, xa, wa, 5);
  DIAG_STAMP(2);
  G1_ITER(8, xa, wa, xb, wb, 5);
  G1_ITER(9, xb, wb, xa, wa, 5);
  G1_ITER(10, xa, wa, xb, wb, 4);
  G1_ITER(11, xb, wb, xa, wa, 3);
  G1_ITER(12, xa, wa, xb, wb, 2);
  G1_ITER(13, xb, wb, xa, wa, 1);
  G1_ITER(14, xa, wa, xb, wb, 0);
  mm(xb, wb);
  DIAG_STAMP(3);
  __syncthreads();
  f32x4* ex = (f32x4*)smem;
  if (wn == 0) {
#pragma unroll
    for (int i = 0; i < 2; ++i) ex[(wm * 2 + i) * 64 + lane] = acc[6][i];
  }
  __syncthreads();
  u16* hT = (u16*)(ws + WS_H);
  auto emit = [&](int t, const f32x4& g, const f32x4& u, int i) {
    u32x2 o;
#pragma unroll
    for (int p = 0; p < 2; ++p) {
      const f2 gb = unpack2(pack2(g[2 * p], g[2 * p + 1]));
      const f2 ub = unpack2(pack2(u[2 * p], u[2 * p + 1]));
      const f2 ge = gelu2(gb);
      const f2 hv = unpack2(pack2(ge.x, ge.y)) * ub;
      o[p] = pack2(hv.x, hv.y);
    }
    const int T = nt * 7 + t;
    const int ks2 = T >> 1;
    const int q = 2 * (T & 1) + (lane >> 5);
    const int m16 = mt * 8 + wm * 2 + i;
    st8(hT + ((size_t)(m16 * 56 + ks2) * 64 + q * 16 + (lane & 15)) * 8 + ((lane >> 4) & 1) * 4, o);
  };
  if (wn == 0) {
#pragma unroll
    for (int t = 0; t < 3; ++t)
#pragma unroll
      for (int i = 0; i < 2; ++i) emit(t, acc[2 * t][i], acc[2 * t + 1][i], i);
  } else {
#pragma unroll
    for (int i = 0; i < 2; ++i) emit(3, ex[(wm * 2 + i) * 64 + lane], acc[0][i], i);
#pragma unroll
    for (int t = 4; t < 7; ++t)
#pragma unroll
      for (int i = 0; i < 2; ++i) emit(t, acc[2 * t - 7][i], acc[2 * t - 6][i], i);
  }
  DIAG_END(ws);
}

constexpr int UC_LEN = 2080;
constexpr int KR_LEN = 3840;
constexpr int CV_UCP = 0;
constexpr int CV_KR = CV_UCP + 8 * UC_LEN * 2;
constexpr int CV_URAW = CV_UCP + 6 * UC_LEN * 2;
constexpr int CV_KRAW = CV_URAW + 4224;
constexpr int CV_MAX = CV_KRAW + 2592;
constexpr int CV_CH_STRIDE = 40960;
constexpr int G2_RED = 4 * 64 * 68 * 4;
constexpr int K3_LDS = 2 * CV_CH_STRIDE;

__device__ __forceinline__ u32 bf2_to_h2(u32 w) {
  auto r = __builtin_amdgcn_cvt_pkrtz(__uint_as_float(w << 16), __uint_as_float(w & 0xffff0000u));
  return __builtin_bit_cast(u32, r);
}

__global__ __launch_bounds__(512, 4) void k3_kernel(const u16* __restrict__ x, const u16* __restrict__ dskip,
                                                    char* __restrict__ ws, u16* __restrict__ out) {
  extern __shared__ __attribute__((aligned(16))) char smem[];
  const int tid = threadIdx.x, lane = tid & 63;
  const int wid = __builtin_amdgcn_readfirstlane(tid >> 6);
  DIAG_START(2)
  const int bid = blockIdx.x;
  if (bid < 256) {
    __builtin_amdgcn_s_setprio(3);
    const int ch = wid >> 2, w4 = wid & 3;
    const int d = bid * 2 + ch;
    const int t2 = tid & 255;
    char* cbase = smem + ch * CV_CH_STRIDE;
    u16* ucp = (u16*)(cbase + CV_UCP);
    u16* kr = (u16*)(cbase + CV_KR);
    u32x4* uraw = (u32x4*)(cbase + CV_URAW);
    float* kraw = (float*)(cbase + CV_KRAW);
    float* kmax = (float*)(cbase + CV_MAX);
    const u16* ug = (const u16*)(ws + WS_XNTR) + (size_t)d * L_;
    const float* kc = (const float*)(ws + WS_KC) + (size_t)d * KC_STRIDE;
    const u32x4 uv0 = *(const u32x4*)(ug + t2 * 8);
    const float kv0 = kc[t2], kv1 = kc[t2 + 256], kv2 = (t2 < 129) ? kc[t2 + 512] : 0.f;
    const float dsk = bf2f(dskip[d]);
    if (t2 < 4) { uraw[t2] = u32x4{0, 0, 0, 0}; uraw[260 + t2] = u32x4{0, 0, 0, 0}; }
    {
      u32x4 h;
#pragma unroll
      for (int j = 0; j < 4; ++j) h[j] = bf2_to_h2(uv0[j]);
      uraw[4 + t2] = h;
    }
    kraw[t2] = kv0;
    kraw[t2 + 256] = kv1;
    if (t2 < 136) kraw[t2 + 512] = kv2;
    {
      const float mx = wave_max(fmaxf(fmaxf(fabsf(kv0), fabsf(kv1)), fabsf(kv2)));
      if (lane == 0) kmax[w4] = mx;
    }
    __syncthreads();
    DIAG_STAMP(1);
    float kscale, kinv;
    {
      const float mx = fmaxf(fmaxf(kmax[0], kmax[1]), fmaxf(kmax[2], kmax[3]));
      int e = (int)((__float_as_uint(mx) >> 23) & 0xff);
      e = min(max(e, 20), 240);
      kscale = __uint_as_float((u32)(127 + 13 + 127 - e) << 23);
      kinv = __uint_as_float((u32)(e - 13) << 23);
    }
    u32x4 c6[2], c7[2];
#pragma unroll
    for (int it = 0; it < 2; ++it) {
      const int tb = it ? 256 + (t2 & 3) : t2;
      u32 w[8];
      u32x4 a = uraw[tb], b = uraw[tb + 1];
#pragma unroll
      for (int j = 0; j < 4; ++j) { w[j] = a[j]; w[4 + j] = b[j]; }
#pragma unroll
      for (int c = 0; c < 8; ++c) {
        u32x4 o;
#pragma unroll
        for (int j = 0; j < 4; ++j)
          o[j] = (c & 1) ? w[j + (c + 1) / 2] : __builtin_amdgcn_alignbit(w[j + c / 2 + 1], w[j + c / 2], 16);
        if (c < 6) { if (it == 0 || t2 < 4) *(u32x4*)(ucp + c * UC_LEN + tb * 8) = o; }
        else if (c == 6) c6[it] = o;
        else c7[it] = o;
      }
    }
    {
      float kk[8];
#pragma unroll
      for (int j = 0; j < 8; ++j) {
        const int jj = 2047 - (t2 * 8 + j);
        const int cbi = (int)f2bf((float)jj) - 17024;
        kk[j] = kraw[jj < 128 ? jj : cbi] * kscale;
      }
      f16x8 o;
#pragma unroll
      for (int j = 0; j < 8; ++j) o[j] = (_Float16)kk[j];
      *(f16x8*)(kr + t2 * 8 + 8 * (t2 >> 2)) = o;
      if (t2 < 128) { const int tb = t2 + 256; *(u32x4*)(kr + tb * 8 + 8 * (tb >> 2)) = u32x4{0, 0, 0, 0}; }
    }
    __syncthreads();
    *(u32x4*)(ucp + 6 * UC_LEN + t2 * 8) = c6[0];
    *(u32x4*)(ucp + 7 * UC_LEN + t2 * 8) = c7[0];
    if (t2 < 4) {
      *(u32x4*)(ucp + 6 * UC_LEN + (256 + t2) * 8) = c6[1];
      *(u32x4*)(ucp + 7 * UC_LEN + (256 + t2) * 8) = c7[1];
    }
    __syncthreads();
    DIAG_STAMP(2);
    const int r = lane & 31, h = lane >> 5;
    const u16* ap = ucp + (r & 7) * UC_LEN + 8 * h + 8 * (r >> 3);
    const int bofs0 = 2048 - 32 * (r + 1) + 8 * h + 8 * (63 - r);
    const int bofs1 = bofs0 - 1024 - 256;
    f32x16 acc0 = {}, acc1 = {};
    {
      const u16* apw = ap + 16 * (w4 * 16);
      const u16* bp0 = kr + bofs0 + 16 * (w4 * 16) + 8 * (w4 * 8);
      const u16* bp1 = kr + bofs1 + 16 * (w4 * 16) + 8 * (w4 * 8);
      auto lda = [&](int s) -> f16x8 { return *(const f16x8*)(apw + 16 * s); };
      auto ldb = [&](const u16* b, int s) -> f16x8 { return *(const f16x8*)(b + 16 * s + 8 * (s >> 1)); };
      f16x8 a0 = lda(0), p0 = ldb(bp0, 0), q0 = ldb(bp1, 0), c0 = lda(64);
      f16x8 a1 = lda(1), p1 = ldb(bp0, 1), q1 = ldb(bp1, 1), c1 = lda(65);
#pragma unroll
      for (int it = 0; it < 8; ++it) {
        f16x8 na0, na1, nc0, nc1, np0, np1, nq0, nq1;
        if (it < 7) {
          const int sn = 2 * it + 2;
          na0 = lda(sn); np0 = ldb(bp0, sn); nq0 = ldb(bp1, sn); nc0 = lda(sn + 64);
          na1 = lda(sn + 1); np1 = ldb(bp0, sn + 1); nq1 = ldb(bp1, sn + 1); nc1 = lda(sn + 65);
        }
        __builtin_amdgcn_sched_barrier(0);
        acc0 = __builtin_amdgcn_mfma_f32_32x32x16_f16(a0, p0, acc0, 0, 0, 0);
        acc1 = __builtin_amdgcn_mfma_f32_32x32x16_f16(a0, q0, acc1, 0, 0, 0);
        acc1 = __builtin_amdgcn_mfma_f32_32x32x16_f16(c0, p0, acc1, 0, 0, 0);
        acc0 = __builtin_amdgcn_mfma_f32_32x32x16_f16(a1, p1, acc0, 0, 0, 0);
        acc1 = __builtin_amdgcn_mfma_f32_32x32x16_f16(a1, q1, acc1, 0, 0, 0);
        acc1 = __builtin_amdgcn_mfma_f32_32x32x16_f16(c1, p1, acc1, 0, 0, 0);
        __builtin_amdgcn_sched_barrier(0);
        if (it < 7) { a0 = na0; a1 = na1; c0 = nc0; c1 = nc1; p0 = np0; p1 = np1; q0 = nq0; q1 = nq1; }
      }
    }
    DIAG_STAMP(3);
    __syncthreads();
    DIAG_STAMP(4);
    float* red = (float*)cbase;
#pragma unroll
    for (int g = 0; g < 4; ++g) {
      f32x4 v0 = {acc0[4 * g], acc0[4 * g + 1], acc0[4 * g + 2], acc0[4 * g + 3]};
      f32x4 v1 = {acc1[4 * g], acc1[4 * g + 1], acc1[4 * g + 2], acc1[4 * g + 3]};
      *(f32x4*)(red + (w4 * 32 + r) * 36 + 8 * g + 4 * h) = v0;
      *(f32x4*)(red + ((4 + w4) * 32 + r) * 36 + 8 * g + 4 * h) = v1;
    }
    __syncthreads();
    u16* at = (u16*)(ws + WS_AT) + (size_t)d * L_;
    {
      const int c = t2;
      const int T = c >> 7, a = (c >> 2) & 31, b0 = (c & 3) * 8;
      f32x4 s0 = {}, s1 = {};
      const float* p = red + (T * 4 * 32 + a) * 36 + b0;
#pragma unroll
      for (int w = 0; w < 4; ++w) { s0 += *(const f32x4*)(p + w * 32 * 36); s1 += *(const f32x4*)(p + w * 32 * 36 + 4); }
      u32x4 o;
#pragma unroll
      for (int j = 0; j < 4; ++j) {
        const f2 y = unpack2((j < 2) ? pack2(s0[2 * j] * kinv, s0[2 * j + 1] * kinv) : pack2(s1[2 * j - 4] * kinv, s1[2 * j - 3] * kinv));
        const f2 us = unpack2(uv0[j]) * dsk;
        const f2 t = unpack2(pack2(us.x, us.y)) + y;
        u32 ov = pack2(t.x, t.y);
        if ((ov & 0xffffu) == 0xffffu) ov &= 0xffffffc0u;
        if ((ov >> 16) == 0xffffu) ov &= 0xffc0ffffu;
        o[j] = ov;
      }
      st16_wt(at + c * 8, o);
    }
    DIAG_END(ws);
    return;
  }
  {
    const int gb = bid - 256;
    const int xcd = gb & 7, loc = gb >> 3;
    const int mt = xcd * 4 + (loc & 3);
    const int nt = loc >> 2;
    const u16* hT = (const u16*)(ws + WS_H);
    const u16* wdt = (const u16*)(ws + WS_WD);
    float* red = (float*)smem;
    f32x4 acc[4][4] = {};
    const int ks0 = wid * 7;
    const __amdgpu_buffer_rsrc_t hr = __builtin_amdgcn_make_buffer_rsrc((void*)(hT + ((size_t)(mt * 4) * 56 + ks0) * 512), 0, 4 * 56 * 1024, 0x00020000);
    const __amdgpu_buffer_rsrc_t wr = __builtin_amdgcn_make_buffer_rsrc((void*)(wdt + ((size_t)(nt * 4) * 56 + ks0) * 512), 0, 4 * 56 * 1024, 0x00020000);
    const int vo = lane * 16;
#define G2_LD(rs, i, k) __builtin_bit_cast(bf16x8, __builtin_amdgcn_raw_buffer_load_b128(rs, vo, ((i) * 56 + (k)) * 1024, 0))
    bf16x8 ha[4], hb[4], wa[4];
#pragma unroll
    for (int i = 0; i < 4; ++i) { ha[i] = G2_LD(hr, i, 0); wa[i] = G2_LD(wr, i, 0); }
#define G2_STEP(k, HC, HN)                                                                             \
  do {                                                                                                 \
    if ((k) + 1 < 7) {                                                                                 \
      _Pragma("unroll") for (int i = 0; i < 4; ++i) HN[i] = G2_LD(hr, i, (k) + 1);                    \
    }                                                                                                  \
    _Pragma("unroll") for (int j = 0; j < 4; ++j) {                                                    \
      __builtin_amdgcn_sched_barrier(0);                                                               \
      _Pragma("unroll") for (int i = 0; i < 4; ++i)                                                    \
        acc[j][i] = __builtin_amdgcn_mfma_f32_16x16x32_bf16(wa[j], HC[i], acc[j][i], 0, 0, 0);         \
      __builtin_amdgcn_sched_barrier(0);                                                               \
      if ((k) + 1 < 7) wa[j] = G2_LD(wr, j, (k) + 1);                                                  \
    }                                                                                                  \
  } while (0)
    G2_STEP(0, ha, hb);
    G2_STEP(1, hb, ha);
    G2_STEP(2, ha, hb);
    G2_STEP(3, hb, ha);
    G2_STEP(4, ha, hb);
    G2_STEP(5, hb, ha);
    G2_STEP(6, ha, hb);
    DIAG_STAMP(1);
    const int w3 = wid & 3;
    float* rp = red + (w3 * 64 + (lane & 15)) * 68 + 4 * (lane >> 4);
    if (wid >= 4) {
#pragma unroll
      for (int j = 0; j < 4; ++j)
#pragma unroll
        for (int i = 0; i < 4; ++i) *(f32x4*)(rp + i * 16 * 68 + j * 16) = acc[j][i];
    }
    __syncthreads();
    if (wid < 4) {
#pragma unroll
      for (int j = 0; j < 4; ++j)
#pragma unroll
        for (int i = 0; i < 4; ++i) { f32x4* q = (f32x4*)(rp + i * 16 * 68 + j * 16); *q = *q + acc[j][i]; }
    }
    const int m = tid >> 3, n0 = (tid & 7) * 8;
    const size_t go = (size_t)(mt * 64 + m) * D_ + nt * 64 + n0;
    const u32x4 xv = *(const u32x4*)(x + go);
    __syncthreads();
    DIAG_STAMP(2);
    f32x4 p0 = {}, p1 = {};
#pragma unroll
    for (int w = 0; w < 4; ++w) {
      p0 += *(const f32x4*)(red + (w * 64 + m) * 68 + n0);
      p1 += *(const f32x4*)(red + (w * 64 + m) * 68 + n0 + 4);
    }
    u32 a0, a1, a2, a3, a4, a5, a6, a7;
    {
      const u16* at = (const u16*)(ws + WS_AT) + (size_t)(nt * 64 + n0) * L_ + mt * 64 + m;
      for (unsigned spins = 0; spins < (1u << 20); ++spins) {
        asm volatile(
            "global_load_ushort %0, %8, off sc1\n\t"
            "global_load_ushort %1, %9, off sc1\n\t"
            "global_load_ushort %2, %10, off sc1\n\t"
            "global_load_ushort %3, %11, off sc1\n\t"
            "global_load_ushort %4, %12, off sc1\n\t"
            "global_load_ushort %5, %13, off sc1\n\t"
            "global_load_ushort %6, %14, off sc1\n\t"
            "global_load_ushort %7, %15, off sc1\n\t"
            "s_waitcnt vmcnt(0)"
            : "=&v"(a0), "=&v"(a1), "=&v"(a2), "=&v"(a3), "=&v"(a4), "=&v"(a5), "=&v"(a6), "=&v"(a7)
            : "v"(at), "v"(at + L_), "v"(at + 2 * L_), "v"(at + 3 * L_), "v"(at + 4 * L_), "v"(at + 5 * L_), "v"(at + 6 * L_), "v"(at + 7 * L_)
            : "memory");
        const bool pend = (a0 == 0xffffu) | (a1 == 0xffffu) | (a2 == 0xffffu) | (a3 == 0xffffu) | (a4 == 0xffffu) |
                          (a5 == 0xffffu) | (a6 == 0xffffu) | (a7 == 0xffffu);
        if (!__any(pend)) break;
        __builtin_amdgcn_s_sleep(2);
      }
    }
    const u32 av[8] = {a0, a1, a2, a3, a4, a5, a6, a7};
    u32x4 o;
#pragma unroll
    for (int j = 0; j < 4; ++j) {
      const f2 xx = unpack2(xv[j]);
      const f2 aa = f2{__uint_as_float(av[2 * j] << 16), __uint_as_float(av[2 * j + 1] << 16)};
      const f2 r1 = xx + aa;
      const f2 bb = unpack2((j < 2) ? pack2(p0[2 * j], p0[2 * j + 1]) : pack2(p1[2 * j - 4], p1[2 * j - 3]));
      const f2 r2 = unpack2(pack2(r1.x, r1.y)) + bb;
      o[j] = pack2(r2.x, r2.y);
    }
    *(u32x4*)(out + go) = o;
    DIAG_END(ws);
  }
}

extern "C" void kernel_launch(void* const* d_in, const int* in_sizes, int n_in, void* d_out, int out_size,
                              void* d_ws, size_t ws_size, hipStream_t stream) {
  const u16* x = (const u16*)d_in[0];
  const u16* logA = (const u16*)d_in[1];
  const u16* Aim = (const u16*)d_in[2];
  const u16* Cre = (const u16*)d_in[3];
  const u16* Cim = (const u16*)d_in[4];
  const u16* logdt = (const u16*)d_in[5];
  const u16* dskip = (const u16*)d_in[6];
  const u16* wg = (const u16*)d_in[8];
  const u16* wu = (const u16*)d_in[9];
  const u16* wd = (const u16*)d_in[10];
  char* ws = (char*)d_ws;
  (void)hipFuncSetAttribute((const void*)k2_kernel, hipFuncAttributeMaxDynamicSharedMemorySize, K2_LDS);
  (void)hipFuncSetAttribute((const void*)k3_kernel, hipFuncAttributeMaxDynamicSharedMemorySize, K3_LDS);
  k1_kernel<<<K1_LN_BLOCKS + K1_WT_BLOCKS + K1_KG_BLOCKS, 256, 0, stream>>>(x, logA, Aim, Cre, Cim, logdt, wg, wu, wd, ws);
  k2_kernel<<<256, 512, K2_LDS, stream>>>(ws);
  k3_kernel<<<512, 512, K3_LDS, stream>>>(x, dskip, ws, (u16*)d_out);
}
```

```cpp
#include <hip/hip_runtime.h>
#include <hip/hip_bf16.h>
#define WT_STORES

typedef unsigned short u16;
typedef unsigned int u32;
using bf16x8 = __attribute__((ext_vector_type(8))) short;
using f16x8 = __attribute__((ext_vector_type(8))) _Float16;
using f16x4 = __attribute__((ext_vector_type(4))) _Float16;
using f32x4 = __attribute__((ext_vector_type(4))) float;
using f32x16 = __attribute__((ext_vector_type(16))) float;
using u32x4 = __attribute__((ext_vector_type(4))) unsigned int;
using u32x2 = __attribute__((ext_vector_type(2))) unsigned int;
typedef float f2 __attribute__((ext_vector_type(2)));
typedef __attribute__((address_space(1))) unsigned gu32;

constexpr int L_ = 2048, D_ = 512, N_ = 128, F_ = 1792;
constexpr int KC_STRIDE = 648;

constexpr size_t WS_CNT = 0;
constexpr size_t WS_XNT = 4096;
constexpr size_t WS_XNTR = WS_XNT + (size_t)L_ * D_ * 2;
constexpr size_t WS_KC = WS_XNTR + (size_t)L_ * D_ * 2;
constexpr size_t WS_H = WS_KC + (size_t)D_ * KC_STRIDE * 4;
constexpr size_t WS_WG = WS_H + (size_t)L_ * F_ * 2;
constexpr size_t WS_WU = WS_WG + (size_t)D_ * F_ * 2;
constexpr size_t WS_WD = WS_WU + (size_t)D_ * F_ * 2;
constexpr size_t WS_AT = WS_WD + (size_t)D_ * F_ * 2;

#define DIAG_START(k)
#define DIAG_STAMP(i)
#define DIAG_END(wsp)

__device__ __forceinline__ float bf2f(u16 v) { return __uint_as_float(((u32)v) << 16); }
__device__ __forceinline__ u16 f2bf(float f) {
  __bf16 b = (__bf16)f;
  return __builtin_bit_cast(u16, b);
}
__device__ __forceinline__ float rbf(float f) { return bf2f(f2bf(f)); }
__device__ __forceinline__ u32 pack2(float lo, float hi) {
  typedef __bf16 b2 __attribute__((ext_vector_type(2)));
  b2 r = __builtin_convertvector(f2{lo, hi}, b2);
  return __builtin_bit_cast(u32, r);
}
__device__ __forceinline__ f2 unpack2(u32 w) { return f2{__uint_as_float(w << 16), __uint_as_float(w & 0xffff0000u)}; }

__device__ __forceinline__ void st16_wt(void* p, u32x4 v) { asm volatile("global_store_dwordx4 %0, %1, off sc1\n\ts_nop 1" ::"v"(p), "v"(v) : "memory"); }
__device__ __forceinline__ void st16(void* p, u32x4 v) { st16_wt(p, v); }
__device__ __forceinline__ void st8(void* p, u32x2 v) { asm volatile("global_store_dwordx2 %0, %1, off sc1\n\ts_nop 1" ::"v"(p), "v"(v) : "memory"); }

#define DPP_F(v, ctrl) __builtin_bit_cast(float, __builtin_amdgcn_update_dpp(0, __builtin_bit_cast(int, v), ctrl, 0xf, 0xf, true))
__device__ __forceinline__ float wave_sum(float v) {
  v += DPP_F(v, 0xB1);
  v += DPP_F(v, 0x4E);
  v += DPP_F(v, 0x141);
  v += DPP_F(v, 0x140);
  const int iv = __builtin_bit_cast(int, v);
  return (__builtin_bit_cast(float, __builtin_amdgcn_readlane(iv, 0)) + __builtin_bit_cast(float, __builtin_amdgcn_readlane(iv, 16))) +
         (__builtin_bit_cast(float, __builtin_amdgcn_readlane(iv, 32)) + __builtin_bit_cast(float, __builtin_amdgcn_readlane(iv, 48)));
}
__device__ __forceinline__ float wave_max(float v) {
  v = fmaxf(v, DPP_F(v, 0xB1));
  v = fmaxf(v, DPP_F(v, 0x4E));
  v = fmaxf(v, DPP_F(v, 0x141));
  v = fmaxf(v, DPP_F(v, 0x140));
  const int iv = __builtin_bit_cast(int, v);
  return fmaxf(fmaxf(__builtin_bit_cast(float, __builtin_amdgcn_readlane(iv, 0)), __builtin_bit_cast(float, __builtin_amdgcn_readlane(iv, 16))),
               fmaxf(__builtin_bit_cast(float, __builtin_amdgcn_readlane(iv, 32)), __builtin_bit_cast(float, __builtin_amdgcn_readlane(iv, 48))));
}

constexpr int K1_LN_BLOCKS = 128;
constexpr int K1_WT_BLOCKS = 336;
constexpr int K1_KG_BLOCKS = 512;

__device__ __forceinline__ void cmul6(f2& r0, f2& r1, f2& r2, f2& r3, f2& r4, f2& r5, f2 a0, f2 b0, f2 a1, f2 b1,
                                      f2 a2, f2 b2, f2 a3, f2 b3, f2 a4, f2 b4, f2 a5, f2 b5) {
  asm("v_pk_mul_f32 %0, %6, %7 op_sel:[1,1] op_sel_hi:[1,0] neg_lo:[0,1]\n\t"
      "v_pk_mul_f32 %1, %8, %9 op_sel:[1,1] op_sel_hi:[1,0] neg_lo:[0,1]\n\t"
      "v_pk_mul_f32 %2, %10, %11 op_sel:[1,1] op_sel_hi:[1,0] neg_lo:[0,1]\n\t"
      "v_pk_mul_f32 %3, %12, %13 op_sel:[1,1] op_sel_hi:[1,0] neg_lo:[0,1]\n\t"
      "v_pk_mul_f32 %4, %14, %15 op_sel:[1,1] op_sel_hi:[1,0] neg_lo:[0,1]\n\t"
      "v_pk_mul_f32 %5, %16, %17 op_sel:[1,1] op_sel_hi:[1,0] neg_lo:[0,1]\n\t"
      "v_pk_fma_f32 %0, %6, %7, %0 op_sel:[0,0,0] op_sel_hi:[0,1,1]\n\t"
      "v_pk_fma_f32 %1, %8, %9, %1 op_sel:[0,0,0] op_sel_hi:[0,1,1]\n\t"
      "v_pk_fma_f32 %2, %10, %11, %2 op_sel:[0,0,0] op_sel_hi:[0,1,1]\n\t"
      "v_pk_fma_f32 %3, %12, %13, %3 op_sel:[0,0,0] op_sel_hi:[0,1,1]\n\t"
      "v_pk_fma_f32 %4, %14, %15, %4 op_sel:[0,0,0] op_sel_hi:[0,1,1]\n\t"
      "v_pk_fma_f32 %5, %16, %17, %5 op_sel:[0,0,0] op_sel_hi:[0,1,1]\n\t"
      "s_nop 1"
      : "=&v"(r0), "=&v"(r1), "=&v"(r2), "=&v"(r3), "=&v"(r4), "=&v"(r5)
      : "v"(a0), "v"(b0), "v"(a1), "v"(b1), "v"(a2), "v"(b2), "v"(a3), "v"(b3), "v"(a4), "v"(b4), "v"(a5), "v"(b5));
}
__device__ __forceinline__ void cmul2(f2& r0, f2& r1, f2 a0, f2 b0, f2 a1, f2 b1) {
  asm("v_pk_mul_f32 %0, %2, %3 op_sel:[1,1] op_sel_hi:[1,0] neg_lo:[0,1]\n\t"
      "v_pk_mul_f32 %1, %4, %5 op_sel:[1,1] op_sel_hi:[1,0] neg_lo:[0,1]\n\t"
      "s_nop 0\n\t"
      "v_pk_fma_f32 %0, %2, %3, %0 op_sel:[0,0,0] op_sel_hi:[0,1,1]\n\t"
      "v_pk_fma_f32 %1, %4, %5, %1 op_sel:[0,0,0] op_sel_hi:[0,1,1]\n\t"
      "s_nop 1"
      : "=&v"(r0), "=&v"(r1)
      : "v"(a0), "v"(b0), "v"(a1), "v"(b1));
}
__device__ __forceinline__ f2 cexp_(float zr, float zi, float t) {
  const float m = __builtin_amdgcn_exp2f((zr * t) * 1.44269504088896341f);
  float r = (zi * t) * 0.15915494309189535f;
  r -= rintf(r);
  return f2{m * __builtin_amdgcn_cosf(r), m * __builtin_amdgcn_sinf(r)};
}

__global__ __launch_bounds__(256) void k1_kernel(const u16* __restrict__ x, const u16* __restrict__ logA,
                                                 const u16* __restrict__ Aim, const u16* __restrict__ Cre,
                                                 const u16* __restrict__ Cim, const u16* __restrict__ logdt,
                                                 const u16* __restrict__ wg, const u16* __restrict__ wu,
                                                 const u16* __restrict__ wd, char* __restrict__ ws) {
  __shared__ __attribute__((aligned(16))) char smem[18432];
  const int tid = threadIdx.x, lane = tid & 63, wid = tid >> 6;
  DIAG_START(0)
  int bid = blockIdx.x - K1_KG_BLOCKS;
  if (bid >= 0 && bid < K1_LN_BLOCKS) {
    __builtin_amdgcn_s_setprio(3);
    u16* tile = (u16*)smem;
    u16* xnt = (u16*)(ws + WS_XNT);
    const int r0 = bid * 16;
    u32x4 v[4];
#pragma unroll
    for (int i = 0; i < 4; ++i) v[i] = *(const u32x4*)(x + (size_t)(r0 + wid * 4 + i) * D_ + lane * 8);
#pragma unroll
    for (int i = 0; i < 4; ++i) {
      const int rl = wid * 4 + i;
      f2 f[4];
#pragma unroll
      for (int j = 0; j < 4; ++j) f[j] = unpack2(v[i][j]);
      const f2 s2 = (f[0] + f[1]) + (f[2] + f[3]);
      const float mean = wave_sum(s2.x + s2.y) * (1.f / D_);
      f2 q2 = {0.f, 0.f};
#pragma unroll
      for (int j = 0; j < 4; ++j) { const f2 t = f[j] - mean; q2 = __builtin_elementwise_fma(t, t, q2); }
      const float q = wave_sum(q2.x + q2.y);
      const float mb = rbf(mean);
      const float vb = rbf(q * (1.f / D_));
      const float sd = rbf(sqrtf(rbf(vb + 1e-5f)));
      const float rs = __builtin_amdgcn_rcpf(sd);
      u32x4 o;
#pragma unroll
      for (int j = 0; j < 4; ++j) {
        const f2 t = f[j] - mb;
        const f2 a = unpack2(pack2(t.x, t.y));
        f2 qq = a * rs;
        const f2 rem = __builtin_elementwise_fma(-qq, f2{sd, sd}, a);
        qq = __builtin_elementwise_fma(rem, f2{rs, rs}, qq);
        o[j] = pack2(qq.x, qq.y);
      }
      *(u32x4*)(tile + rl * 520 + lane * 8) = o;
    }
    DIAG_STAMP(1);
    __syncthreads();
#pragma unroll
    for (int it = 0; it < 4; ++it) {
      const int c = tid + it * 256;
      const u32x4 o = *(const u32x4*)(tile + (c & 15) * 520 + (c >> 4) * 8);
      st16(xnt + ((size_t)bid * 1024 + c) * 8, o);
    }
    u16* xtr = (u16*)(ws + WS_XNTR);
#pragma unroll
    for (int it = 0; it < 4; ++it) {
      const int c = tid + it * 256;
      const int d = c >> 1, g = c & 1;
      u32x4 o;
#pragma unroll
      for (int j = 0; j < 4; ++j)
        o[j] = (u32)tile[(g * 8 + 2 * j) * 520 + d] | ((u32)tile[(g * 8 + 2 * j + 1) * 520 + d] << 16);
      st16(xtr + ((size_t)bid * 1024 + c) * 8, o);
    }
    DIAG_END(ws);
    return;
  }
  bid -= K1_LN_BLOCKS;
  if (bid >= 0) {
    __builtin_amdgcn_s_setprio(2);
    int task = bid * 4 + wid;
    const u16* W; u16* Wt; int Kd, Nd;
    if (task < 448) { W = wg; Wt = (u16*)(ws + WS_WG); Kd = D_; Nd = F_; }
    else if (task < 896) { task -= 448; W = wu; Wt = (u16*)(ws + WS_WU); Kd = D_; Nd = F_; }
    else { task -= 896; W = wd; Wt = (u16*)(ws + WS_WD); Kd = F_; Nd = D_; }
    const int nks = Kd / 32;
    const int ks = task % nks, nb = task / nks;
    const int col = nb * 64 + lane;
    u16 v[32];
#pragma unroll
    for (int k = 0; k < 32; ++k) v[k] = W[(size_t)(ks * 32 + k) * Nd + col];
#pragma unroll
    for (int q = 0; q < 4; ++q) {
      u32x4 o;
#pragma unroll
      for (int j = 0; j < 4; ++j) o[j] = (u32)v[q * 8 + 2 * j] | ((u32)v[q * 8 + 2 * j + 1] << 16);
      st16(Wt + ((size_t)((col >> 4) * nks + ks) * 64 + q * 16 + (col & 15)) * 8, o);
    }
    DIAG_END(ws);
    return;
  }
  bid = blockIdx.x;
  {
    const int d = bid;
    float4* prm = (float4*)(smem + 16384);
    if (tid < 128) {
      const u16 la = logA[d * N_ + tid], ai = Aim[d * N_ + tid], cr = Cre[d * N_ + tid], ci = Cim[d * N_ + tid];
      const float dt = rbf(expf(bf2f(logdt[d])));
      const float ar = -rbf(expf(bf2f(la)));
      prm[tid] = make_float4(ar * dt, bf2f(ai) * dt, bf2f(cr) * dt, bf2f(ci) * dt);
    }
    __syncthreads();
    DIAG_STAMP(1);
    const int kq = lane >> 4, idx = lane & 15;
    f32x4 acc[4] = {};
    const float ty = (float)idx, te = (float)(idx == 0 ? 256 : 16 * idx);
    float4 pr[8];
#pragma unroll
    for (int s = 0; s < 8; ++s) pr[s] = prm[wid * 32 + s * 4 + kq];
#pragma unroll
    for (int s = 0; s < 8; s += 2) {
      const float4 pa = pr[s], pb = pr[s + 1];
      const f2 Ca = f2{pa.z, -pa.w}, Cb = f2{pb.z, -pb.w};
      f2 Ya[4], Yb[4], Xa[4], Xb[4];
      Ya[0] = cexp_(pa.x, pa.y, ty); Yb[0] = cexp_(pb.x, pb.y, ty);
      f2 Ea = cexp_(pa.x, -pa.y, te), Eb = cexp_(pb.x, -pb.y, te);
      f2 Fa = (idx == 0) ? f2{1.f, 0.f} : Ea, Fb = (idx == 0) ? f2{1.f, 0.f} : Eb;
#pragma unroll
      for (int e = 0; e < 3; ++e) {
        f2 nEa, nEb;
        cmul6(Xa[e], Xb[e], Ya[e + 1], Yb[e + 1], nEa, nEb, Ca, Fa, Cb, Fb, Ya[e], Ya[e], Yb[e], Yb[e], Ea, Ea, Eb, Eb);
        Ea = nEa; Eb = nEb; Fa = nEa; Fb = nEb;
      }
      cmul2(Xa[3], Xb[3], Ca, Ea, Cb, Eb);
#pragma unroll
      for (int e = 0; e < 4; ++e) acc[e] = __builtin_amdgcn_mfma_f32_16x16x4f32(Xa[e].x, Ya[e].x, acc[e], 0, 0, 0);
#pragma unroll
      for (int e = 0; e < 4; ++e) acc[e] = __builtin_amdgcn_mfma_f32_16x16x4f32(Xa[e].y, Ya[e].y, acc[e], 0, 0, 0);
#pragma unroll
      for (int e = 0; e < 4; ++e) acc[e] = __builtin_amdgcn_mfma_f32_16x16x4f32(Xb[e].x, Yb[e].x, acc[e], 0, 0, 0);
#pragma unroll
      for (int e = 0; e < 4; ++e) acc[e] = __builtin_amdgcn_mfma_f32_16x16x4f32(Xb[e].y, Yb[e].y, acc[e], 0, 0, 0);
    }
    DIAG_STAMP(2);
    float* red = (float*)smem;
#pragma unroll
    for (int e = 0; e < 4; ++e)
#pragma unroll
      for (int r = 0; r < 4; ++r) red[(wid * 4 + e) * 256 + ((lane >> 4) * 4 + r) * 16 + (lane & 15)] = acc[e][r];
    __syncthreads();
    float* kc = (float*)(ws + WS_KC) + (size_t)d * KC_STRIDE;
#pragma unroll
    for (int e = 0; e < 4; ++e) {
      const float v = 2.f * ((red[(0 * 4 + e) * 256 + tid] + red[(1 * 4 + e) * 256 + tid]) +
                             (red[(2 * 4 + e) * 256 + tid] + red[(3 * 4 + e) * 256 + tid]));
      if (e == 0) kc[tid] = v;
      else if (tid >= 128) kc[128 * e + tid] = v;
      else if (e == 3 && tid == 0) kc[640] = v;
    }
    DIAG_END(ws);
  }
}

constexpr int G1_STAGE = 22 * 1024;
constexpr int G1_NBUF = 7;
constexpr int K2_LDS = G1_STAGE * G1_NBUF;

__device__ __forceinline__ f2 gelu2(f2 g) {
  const float c0 = -2.f * 0.7978845608028654f * 1.44269504088896341f;
  const float c1 = c0 * 0.044715f;
  const f2 t = g * __builtin_elementwise_fma(g * g, f2{c1, c1}, f2{c0, c0});
  const f2 dn = f2{__builtin_amdgcn_exp2f(t.x), __builtin_amdgcn_exp2f(t.y)} + 1.f;
  return g * f2{__builtin_amdgcn_rcpf(dn.x), __builtin_amdgcn_rcpf(dn.y)};
}

__device__ __forceinline__ void glds16(const void* g, void* l) {
  __builtin_amdgcn_global_load_lds((const __attribute__((address_space(1))) u32*)g,
                                   (__attribute__((address_space(3))) u32*)l, 16, 0, 0);
}

__global__ __launch_bounds__(512, 2) void k2_kernel(char* __restrict__ ws) {
  extern __shared__ __attribute__((aligned(16))) char smem[];
  const int tid = threadIdx.x, lane = tid & 63;
  const int wid = __builtin_amdgcn_readfirstlane(tid >> 6);
  DIAG_START(1)
  const int bid = blockIdx.x;
  *(u32x4*)(ws + WS_AT + ((size_t)bid * 512 + tid) * 16) = u32x4{0xffffffffu, 0xffffffffu, 0xffffffffu, 0xffffffffu};
  const int xcd = bid & 7, loc = bid >> 3;
  const int mt = (xcd >> 1) * 4 + (loc & 3);
  const int nt = (xcd & 1) * 8 + (loc >> 2);
  const u16* xnt = (const u16*)(ws + WS_XNT);
  const u16* wgt = (const u16*)(ws + WS_WG);
  const u16* wut = (const u16*)(ws + WS_WU);
  auto src = [&](int f) -> const u16* {
    if (f < 8) return xnt + (size_t)(mt * 8 + f) * 16 * 512;
    const int s = f - 8;
    return ((s & 1) ? wut : wgt) + (size_t)(nt * 7 + (s >> 1)) * 16 * 512;
  };
  const u16* s0 = src(wid) + lane * 8;
  const u16* s1 = src(wid + 8) + lane * 8;
  const u16* s2 = src(wid < 6 ? wid + 16 : 21) + lane * 8;
  auto stage = [&](int ks) {
    char* base = smem + (ks % G1_NBUF) * G1_STAGE + wid * 1024 + lane * 16;
    glds16(s0 + ks * 512, base);
    glds16(s1 + ks * 512, base + 8192);
    if (wid < 6) glds16(s2 + ks * 512, base + 16384);
  };
  const int wm = wid >> 1, wn = wid & 1;
  f32x4 acc[7][2] = {};
  bf16x8 xa[2], wa[7], xb[2], wb[7];
  auto rd = [&](int ks, bf16x8(&xf)[2], bf16x8(&wf)[7]) {
    const char* base = smem + (ks % G1_NBUF) * G1_STAGE + lane * 16;
#pragma unroll
    for (int i = 0; i < 2; ++i) xf[i] = *(const bf16x8*)(base + (wm * 2 + i) * 1024);
#pragma unroll
    for (int j = 0; j < 7; ++j) wf[j] = *(const bf16x8*)(base + (8 + wn * 7 + j) * 1024);
  };
  auto mm = [&](bf16x8(&xf)[2], bf16x8(&wf)[7]) {
#pragma unroll
    for (int j = 0; j < 7; ++j)
#pragma unroll
      for (int i = 0; i < 2; ++i) acc[j][i] = __builtin_amdgcn_mfma_f32_16x16x32_bf16(wf[j], xf[i], acc[j][i], 0, 0, 0);
  };
#define G1_WAIT(n) do { if (wid < 6) asm volatile("s_waitcnt vmcnt(%0)" ::"n"(3 * (n)) : "memory"); \
                        else asm volatile("s_waitcnt vmcnt(%0)" ::"n"(2 * (n)) : "memory"); } while (0)
#define G1_BAR() do { asm volatile("s_waitcnt lgkmcnt(0)" ::: "memory"); __builtin_amdgcn_s_barrier(); } while (0)
#pragma unroll
  for (int k = 0; k < 7; ++k) stage(k);
  G1_WAIT(6);
  G1_BAR();
  DIAG_STAMP(1);
  rd(0, xa, wa);
#define G1_ITER(k, XC, WC, XN, WN, NLEFT)                                                          \
  do {                                                                                             \
    G1_WAIT(NLEFT);                                                                                \
    G1_BAR();                                                                                      \
    const char* rb_ = smem + (((k) + 1) % G1_NBUF) * G1_STAGE + lane * 16;                         \
    _Pragma("unroll") for (int j = 0; j < 7; ++j) {                                                \
      acc[j][0] = __builtin_amdgcn_mfma_f32_16x16x32_bf16(WC[j], XC[0], acc[j][0], 0, 0, 0);       \
      if (j < 2) XN[j] = *(const bf16x8*)(rb_ + (wm * 2 + j) * 1024);                              \
      acc[j][1] = __builtin_amdgcn_mfma_f32_16x16x32_bf16(WC[j], XC[1], acc[j][1], 0, 0, 0);       \
      WN[j] = *(const bf16x8*)(rb_ + (8 + wn * 7 + j) * 1024);                                     \
      __builtin_amdgcn_sched_barrier(0);                                                           \
      if (j == 3 && (k) + 7 < 16) { stage((k) + 7); __builtin_amdgcn_sched_barrier(0); }           \
    }                                                                                              \
  } while (0)
  G1_ITER(0, xa, wa, xb, wb, 5);
  G1_ITER(1, xb, wb, xa, wa, 5);
  G1_ITER(2, xa, wa, xb, wb, 5);
  G1_ITER(3, xb, wb, xa, wa, 5);
  G1_ITER(4, xa, wa, xb, wb, 5);
  G1_ITER(5, xb, wb, xa, wa, 5);
  G1_ITER(6, xa, wa, xb, wb, 5);
  G1_ITER(7, xb, wb, xa, wa, 5);
  DIAG_STAMP(2);
  G1_ITER(8, xa, wa, xb, wb, 5);
  G1_ITER(9, xb, wb, xa, wa, 5);
  G1_ITER(10, xa, wa, xb, wb, 4);
  G1_ITER(11, xb, wb, xa, wa, 3);
  G1_ITER(12, xa, wa, xb, wb, 2);
  G1_ITER(13, xb, wb, xa, wa, 1);
  G1_ITER(14, xa, wa, xb, wb, 0);
  mm(xb, wb);
  DIAG_STAMP(3);
  __syncthreads();
  f32x4* ex = (f32x4*)smem;
  if (wn == 0) {
#pragma unroll
    for (int i = 0; i < 2; ++i) ex[(wm * 2 + i) * 64 + lane] = acc[6][i];
  }
  __syncthreads();
  u16* hT = (u16*)(ws + WS_H);
  char* hs = smem + 16384;
  auto emit = [&](int t, const f32x4& g, const f32x4& u, int i) {
    u32x2 o;
#pragma unroll
    for (int p = 0; p < 2; ++p) {
      const f2 gb = unpack2(pack2(g[2 * p], g[2 * p + 1]));
      const f2 ub = unpack2(pack2(u[2 * p], u[2 * p + 1]));
      const f2 ge = gelu2(gb);
      const f2 hv = unpack2(pack2(ge.x, ge.y)) * ub;
      o[p] = pack2(hv.x, hv.y);
    }
    *(u32x2*)(hs + (((wm * 2 + i) * 7 + t) * 32 + (lane >> 5) * 16 + (lane & 15)) * 16 + ((lane >> 4) & 1) * 8) = o;
  };
  if (wn == 0) {
#pragma unroll
    for (int t = 0; t < 3; ++t)
#pragma unroll
      for (int i = 0; i < 2; ++i) emit(t, acc[2 * t][i], acc[2 * t + 1][i], i);
  } else {
#pragma unroll
    for (int i = 0; i < 2; ++i) emit(3, ex[(wm * 2 + i) * 64 + lane], acc[0][i], i);
#pragma unroll
    for (int t = 4; t < 7; ++t)
#pragma unroll
      for (int i = 0; i < 2; ++i) emit(t, acc[2 * t - 7][i], acc[2 * t - 6][i], i);
  }
  __syncthreads();
#pragma unroll
  for (int it = 0; it < 4; ++it) {
    const int c = tid + it * 512;
    if (c < 1792) {
      const int ml = c / 224, rem = c - ml * 224;
      st16(hT + ((size_t)(mt * 8 + ml) * 56 * 64 + (size_t)nt * 7 * 32 + rem) * 8, *(const u32x4*)(hs + c * 16));
    }
  }
  DIAG_END(ws);
}

constexpr int UC_LEN = 2080;
constexpr int KR_LEN = 3840;
constexpr int CV_UCP = 0;
constexpr int CV_KR = CV_UCP + 8 * UC_LEN * 2;
constexpr int CV_URAW = CV_UCP + 6 * UC_LEN * 2;
constexpr int CV_KRAW = CV_URAW + 4224;
constexpr int CV_MAX = CV_KRAW + 2592;
constexpr int CV_CH_STRIDE = 40960;
constexpr int G2_RED = 4 * 64 * 68 * 4;
constexpr int K3_LDS = 2 * CV_CH_STRIDE;

__device__ __forceinline__ u32 bf2_to_h2(u32 w) {
  auto r = __builtin_amdgcn_cvt_pkrtz(__uint_as_float(w << 16), __uint_as_float(w & 0xffff0000u));
  return __builtin_bit_cast(u32, r);
}

__global__ __launch_bounds__(512, 4) void k3_kernel(const u16* __restrict__ x, const u16* __restrict__ dskip,
                                                    char* __restrict__ ws, u16* __restrict__ out) {
  extern __shared__ __attribute__((aligned(16))) char smem[];
  const int tid = threadIdx.x, lane = tid & 63;
  const int wid = __builtin_amdgcn_readfirstlane(tid >> 6);
  DIAG_START(2)
  const int bid = blockIdx.x;
  if (bid < 256) {
    __builtin_amdgcn_s_setprio(3);
    const int ch = wid >> 2, w4 = wid & 3;
    const int d = bid * 2 + ch;
    const int t2 = tid & 255;
    char* cbase = smem + ch * CV_CH_STRIDE;
    u16* ucp = (u16*)(cbase + CV_UCP);
    u16* kr = (u16*)(cbase + CV_KR);
    u32x4* uraw = (u32x4*)(cbase + CV_URAW);
    float* kraw = (float*)(cbase + CV_KRAW);
    float* kmax = (float*)(cbase + CV_MAX);
    const u16* ug = (const u16*)(ws + WS_XNTR) + (size_t)(t2 >> 1) * 8192 + d * 16 + (t2 & 1) * 8;
    const float* kc = (const float*)(ws + WS_KC) + (size_t)d * KC_STRIDE;
    const u32x4 uv0 = *(const u32x4*)ug;
    const float kv0 = kc[t2], kv1 = kc[t2 + 256], kv2 = (t2 < 129) ? kc[t2 + 512] : 0.f;
    const float dsk = bf2f(dskip[d]);
    if (t2 < 4) { uraw[t2] = u32x4{0, 0, 0, 0}; uraw[260 + t2] = u32x4{0, 0, 0, 0}; }
    {
      u32x4 h;
#pragma unroll
      for (int j = 0; j < 4; ++j) h[j] = bf2_to_h2(uv0[j]);
      uraw[4 + t2] = h;
    }
    kraw[t2] = kv0;
    kraw[t2 + 256] = kv1;
    if (t2 < 136) kraw[t2 + 512] = kv2;
    {
      const float mx = wave_max(fmaxf(fmaxf(fabsf(kv0), fabsf(kv1)), fabsf(kv2)));
      if (lane == 0) kmax[w4] = mx;
    }
    __syncthreads();
    DIAG_STAMP(1);
    float kscale, kinv;
    {
      const float mx = fmaxf(fmaxf(kmax[0], kmax[1]), fmaxf(kmax[2], kmax[3]));
      int e = (int)((__float_as_uint(mx) >> 23) & 0xff);
      e = min(max(e, 20), 240);
      kscale = __uint_as_float((u32)(127 + 13 + 127 - e) << 23);
      kinv = __uint_as_float((u32)(e - 13) << 23);
    }
    u32x4 c6[2], c7[2];
#pragma unroll
    for (int it = 0; it < 2; ++it) {
      const int tb = it ? 256 + (t2 & 3) : t2;
      u32 w[8];
      u32x4 a = uraw[tb], b = uraw[tb + 1];
#pragma unroll
      for (int j = 0; j < 4; ++j) { w[j] = a[j]; w[4 + j] = b[j]; }
#pragma unroll
      for (int c = 0; c < 8; ++c) {
        u32x4 o;
#pragma unroll
        for (int j = 0; j < 4; ++j)
          o[j] = (c & 1) ? w[j + (c + 1) / 2] : __builtin_amdgcn_alignbit(w[j + c / 2 + 1], w[j + c / 2], 16);
        if (c < 6) { if (it == 0 || t2 < 4) *(u32x4*)(ucp + c * UC_LEN + tb * 8) = o; }
        else if (c == 6) c6[it] = o;
        else c7[it] = o;
      }
    }
    {
      float kk[8];
#pragma unroll
      for (int j = 0; j < 8; ++j) {
        const int jj = 2047 - (t2 * 8 + j);
        const int cbi = (int)f2bf((float)jj) - 17024;
        kk[j] = kraw[jj < 128 ? jj : cbi] * kscale;
      }
      f16x8 o;
#pragma unroll
      for (int j = 0; j < 8; ++j) o[j] = (_Float16)kk[j];
      *(f16x8*)(kr + t2 * 8 + 8 * (t2 >> 2)) = o;
      if (t2 < 128) { const int tb = t2 + 256; *(u32x4*)(kr + tb * 8 + 8 * (tb >> 2)) = u32x4{0, 0, 0, 0}; }
    }
    __syncthreads();
    *(u32x4*)(ucp + 6 * UC_LEN + t2 * 8) = c6[0];
    *(u32x4*)(ucp + 7 * UC_LEN + t2 * 8) = c7[0];
    if (t2 < 4) {
      *(u32x4*)(ucp + 6 * UC_LEN + (256 + t2) * 8) = c6[1];
      *(u32x4*)(ucp + 7 * UC_LEN + (256 + t2) * 8) = c7[1];
    }
    __syncthreads();
    DIAG_STAMP(2);
    const int r = lane & 31, h = lane >> 5;
    const u16* ap = ucp + (r & 7) * UC_LEN + 8 * h + 8 * (r >> 3);
    const int bofs0 = 2048 - 32 * (r + 1) + 8 * h + 8 * (63 - r);
    const int bofs1 = bofs0 - 1024 - 256;
    f32x16 acc0 = {}, acc1 = {};
    {
      const u16* apw = ap + 16 * (w4 * 16);
      const u16* bp0 = kr + bofs0 + 16 * (w4 * 16) + 8 * (w4 * 8);
      const u16* bp1 = kr + bofs1 + 16 * (w4 * 16) + 8 * (w4 * 8);
      auto lda = [&](int s) -> f16x8 { return *(const f16x8*)(apw + 16 * s); };
      auto ldb = [&](const u16* b, int s) -> f16x8 { return *(const f16x8*)(b + 16 * s + 8 * (s >> 1)); };
      f16x8 a0 = lda(0), p0 = ldb(bp0, 0), q0 = ldb(bp1, 0), c0 = lda(64);
      f16x8 a1 = lda(1), p1 = ldb(bp0, 1), q1 = ldb(bp1, 1), c1 = lda(65);
#pragma unroll
      for (int it = 0; it < 8; ++it) {
        f16x8 na0, na1, nc0, nc1, np0, np1, nq0, nq1;
        if (it < 7) {
          const int sn = 2 * it + 2;
          na0 = lda(sn); np0 = ldb(bp0, sn); nq0 = ldb(bp1, sn); nc0 = lda(sn + 64);
          na1 = lda(sn + 1); np1 = ldb(bp0, sn + 1); nq1 = ldb(bp1, sn + 1); nc1 = lda(sn + 65);
        }
        __builtin_amdgcn_sched_barrier(0);
        acc0 = __builtin_amdgcn_mfma_f32_32x32x16_f16(a0, p0, acc0, 0, 0, 0);
        acc1 = __builtin_amdgcn_mfma_f32_32x32x16_f16(a0, q0, acc1, 0, 0, 0);
        acc1 = __builtin_amdgcn_mfma_f32_32x32x16_f16(c0, p0, acc1, 0, 0, 0);
        acc0 = __builtin_amdgcn_mfma_f32_32x32x16_f16(a1, p1, acc0, 0, 0, 0);
        acc1 = __builtin_amdgcn_mfma_f32_32x32x16_f16(a1, q1, acc1, 0, 0, 0);
        acc1 = __builtin_amdgcn_mfma_f32_32x32x16_f16(c1, p1, acc1, 0, 0, 0);
        __builtin_amdgcn_sched_barrier(0);
        if (it < 7) { a0 = na0; a1 = na1; c0 = nc0; c1 = nc1; p0 = np0; p1 = np1; q0 = nq0; q1 = nq1; }
      }
    }
    DIAG_STAMP(3);
    __syncthreads();
    DIAG_STAMP(4);
    float* red = (float*)cbase;
#pragma unroll
    for (int g = 0; g < 4; ++g) {
      f32x4 v0 = {acc0[4 * g], acc0[4 * g + 1], acc0[4 * g + 2], acc0[4 * g + 3]};
      f32x4 v1 = {acc1[4 * g], acc1[4 * g + 1], acc1[4 * g + 2], acc1[4 * g + 3]};
      *(f32x4*)(red + (w4 * 32 + r) * 36 + 8 * g + 4 * h) = v0;
      *(f32x4*)(red + ((4 + w4) * 32 + r) * 36 + 8 * g + 4 * h) = v1;
    }
    __syncthreads();
    u16* at = (u16*)(ws + WS_AT) + (size_t)d * L_;
    {
      const int c = t2;
      const int T = c >> 7, a = (c >> 2) & 31, b0 = (c & 3) * 8;
      f32x4 s0 = {}, s1 = {};
      const float* p = red + (T * 4 * 32 + a) * 36 + b0;
#pragma unroll
      for (int w = 0; w < 4; ++w) { s0 += *(const f32x4*)(p + w * 32 * 36); s1 += *(const f32x4*)(p + w * 32 * 36 + 4); }
      u32x4 o;
#pragma unroll
      for (int j = 0; j < 4; ++j) {
        const f2 y = unpack2((j < 2) ? pack2(s0[2 * j] * kinv, s0[2 * j + 1] * kinv) : pack2(s1[2 * j - 4] * kinv, s1[2 * j - 3] * kinv));
        const f2 us = unpack2(uv0[j]) * dsk;
        const f2 t = unpack2(pack2(us.x, us.y)) + y;
        u32 ov = pack2(t.x, t.y);
        if ((ov & 0xffffu) == 0xffffu) ov &= 0xffffffc0u;
        if ((ov >> 16) == 0xffffu) ov &= 0xffc0ffffu;
        o[j] = ov;
      }
      st16_wt(at + c * 8, o);
    }
    DIAG_END(ws);
    return;
  }
  {
    const int gb = bid - 256;
    const int xcd = gb & 7, loc = gb >> 3;
    const int mt = xcd * 4 + (loc & 3);
    const int nt = loc >> 2;
    const u16* hT = (const u16*)(ws + WS_H);
    const u16* wdt = (const u16*)(ws + WS_WD);
    float* red = (float*)smem;
    f32x4 acc[4][4] = {};
    const int ks0 = wid * 7;
    const __amdgpu_buffer_rsrc_t hr = __builtin_amdgcn_make_buffer_rsrc((void*)(hT + ((size_t)(mt * 4) * 56 + ks0) * 512), 0, 4 * 56 * 1024, 0x00020000);
    const __amdgpu_buffer_rsrc_t wr = __builtin_amdgcn_make_buffer_rsrc((void*)(wdt + ((size_t)(nt * 4) * 56 + ks0) * 512), 0, 4 * 56 * 1024, 0x00020000);
    const int vo = lane * 16;
#define G2_LD(rs, i, k) __builtin_bit_cast(bf16x8, __builtin_amdgcn_raw_buffer_load_b128(rs, vo, ((i) * 56 + (k)) * 1024, 0))
    bf16x8 ha[4], hb[4], wa[4];
#pragma unroll
    for (int i = 0; i < 4; ++i) { ha[i] = G2_LD(hr, i, 0); wa[i] = G2_LD(wr, i, 0); }
    char* pf = smem + wid * 10240 + lane * 16;
    {
      const u16* hp = hT + ((size_t)(mt * 4) * 56 + ks0) * 512 + lane * 8;
      const u16* wp = wdt + ((size_t)(nt * 4) * 56 + ks0) * 512 + lane * 8;
#pragma unroll
      for (int i = 0; i < 4; ++i) {
        glds16(hp + (size_t)(i * 56 + 6) * 512, pf + i * 1024);
        glds16(wp + (size_t)(i * 56 + 6) * 512, pf + (4 + i) * 1024);
      }
      glds16(wp + (size_t)(2 * 56 + 5) * 512, pf + 8 * 1024);
      glds16(wp + (size_t)(3 * 56 + 5) * 512, pf + 9 * 1024);
    }
#define G2_STEP(k, HC, HN)                                                                             \
  do {                                                                                                 \
    if ((k) + 1 < 6) {                                                                                 \
      _Pragma("unroll") for (int i = 0; i < 4; ++i) HN[i] = G2_LD(hr, i, (k) + 1);                    \
    } else if ((k) + 1 == 6) {                                                                         \
      _Pragma("unroll") for (int i = 0; i < 4; ++i) HN[i] = *(const bf16x8*)(pf + i * 1024);          \
    }                                                                                                  \
    _Pragma("unroll") for (int j = 0; j < 4; ++j) {                                                    \
      __builtin_amdgcn_sched_barrier(0);                                                               \
      _Pragma("unroll") for (int i = 0; i < 4; ++i)                                                    \
        acc[j][i] = __builtin_amdgcn_mfma_f32_16x16x32_bf16(wa[j], HC[i], acc[j][i], 0, 0, 0);         \
      __builtin_amdgcn_sched_barrier(0);                                                               \
      if ((k) + 1 < 5 || ((k) + 1 == 5 && j < 2)) wa[j] = G2_LD(wr, j, (k) + 1);                       \
      else if ((k) + 1 == 5) wa[j] = *(const bf16x8*)(pf + (6 + j) * 1024);                            \
      else if ((k) + 1 == 6) wa[j] = *(const bf16x8*)(pf + (4 + j) * 1024);                            \
    }                                                                                                  \
  } while (0)
    G2_STEP(0, ha, hb);
    G2_STEP(1, hb, ha);
    G2_STEP(2, ha, hb);
    G2_STEP(3, hb, ha);
    asm volatile("s_waitcnt vmcnt(8)" ::: "memory");
    G2_STEP(4, ha, hb);
    G2_STEP(5, hb, ha);
    G2_STEP(6, ha, hb);
    __syncthreads();
    DIAG_STAMP(1);
    const int w3 = wid & 3;
    float* rp = red + (w3 * 64 + (lane & 15)) * 68 + 4 * (lane >> 4);
    if (wid >= 4) {
#pragma unroll
      for (int j = 0; j < 4; ++j)
#pragma unroll
        for (int i = 0; i < 4; ++i) *(f32x4*)(rp + i * 16 * 68 + j * 16) = acc[j][i];
    }
    __syncthreads();
    if (wid < 4) {
#pragma unroll
      for (int j = 0; j < 4; ++j)
#pragma unroll
        for (int i = 0; i < 4; ++i) { f32x4* q = (f32x4*)(rp + i * 16 * 68 + j * 16); *q = *q + acc[j][i]; }
    }
    const int m = tid >> 3, n0 = (tid & 7) * 8;
    const size_t go = (size_t)(mt * 64 + m) * D_ + nt * 64 + n0;
    const u32x4 xv = *(const u32x4*)(x + go);
    __syncthreads();
    DIAG_STAMP(2);
    f32x4 p0 = {}, p1 = {};
#pragma unroll
    for (int w = 0; w < 4; ++w) {
      p0 += *(const f32x4*)(red + (w * 64 + m) * 68 + n0);
      p1 += *(const f32x4*)(red + (w * 64 + m) * 68 + n0 + 4);
    }
    u32 a0, a1, a2, a3, a4, a5, a6, a7;
    {
      const u16* at = (const u16*)(ws + WS_AT) + (size_t)(nt * 64 + n0) * L_ + mt * 64 + m;
      for (unsigned spins = 0; spins < (1u << 20); ++spins) {
        asm volatile(
            "global_load_ushort %0, %8, off sc1\n\t"
            "global_load_ushort %1, %9, off sc1\n\t"
            "global_load_ushort %2, %10, off sc1\n\t"
            "global_load_ushort %3, %11, off sc1\n\t"
            "global_load_ushort %4, %12, off sc1\n\t"
            "global_load_ushort %5, %13, off sc1\n\t"
            "global_load_ushort %6, %14, off sc1\n\t"
            "global_load_ushort %7, %15, off sc1\n\t"
            "s_waitcnt vmcnt(0)"
            : "=&v"(a0), "=&v"(a1), "=&v"(a2), "=&v"(a3), "=&v"(a4), "=&v"(a5), "=&v"(a6), "=&v"(a7)
            : "v"(at), "v"(at + L_), "v"(at + 2 * L_), "v"(at + 3 * L_), "v"(at + 4 * L_), "v"(at + 5 * L_), "v"(at + 6 * L_), "v"(at + 7 * L_)
            : "memory");
        const bool pend = (a0 == 0xffffu) | (a1 == 0xffffu) | (a2 == 0xffffu) | (a3 == 0xffffu) | (a4 == 0xffffu) |
                          (a5 == 0xffffu) | (a6 == 0xffffu) | (a7 == 0xffffu);
        if (!__any(pend)) break;
        __builtin_amdgcn_s_sleep(2);
      }
    }
    const u32 av[8] = {a0, a1, a2, a3, a4, a5, a6, a7};
    u32x4 o;
#pragma unroll
    for (int j = 0; j < 4; ++j) {
      const f2 xx = unpack2(xv[j]);
      const f2 aa = f2{__uint_as_float(av[2 * j] << 16), __uint_as_float(av[2 * j + 1] << 16)};
      const f2 r1 = xx + aa;
      const f2 bb = unpack2((j < 2) ? pack2(p0[2 * j], p0[2 * j + 1]) : pack2(p1[2 * j - 4], p1[2 * j - 3]));
      const f2 r2 = unpack2(pack2(r1.x, r1.y)) + bb;
      o[j] = pack2(r2.x, r2.y);
    }
    *(u32x4*)(out + go) = o;
    DIAG_END(ws);
  }
}

extern "C" void kernel_launch(void* const* d_in, const int* in_sizes, int n_in, void* d_out, int out_size,
                              void* d_ws, size_t ws_size, hipStream_t stream) {
  const u16* x = (const u16*)d_in[0];
  const u16* logA = (const u16*)d_in[1];
  const u16* Aim = (const u16*)d_in[2];
  const u16* Cre = (const u16*)d_in[3];
  const u16* Cim = (const u16*)d_in[4];
  const u16* logdt = (const u16*)d_in[5];
  const u16* dskip = (const u16*)d_in[6];
  const u16* wg = (const u16*)d_in[8];
  const u16* wu = (const u16*)d_in[9];
  const u16* wd = (const u16*)d_in[10];
  char* ws = (char*)d_ws;
  (void)hipFuncSetAttribute((const void*)k2_kernel, hipFuncAttributeMaxDynamicSharedMemorySize, K2_LDS);
  (void)hipFuncSetAttribute((const void*)k3_kernel, hipFuncAttributeMaxDynamicSharedMemorySize, K3_LDS);
  k1_kernel<<<K1_LN_BLOCKS + K1_WT_BLOCKS + K1_KG_BLOCKS, 256, 0, stream>>>(x, logA, Aim, Cre, Cim, logdt, wg, wu, wd, ws);
  k2_kernel<<<256, 512, K2_LDS, stream>>>(ws);
  k3_kernel<<<512, 512, K3_LDS, stream>>>(x, dskip, ws, (u16*)d_out);
}
```

```cpp
#include <hip/hip_runtime.h>
#include <hip/hip_bf16.h>
#define WT_STORES
#define CV_PRIO 1
#define G2_PRIO 3

typedef unsigned short u16;
typedef unsigned int u32;
using bf16x8 = __attribute__((ext_vector_type(8))) short;
using f16x8 = __attribute__((ext_vector_type(8))) _Float16;
using f16x4 = __attribute__((ext_vector_type(4))) _Float16;
using f32x4 = __attribute__((ext_vector_type(4))) float;
using f32x16 = __attribute__((ext_vector_type(16))) float;
using u32x4 = __attribute__((ext_vector_type(4))) unsigned int;
using u32x2 = __attribute__((ext_vector_type(2))) unsigned int;
typedef float f2 __attribute__((ext_vector_type(2)));
typedef __attribute__((address_space(1))) unsigned gu32;

constexpr int L_ = 2048, D_ = 512, N_ = 128, F_ = 1792;
constexpr int KC_STRIDE = 648;

constexpr size_t WS_CNT = 0;
constexpr size_t WS_XNT = 4096;
constexpr size_t WS_XNTR = WS_XNT + (size_t)L_ * D_ * 2;
constexpr size_t WS_KC = WS_XNTR + (size_t)L_ * D_ * 2;
constexpr size_t WS_H = WS_KC + (size_t)D_ * KC_STRIDE * 4;
constexpr size_t WS_WG = WS_H + (size_t)L_ * F_ * 2;
constexpr size_t WS_WU = WS_WG + (size_t)D_ * F_ * 2;
constexpr size_t WS_WD = WS_WU + (size_t)D_ * F_ * 2;
constexpr size_t WS_AT = WS_WD + (size_t)D_ * F_ * 2;

#define DIAG_START(k)
#define DIAG_STAMP(i)
#define DIAG_END(wsp)
#define DIAG_STAMP2(i)

__device__ __forceinline__ float bf2f(u16 v) { return __uint_as_float(((u32)v) << 16); }
__device__ __forceinline__ u16 f2bf(float f) {
  __bf16 b = (__bf16)f;
  return __builtin_bit_cast(u16, b);
}
__device__ __forceinline__ float rbf(float f) { return bf2f(f2bf(f)); }
__device__ __forceinline__ u32 pack2(float lo, float hi) {
  typedef __bf16 b2 __attribute__((ext_vector_type(2)));
  b2 r = __builtin_convertvector(f2{lo, hi}, b2);
  return __builtin_bit_cast(u32, r);
}
__device__ __forceinline__ f2 unpack2(u32 w) { return f2{__uint_as_float(w << 16), __uint_as_float(w & 0xffff0000u)}; }

__device__ __forceinline__ void st16_wt(void* p, u32x4 v) { asm volatile("global_store_dwordx4 %0, %1, off sc1\n\ts_nop 1" ::"v"(p), "v"(v) : "memory"); }
__device__ __forceinline__ void st16(void* p, u32x4 v) { st16_wt(p, v); }
__device__ __forceinline__ void st8(void* p, u32x2 v) { asm volatile("global_store_dwordx2 %0, %1, off sc1\n\ts_nop 1" ::"v"(p), "v"(v) : "memory"); }

#define DPP_F(v, ctrl) __builtin_bit_cast(float, __builtin_amdgcn_update_dpp(0, __builtin_bit_cast(int, v), ctrl, 0xf, 0xf, true))
__device__ __forceinline__ float wave_sum(float v) {
  v += DPP_F(v, 0xB1);
  v += DPP_F(v, 0x4E);
  v += DPP_F(v, 0x141);
  v += DPP_F(v, 0x140);
  const int iv = __builtin_bit_cast(int, v);
  return (__builtin_bit_cast(float, __builtin_amdgcn_readlane(iv, 0)) + __builtin_bit_cast(float, __builtin_amdgcn_readlane(iv, 16))) +
         (__builtin_bit_cast(float, __builtin_amdgcn_readlane(iv, 32)) + __builtin_bit_cast(float, __builtin_amdgcn_readlane(iv, 48)));
}
__device__ __forceinline__ float wave_max(float v) {
  v = fmaxf(v, DPP_F(v, 0xB1));
  v = fmaxf(v, DPP_F(v, 0x4E));
  v = fmaxf(v, DPP_F(v, 0x141));
  v = fmaxf(v, DPP_F(v, 0x140));
  const int iv = __builtin_bit_cast(int, v);
  return fmaxf(fmaxf(__builtin_bit_cast(float, __builtin_amdgcn_readlane(iv, 0)), __builtin_bit_cast(float, __builtin_amdgcn_readlane(iv, 16))),
               fmaxf(__builtin_bit_cast(float, __builtin_amdgcn_readlane(iv, 32)), __builtin_bit_cast(float, __builtin_amdgcn_readlane(iv, 48))));
}

constexpr int K1_LN_BLOCKS = 128;
constexpr int K1_WT_BLOCKS = 336;
constexpr int K1_KG_BLOCKS = 512;

__device__ __forceinline__ void cmul6(f2& r0, f2& r1, f2& r2, f2& r3, f2& r4, f2& r5, f2 a0, f2 b0, f2 a1, f2 b1,
                                      f2 a2, f2 b2, f2 a3, f2 b3, f2 a4, f2 b4, f2 a5, f2 b5) {
  asm("v_pk_mul_f32 %0, %6, %7 op_sel:[1,1] op_sel_hi:[1,0] neg_lo:[0,1]\n\t"
      "v_pk_mul_f32 %1, %8, %9 op_sel:[1,1] op_sel_hi:[1,0] neg_lo:[0,1]\n\t"
      "v_pk_mul_f32 %2, %10, %11 op_sel:[1,1] op_sel_hi:[1,0] neg_lo:[0,1]\n\t"
      "v_pk_mul_f32 %3, %12, %13 op_sel:[1,1] op_sel_hi:[1,0] neg_lo:[0,1]\n\t"
      "v_pk_mul_f32 %4, %14, %15 op_sel:[1,1] op_sel_hi:[1,0] neg_lo:[0,1]\n\t"
      "v_pk_mul_f32 %5, %16, %17 op_sel:[1,1] op_sel_hi:[1,0] neg_lo:[0,1]\n\t"
      "v_pk_fma_f32 %0, %6, %7, %0 op_sel:[0,0,0] op_sel_hi:[0,1,1]\n\t"
      "v_pk_fma_f32 %1, %8, %9, %1 op_sel:[0,0,0] op_sel_hi:[0,1,1]\n\t"
      "v_pk_fma_f32 %2, %10, %11, %2 op_sel:[0,0,0] op_sel_hi:[0,1,1]\n\t"
      "v_pk_fma_f32 %3, %12, %13, %3 op_sel:[0,0,0] op_sel_hi:[0,1,1]\n\t"
      "v_pk_fma_f32 %4, %14, %15, %4 op_sel:[0,0,0] op_sel_hi:[0,1,1]\n\t"
      "v_pk_fma_f32 %5, %16, %17, %5 op_sel:[0,0,0] op_sel_hi:[0,1,1]\n\t"
      "s_nop 1"
      : "=&v"(r0), "=&v"(r1), "=&v"(r2), "=&v"(r3), "=&v"(r4), "=&v"(r5)
      : "v"(a0), "v"(b0), "v"(a1), "v"(b1), "v"(a2), "v"(b2), "v"(a3), "v"(b3), "v"(a4), "v"(b4), "v"(a5), "v"(b5));
}
__device__ __forceinline__ void cmul2(f2& r0, f2& r1, f2 a0, f2 b0, f2 a1, f2 b1) {
  asm("v_pk_mul_f32 %0, %2, %3 op_sel:[1,1] op_sel_hi:[1,0] neg_lo:[0,1]\n\t"
      "v_pk_mul_f32 %1, %4, %5 op_sel:[1,1] op_sel_hi:[1,0] neg_lo:[0,1]\n\t"
      "s_nop 0\n\t"
      "v_pk_fma_f32 %0, %2, %3, %0 op_sel:[0,0,0] op_sel_hi:[0,1,1]\n\t"
      "v_pk_fma_f32 %1, %4, %5, %1 op_sel:[0,0,0] op_sel_hi:[0,1,1]\n\t"
      "s_nop 1"
      : "=&v"(r0), "=&v"(r1)
      : "v"(a0), "v"(b0), "v"(a1), "v"(b1));
}
__device__ __forceinline__ f2 cexp_(float zr, float zi, float t) {
  const float m = __builtin_amdgcn_exp2f((zr * t) * 1.44269504088896341f);
  float r = (zi * t) * 0.15915494309189535f;
  r -= rintf(r);
  return f2{m * __builtin_amdgcn_cosf(r), m * __builtin_amdgcn_sinf(r)};
}

__global__ __launch_bounds__(256) void k1_kernel(const u16* __restrict__ x, const u16* __restrict__ logA,
                                                 const u16* __restrict__ Aim, const u16* __restrict__ Cre,
                                                 const u16* __restrict__ Cim, const u16* __restrict__ logdt,
                                                 const u16* __restrict__ wg, const u16* __restrict__ wu,
                                                 const u16* __restrict__ wd, char* __restrict__ ws) {
  __shared__ __attribute__((aligned(16))) char smem[18432];
  const int tid = threadIdx.x, lane = tid & 63, wid = tid >> 6;
  DIAG_START(0)
  int bid = blockIdx.x - K1_KG_BLOCKS;
  if (bid >= 0 && bid < K1_LN_BLOCKS) {
    __builtin_amdgcn_s_setprio(3);
    if (bid == 0 && tid < 16) *(u32*)(ws + WS_CNT + tid * 256) = 0u;
#pragma unroll
    for (int it = 0; it < 4; ++it) st16(ws + WS_AT + ((size_t)bid * 1024 + it * 256 + tid) * 16, u32x4{0xffffffffu, 0xffffffffu, 0xffffffffu, 0xffffffffu});
    u16* tile = (u16*)smem;
    u16* xnt = (u16*)(ws + WS_XNT);
    const int r0 = bid * 16;
    u32x4 v[4];
#pragma unroll
    for (int i = 0; i < 4; ++i) v[i] = *(const u32x4*)(x + (size_t)(r0 + wid * 4 + i) * D_ + lane * 8);
#pragma unroll
    for (int i = 0; i < 4; ++i) {
      const int rl = wid * 4 + i;
      f2 f[4];
#pragma unroll
      for (int j = 0; j < 4; ++j) f[j] = unpack2(v[i][j]);
      const f2 s2 = (f[0] + f[1]) + (f[2] + f[3]);
      const float mean = wave_sum(s2.x + s2.y) * (1.f / D_);
      f2 q2 = {0.f, 0.f};
#pragma unroll
      for (int j = 0; j < 4; ++j) { const f2 t = f[j] - mean; q2 = __builtin_elementwise_fma(t, t, q2); }
      const float q = wave_sum(q2.x + q2.y);
      const float mb = rbf(mean);
      const float vb = rbf(q * (1.f / D_));
      const float sd = rbf(sqrtf(rbf(vb + 1e-5f)));
      const float rs = __builtin_amdgcn_rcpf(sd);
      u32x4 o;
#pragma unroll
      for (int j = 0; j < 4; ++j) {
        const f2 t = f[j] - mb;
        const f2 a = unpack2(pack2(t.x, t.y));
        f2 qq = a * rs;
        const f2 rem = __builtin_elementwise_fma(-qq, f2{sd, sd}, a);
        qq = __builtin_elementwise_fma(rem, f2{rs, rs}, qq);
        o[j] = pack2(qq.x, qq.y);
      }
      *(u32x4*)(tile + rl * 520 + lane * 8) = o;
    }
    DIAG_STAMP(1);
    __syncthreads();
#pragma unroll
    for (int it = 0; it < 4; ++it) {
      const int c = tid + it * 256;
      const u32x4 o = *(const u32x4*)(tile + (c & 15) * 520 + (c >> 4) * 8);
      st16(xnt + ((size_t)bid * 1024 + c) * 8, o);
    }
    u16* xtr = (u16*)(ws + WS_XNTR);
#pragma unroll
    for (int it = 0; it < 4; ++it) {
      const int c = tid + it * 256;
      const int d = c >> 1, g = c & 1;
      u32x4 o;
#pragma unroll
      for (int j = 0; j < 4; ++j)
        o[j] = (u32)tile[(g * 8 + 2 * j) * 520 + d] | ((u32)tile[(g * 8 + 2 * j + 1) * 520 + d] << 16);
      st16(xtr + ((size_t)bid * 1024 + c) * 8, o);
    }
    DIAG_END(ws);
    return;
  }
  bid -= K1_LN_BLOCKS;
  if (bid >= 0) {
    __builtin_amdgcn_s_setprio(2);
    int task = bid * 4 + wid;
    const u16* W; u16* Wt; int Kd, Nd;
    if (task < 448) { W = wg; Wt = (u16*)(ws + WS_WG); Kd = D_; Nd = F_; }
    else if (task < 896) { task -= 448; W = wu; Wt = (u16*)(ws + WS_WU); Kd = D_; Nd = F_; }
    else { task -= 896; W = wd; Wt = (u16*)(ws + WS_WD); Kd = F_; Nd = D_; }
    const int nks = Kd / 32;
    const int ks = task % nks, nb = task / nks;
    const int col = nb * 64 + lane;
    u16 v[32];
#pragma unroll
    for (int k = 0; k < 32; ++k) v[k] = W[(size_t)(ks * 32 + k) * Nd + col];
#pragma unroll
    for (int q = 0; q < 4; ++q) {
      u32x4 o;
#pragma unroll
      for (int j = 0; j < 4; ++j) o[j] = (u32)v[q * 8 + 2 * j] | ((u32)v[q * 8 + 2 * j + 1] << 16);
      st16(Wt + ((size_t)((col >> 4) * nks + ks) * 64 + q * 16 + (col & 15)) * 8, o);
    }
    DIAG_END(ws);
    return;
  }
  bid = blockIdx.x;
  {
    const int d = bid;
    float4* prm = (float4*)(smem + 16384);
    if (tid < 128) {
      const u16 la = logA[d * N_ + tid], ai = Aim[d * N_ + tid], cr = Cre[d * N_ + tid], ci = Cim[d * N_ + tid];
      const float dt = rbf(expf(bf2f(logdt[d])));
      const float ar = -rbf(expf(bf2f(la)));
      prm[tid] = make_float4(ar * dt, bf2f(ai) * dt, bf2f(cr) * dt, bf2f(ci) * dt);
    }
    __syncthreads();
    DIAG_STAMP(1);
    const int kq = lane >> 4, idx = lane & 15;
    f32x4 acc[4] = {};
    const float ty = (float)idx, te = (float)(idx == 0 ? 256 : 16 * idx);
    float4 pr[8];
#pragma unroll
    for (int s = 0; s < 8; ++s) pr[s] = prm[wid * 32 + s * 4 + kq];
#pragma unroll
    for (int s = 0; s < 8; s += 2) {
      const float4 pa = pr[s], pb = pr[s + 1];
      const f2 Ca = f2{pa.z, -pa.w}, Cb = f2{pb.z, -pb.w};
      f2 Ya[4], Yb[4], Xa[4], Xb[4];
      Ya[0] = cexp_(pa.x, pa.y, ty); Yb[0] = cexp_(pb.x, pb.y, ty);
      f2 Ea = cexp_(pa.x, -pa.y, te), Eb = cexp_(pb.x, -pb.y, te);
      f2 Fa = (idx == 0) ? f2{1.f, 0.f} : Ea, Fb = (idx == 0) ? f2{1.f, 0.f} : Eb;
#pragma unroll
      for (int e = 0; e < 3; ++e) {
        f2 nEa, nEb;
        cmul6(Xa[e], Xb[e], Ya[e + 1], Yb[e + 1], nEa, nEb, Ca, Fa, Cb, Fb, Ya[e], Ya[e], Yb[e], Yb[e], Ea, Ea, Eb, Eb);
        Ea = nEa; Eb = nEb; Fa = nEa; Fb = nEb;
      }
      cmul2(Xa[3], Xb[3], Ca, Ea, Cb, Eb);
#pragma unroll
      for (int e = 0; e < 4; ++e) acc[e] = __builtin_amdgcn_mfma_f32_16x16x4f32(Xa[e].x, Ya[e].x, acc[e], 0, 0, 0);
#pragma unroll
      for (int e = 0; e < 4; ++e) acc[e] = __builtin_amdgcn_mfma_f32_16x16x4f32(Xa[e].y, Ya[e].y, acc[e], 0, 0, 0);
#pragma unroll
      for (int e = 0; e < 4; ++e) acc[e] = __builtin_amdgcn_mfma_f32_16x16x4f32(Xb[e].x, Yb[e].x, acc[e], 0, 0, 0);
#pragma unroll
      for (int e = 0; e < 4; ++e) acc[e] = __builtin_amdgcn_mfma_f32_16x16x4f32(Xb[e].y, Yb[e].y, acc[e], 0, 0, 0);
    }
    DIAG_STAMP(2);
    float* red = (float*)smem;
#pragma unroll
    for (int e = 0; e < 4; ++e)
#pragma unroll
      for (int r = 0; r < 4; ++r) red[(wid * 4 + e) * 256 + ((lane >> 4) * 4 + r) * 16 + (lane & 15)] = acc[e][r];
    __syncthreads();
    float* kc = (float*)(ws + WS_KC) + (size_t)d * KC_STRIDE;
#pragma unroll
    for (int e = 0; e < 4; ++e) {
      const float v = 2.f * ((red[(0 * 4 + e) * 256 + tid] + red[(1 * 4 + e) * 256 + tid]) +
                             (red[(2 * 4 + e) * 256 + tid] + red[(3 * 4 + e) * 256 + tid]));
      if (e == 0) kc[tid] = v;
      else if (tid >= 128) kc[128 * e + tid] = v;
      else if (e == 3 && tid == 0) kc[640] = v;
    }
    DIAG_END(ws);
  }
}

constexpr int G1_STAGE = 22 * 1024;
constexpr int G1_NBUF = 7;
constexpr int K2_LDS = G1_STAGE * G1_NBUF;

constexpr int UC_LEN = 2080;
constexpr int KR_LEN = 3840;
constexpr int CV_UCP = 0;
constexpr int CV_KR = CV_UCP + 8 * UC_LEN * 2;
constexpr int CV_URAW = CV_KR + KR_LEN * 2;
constexpr int CV_KRAW = CV_URAW + 4224;
constexpr int CV_MAX = CV_KRAW + 2592;
constexpr int CV_CH_STRIDE = 48128;
constexpr int H_STAGE = 2 * CV_CH_STRIDE;

__device__ __forceinline__ f2 gelu2(f2 g) {
  const float c0 = -2.f * 0.7978845608028654f * 1.44269504088896341f;
  const float c1 = c0 * 0.044715f;
  const f2 t = g * __builtin_elementwise_fma(g * g, f2{c1, c1}, f2{c0, c0});
  const f2 dn = f2{__builtin_amdgcn_exp2f(t.x), __builtin_amdgcn_exp2f(t.y)} + 1.f;
  return g * f2{__builtin_amdgcn_rcpf(dn.x), __builtin_amdgcn_rcpf(dn.y)};
}
__device__ __forceinline__ void glds16(const void* g, void* l) {
  __builtin_amdgcn_global_load_lds((const __attribute__((address_space(1))) u32*)g,
                                   (__attribute__((address_space(3))) u32*)l, 16, 0, 0);
}
__device__ __forceinline__ u32 bf2_to_h2(u32 w) {
  auto r = __builtin_amdgcn_cvt_pkrtz(__uint_as_float(w << 16), __uint_as_float(w & 0xffff0000u));
  return __builtin_bit_cast(u32, r);
}
#define RAW_BAR() do { asm volatile("s_waitcnt lgkmcnt(0)" ::: "memory"); __builtin_amdgcn_s_barrier(); } while (0)

__global__ __launch_bounds__(512, 2) void k2_kernel(const u16* __restrict__ x, const u16* __restrict__ dskip,
                                                    char* __restrict__ ws, u16* __restrict__ out) {
  extern __shared__ __attribute__((aligned(16))) char smem[];
  const int tid = threadIdx.x, lane = tid & 63;
  const int wid = __builtin_amdgcn_readfirstlane(tid >> 6);
  DIAG_START(1)
  const int bid = blockIdx.x;
  const int xcd = bid & 7, loc = bid >> 3;
  const int ch = wid >> 2, w4 = wid & 3;
  const int d = bid * 2 + ch;
  const int t2 = tid & 255;
  const int mt = (xcd >> 1) * 4 + (loc & 3);
  const int nt = (xcd & 1) * 8 + (loc >> 2);

  u32x4 uv0; float kv0, kv1, kv2; u32 pds;
  {
    const u16* xnt = (const u16*)(ws + WS_XNT);
    const u16* wgt = (const u16*)(ws + WS_WG);
    const u16* wut = (const u16*)(ws + WS_WU);
    auto src = [&](int f) -> const u16* {
      if (f < 8) return xnt + (size_t)(mt * 8 + f) * 16 * 512;
      const int s = f - 8;
      return ((s & 1) ? wut : wgt) + (size_t)(nt * 7 + (s >> 1)) * 16 * 512;
    };
    const u16* s0 = src(wid) + lane * 8;
    const u16* s1 = src(wid + 8) + lane * 8;
    const u16* s2 = src(wid < 6 ? wid + 16 : 21) + lane * 8;
    auto stage = [&](int ks) {
      char* base = smem + (ks % G1_NBUF) * G1_STAGE + wid * 1024 + lane * 16;
      glds16(s0 + ks * 512, base);
      glds16(s1 + ks * 512, base + 8192);
      if (wid < 6) glds16(s2 + ks * 512, base + 16384);
    };
#define G1_WAIT(n) do { if (wid < 6) asm volatile("s_waitcnt vmcnt(%0)" ::"n"(3 * (n)) : "memory"); \
                        else asm volatile("s_waitcnt vmcnt(%0)" ::"n"(2 * (n)) : "memory"); } while (0)
#pragma unroll
    for (int k = 0; k < G1_NBUF; ++k) stage(k);
    const int wm = wid >> 1, wn = wid & 1;
    f32x4 acc[7][2] = {};
    bf16x8 xa[2], wa[7], xb[2], wb[7];
    G1_WAIT(G1_NBUF - 1);
    RAW_BAR();
    DIAG_STAMP(1);
    {
      const char* base = smem + lane * 16;
#pragma unroll
      for (int i = 0; i < 2; ++i) xa[i] = *(const bf16x8*)(base + (wm * 2 + i) * 1024);
#pragma unroll
      for (int j = 0; j < 7; ++j) wa[j] = *(const bf16x8*)(base + (8 + wn * 7 + j) * 1024);
    }
#define G1_ITER(k, XC, WC, XN, WN, NLEFT)                                                          \
  do {                                                                                             \
    G1_WAIT(NLEFT);                                                                                \
    RAW_BAR();                                                                                     \
    const char* rb_ = smem + (((k) + 1) % G1_NBUF) * G1_STAGE + lane * 16;                         \
    _Pragma("unroll") for (int j = 0; j < 7; ++j) {                                                \
      acc[j][0] = __builtin_amdgcn_mfma_f32_16x16x32_bf16(WC[j], XC[0], acc[j][0], 0, 0, 0);       \
      if (j < 2) XN[j] = *(const bf16x8*)(rb_ + (wm * 2 + j) * 1024);                              \
      acc[j][1] = __builtin_amdgcn_mfma_f32_16x16x32_bf16(WC[j], XC[1], acc[j][1], 0, 0, 0);       \
      WN[j] = *(const bf16x8*)(rb_ + (8 + wn * 7 + j) * 1024);                                     \
      __builtin_amdgcn_sched_barrier(0);                                                           \
      if (j == 3 && (k) + G1_NBUF < 16) { stage((k) + G1_NBUF); __builtin_amdgcn_sched_barrier(0); } \
    }                                                                                              \
  } while (0)
    G1_ITER(0, xa, wa, xb, wb, 5);
    G1_ITER(1, xb, wb, xa, wa, 5);
    G1_ITER(2, xa, wa, xb, wb, 5);
    G1_ITER(3, xb, wb, xa, wa, 5);
    G1_ITER(4, xa, wa, xb, wb, 5);
    G1_ITER(5, xb, wb, xa, wa, 5);
    G1_ITER(6, xa, wa, xb, wb, 5);
    G1_ITER(7, xb, wb, xa, wa, 5);
    G1_ITER(8, xa, wa, xb, wb, 5);
    G1_ITER(9, xb, wb, xa, wa, 5);
    G1_ITER(10, xa, wa, xb, wb, 4);
    G1_ITER(11, xb, wb, xa, wa, 3);
    G1_ITER(12, xa, wa, xb, wb, 2);
    G1_ITER(13, xb, wb, xa, wa, 1);
    G1_ITER(14, xa, wa, xb, wb, 0);
    {
      const u16* ug = (const u16*)(ws + WS_XNTR) + (size_t)(t2 >> 1) * 8192 + d * 16 + (t2 & 1) * 8;
      const float* kc = (const float*)(ws + WS_KC) + (size_t)d * KC_STRIDE;
      uv0 = *(const u32x4*)ug;
      kv0 = kc[t2]; kv1 = kc[t2 + 256]; kv2 = (t2 < 129) ? kc[t2 + 512] : 0.f;
      pds = dskip[d];
    }
    __builtin_amdgcn_sched_barrier(0);
#pragma unroll
    for (int j = 0; j < 7; ++j)
#pragma unroll
      for (int i = 0; i < 2; ++i) acc[j][i] = __builtin_amdgcn_mfma_f32_16x16x32_bf16(wb[j], xb[i], acc[j][i], 0, 0, 0);
    DIAG_STAMP(2);
    RAW_BAR();
    f32x4* ex = (f32x4*)(smem + H_STAGE + 28672);
    if (wn == 0) {
#pragma unroll
      for (int i = 0; i < 2; ++i) ex[(wm * 2 + i) * 64 + lane] = acc[6][i];
    }
    char* hs = smem + H_STAGE;
    auto emit = [&](int t, const f32x4& g, const f32x4& u, int i) {
      u32x2 o;
#pragma unroll
      for (int p = 0; p < 2; ++p) {
        const f2 gb = unpack2(pack2(g[2 * p], g[2 * p + 1]));
        const f2 ub = unpack2(pack2(u[2 * p], u[2 * p + 1]));
        const f2 ge = gelu2(gb);
        const f2 hv = unpack2(pack2(ge.x, ge.y)) * ub;
        o[p] = pack2(hv.x, hv.y);
      }
      *(u32x2*)(hs + (((wm * 2 + i) * 7 + t) * 32 + (lane >> 5) * 16 + (lane & 15)) * 16 + ((lane >> 4) & 1) * 8) = o;
    };
    if (wn == 0) {
#pragma unroll
      for (int t = 0; t < 3; ++t)
#pragma unroll
        for (int i = 0; i < 2; ++i) emit(t, acc[2 * t][i], acc[2 * t + 1][i], i);
      RAW_BAR();
    } else {
#pragma unroll
      for (int t = 4; t < 7; ++t)
#pragma unroll
        for (int i = 0; i < 2; ++i) emit(t, acc[2 * t - 7][i], acc[2 * t - 6][i], i);
      RAW_BAR();
#pragma unroll
      for (int i = 0; i < 2; ++i) emit(3, ex[(wm * 2 + i) * 64 + lane], acc[0][i], i);
    }
    {
      char* cbase = smem + ch * CV_CH_STRIDE;
      u32x4* uraw = (u32x4*)(cbase + CV_URAW);
      float* kraw = (float*)(cbase + CV_KRAW);
      float* kmax = (float*)(cbase + CV_MAX);
      if (t2 < 4) { uraw[t2] = u32x4{0, 0, 0, 0}; uraw[260 + t2] = u32x4{0, 0, 0, 0}; }
      u32x4 h;
#pragma unroll
      for (int j = 0; j < 4; ++j) h[j] = bf2_to_h2(uv0[j]);
      uraw[4 + t2] = h;
      kraw[t2] = kv0;
      kraw[t2 + 256] = kv1;
      if (t2 < 136) kraw[t2 + 512] = kv2;
      const float mx = wave_max(fmaxf(fmaxf(fabsf(kv0), fabsf(kv1)), fabsf(kv2)));
      if (lane == 0) kmax[w4] = mx;
    }
    RAW_BAR();
    u16* hT = (u16*)(ws + WS_H);
#pragma unroll
    for (int it = 0; it < 4; ++it) {
      const int c = tid + it * 512;
      if (c < 1792) {
        const int ml = c / 224, rem = c - ml * 224;
        st16_wt(hT + ((size_t)(mt * 8 + ml) * 56 * 64 + (size_t)nt * 7 * 32 + rem) * 8, *(const u32x4*)(hs + c * 16));
      }
    }
  }
  DIAG_STAMP(3);

  const int mt2 = (xcd >> 1) * 8 + (xcd & 1) * 4 + (loc & 3);
  const int nt2 = loc >> 2;
  const int vo = lane * 16;
  const __amdgpu_buffer_rsrc_t hr = __builtin_amdgcn_make_buffer_rsrc((void*)((const u16*)(ws + WS_H) + ((size_t)(mt2 * 4) * 56 + wid * 7) * 512), 0, 4 * 56 * 1024, 0x00020000);
  const __amdgpu_buffer_rsrc_t wr = __builtin_amdgcn_make_buffer_rsrc((void*)((const u16*)(ws + WS_WD) + ((size_t)(nt2 * 4) * 56 + wid * 7) * 512), 0, 4 * 56 * 1024, 0x00020000);
#define G2_LD(rs, i, k, aux) __builtin_bit_cast(bf16x8, __builtin_amdgcn_raw_buffer_load_b128(rs, vo, ((i) * 56 + (k)) * 1024, aux))
  bf16x8 wd[7][4];

  {
    char* cbase = smem + ch * CV_CH_STRIDE;
    u16* ucp = (u16*)(cbase + CV_UCP);
    u16* kr = (u16*)(cbase + CV_KR);
    const u32x4* uraw = (const u32x4*)(cbase + CV_URAW);
    const float* kraw = (const float*)(cbase + CV_KRAW);
    const float* kmax = (const float*)(cbase + CV_MAX);
    const float dsk = bf2f((u16)pds);
    float kscale, kinv;
    {
      const float mx = fmaxf(fmaxf(kmax[0], kmax[1]), fmaxf(kmax[2], kmax[3]));
      int e = (int)((__float_as_uint(mx) >> 23) & 0xff);
      e = min(max(e, 20), 240);
      kscale = __uint_as_float((u32)(127 + 13 + 127 - e) << 23);
      kinv = __uint_as_float((u32)(e - 13) << 23);
    }
#pragma unroll
    for (int it = 0; it < 2; ++it) {
      const int tb = it ? 256 + (t2 & 3) : t2;
      u32 w[8];
      u32x4 a = uraw[tb], b = uraw[tb + 1];
#pragma unroll
      for (int j = 0; j < 4; ++j) { w[j] = a[j]; w[4 + j] = b[j]; }
      if (it == 0 || t2 < 4) {
#pragma unroll
        for (int c = 0; c < 8; ++c) {
          u32x4 o;
#pragma unroll
          for (int j = 0; j < 4; ++j)
            o[j] = (c & 1) ? w[j + (c + 1) / 2] : __builtin_amdgcn_alignbit(w[j + c / 2 + 1], w[j + c / 2], 16);
          *(u32x4*)(ucp + c * UC_LEN + tb * 8) = o;
        }
      }
    }
    {
      float kk[8];
#pragma unroll
      for (int j = 0; j < 8; ++j) {
        const int jj = 2047 - (t2 * 8 + j);
        const int cbi = (int)f2bf((float)jj) - 17024;
        kk[j] = kraw[jj < 128 ? jj : cbi] * kscale;
      }
      f16x8 o;
#pragma unroll
      for (int j = 0; j < 8; ++j) o[j] = (_Float16)kk[j];
      *(f16x8*)(kr + t2 * 8 + 8 * (t2 >> 2)) = o;
      if (t2 < 128) { const int tb = t2 + 256; *(u32x4*)(kr + tb * 8 + 8 * (tb >> 2)) = u32x4{0, 0, 0, 0}; }
    }
    asm volatile("s_waitcnt vmcnt(0)" ::: "memory");
    RAW_BAR();
    if (tid == 0) __hip_atomic_fetch_add((gu32*)(ws + WS_CNT + mt * 256), 1u, __ATOMIC_RELAXED, __HIP_MEMORY_SCOPE_AGENT);
    DIAG_STAMP(4);
    const int r = lane & 31, h = lane >> 5;
    const u16* ap = ucp + (r & 7) * UC_LEN + 8 * h + 8 * (r >> 3);
    const int bofs0 = 2048 - 32 * (r + 1) + 8 * h + 8 * (63 - r);
    const int bofs1 = bofs0 - 1024 - 256;
    f32x16 acc0 = {}, acc1 = {};
    {
      const u16* apw = ap + 16 * (w4 * 16);
      const u16* bp0 = kr + bofs0 + 16 * (w4 * 16) + 8 * (w4 * 8);
      const u16* bp1 = kr + bofs1 + 16 * (w4 * 16) + 8 * (w4 * 8);
      auto lda = [&](int s) -> f16x8 { return *(const f16x8*)(apw + 16 * s); };
      auto ldb = [&](const u16* b, int s) -> f16x8 { return *(const f16x8*)(b + 16 * s + 8 * (s >> 1)); };
      f16x8 a0 = lda(0), p0 = ldb(bp0, 0), q0 = ldb(bp1, 0), c0 = lda(64);
      f16x8 a1 = lda(1), p1 = ldb(bp0, 1), q1 = ldb(bp1, 1), c1 = lda(65);
#pragma unroll
      for (int it = 0; it < 8; ++it) {
        f16x8 na0, na1, nc0, nc1, np0, np1, nq0, nq1;
        if (it < 7) {
          const int sn = 2 * it + 2;
          na0 = lda(sn); np0 = ldb(bp0, sn); nq0 = ldb(bp1, sn); nc0 = lda(sn + 64);
          na1 = lda(sn + 1); np1 = ldb(bp0, sn + 1); nq1 = ldb(bp1, sn + 1); nc1 = lda(sn + 65);
#pragma unroll
          for (int i = 0; i < 4; ++i) wd[it][i] = G2_LD(wr, i, it, 0);
        }
        __builtin_amdgcn_sched_barrier(0);
        acc0 = __builtin_amdgcn_mfma_f32_32x32x16_f16(a0, p0, acc0, 0, 0, 0);
        acc1 = __builtin_amdgcn_mfma_f32_32x32x16_f16(a0, q0, acc1, 0, 0, 0);
        acc1 = __builtin_amdgcn_mfma_f32_32x32x16_f16(c0, p0, acc1, 0, 0, 0);
        acc0 = __builtin_amdgcn_mfma_f32_32x32x16_f16(a1, p1, acc0, 0, 0, 0);
        acc1 = __builtin_amdgcn_mfma_f32_32x32x16_f16(a1, q1, acc1, 0, 0, 0);
        acc1 = __builtin_amdgcn_mfma_f32_32x32x16_f16(c1, p1, acc1, 0, 0, 0);
        __builtin_amdgcn_sched_barrier(0);
        if (it < 7) { a0 = na0; a1 = na1; c0 = nc0; c1 = nc1; p0 = np0; p1 = np1; q0 = nq0; q1 = nq1; }
      }
    }
    DIAG_STAMP(5);
    RAW_BAR();
    float* red = (float*)cbase;
#pragma unroll
    for (int g = 0; g < 4; ++g) {
      f32x4 v0 = {acc0[4 * g], acc0[4 * g + 1], acc0[4 * g + 2], acc0[4 * g + 3]};
      f32x4 v1 = {acc1[4 * g], acc1[4 * g + 1], acc1[4 * g + 2], acc1[4 * g + 3]};
      *(f32x4*)(red + (w4 * 32 + r) * 36 + 8 * g + 4 * h) = v0;
      *(f32x4*)(red + ((4 + w4) * 32 + r) * 36 + 8 * g + 4 * h) = v1;
    }
    if (tid == 511) {
      gu32* cnt = (gu32*)(ws + WS_CNT + (mt2 >> 1) * 256);
      for (unsigned spins = 0; spins < (1u << 22); ++spins) {
        if (__hip_atomic_load(cnt, __ATOMIC_RELAXED, __HIP_MEMORY_SCOPE_AGENT) >= 16u) break;
        __builtin_amdgcn_s_sleep(1);
      }
    }
    RAW_BAR();
    DIAG_STAMP2(1);
    u16* at = (u16*)(ws + WS_AT) + (size_t)d * L_;
    {
      const int c = t2;
      const int T = c >> 7, a = (c >> 2) & 31, b0 = (c & 3) * 8;
      f32x4 s0 = {}, s1 = {};
      const float* p = red + (T * 4 * 32 + a) * 36 + b0;
#pragma unroll
      for (int w = 0; w < 4; ++w) { s0 += *(const f32x4*)(p + w * 32 * 36); s1 += *(const f32x4*)(p + w * 32 * 36 + 4); }
      u32x4 o;
#pragma unroll
      for (int j = 0; j < 4; ++j) {
        const f2 y = unpack2((j < 2) ? pack2(s0[2 * j] * kinv, s0[2 * j + 1] * kinv) : pack2(s1[2 * j - 4] * kinv, s1[2 * j - 3] * kinv));
        const f2 us = unpack2(uv0[j]) * dsk;
        const f2 t = unpack2(pack2(us.x, us.y)) + y;
        u32 ov = pack2(t.x, t.y);
        if ((ov & 0xffffu) == 0xffffu) ov &= 0xffffffc0u;
        if ((ov >> 16) == 0xffffu) ov &= 0xffc0ffffu;
        o[j] = ov;
      }
      st16_wt(at + c * 8, o);
    }
  }
  DIAG_STAMP2(2);

  {
    f32x4 acc[4][4] = {};
    bf16x8 hb[4][4];
#pragma unroll
    for (int k = 0; k < 4; ++k)
#pragma unroll
      for (int i = 0; i < 4; ++i) hb[k][i] = G2_LD(hr, i, k, 16);
    const int m = tid >> 3, n0 = (tid & 7) * 8;
    const size_t go = (size_t)(mt2 * 64 + m) * D_ + nt2 * 64 + n0;
    const u32x4 xv = *(const u32x4*)(x + go);
    __builtin_amdgcn_sched_barrier(0);
#pragma unroll
    for (int k = 0; k < 7; ++k) {
#pragma unroll
      for (int j = 0; j < 4; ++j)
#pragma unroll
        for (int i = 0; i < 4; ++i) acc[j][i] = __builtin_amdgcn_mfma_f32_16x16x32_bf16(wd[k][j], hb[k & 3][i], acc[j][i], 0, 0, 0);
      if (k < 3) {
#pragma unroll
        for (int i = 0; i < 4; ++i) hb[k][i] = G2_LD(hr, i, k + 4, 16);
      }
      __builtin_amdgcn_sched_barrier(0);
    }
    DIAG_STAMP2(3);
    RAW_BAR();
    float* red = (float*)smem;
    float* rp = red + (wid * 64 + (lane & 15)) * 68 + 4 * (lane >> 4);
#pragma unroll
    for (int j = 0; j < 4; ++j)
#pragma unroll
      for (int i = 0; i < 4; ++i) *(f32x4*)(rp + i * 16 * 68 + j * 16) = acc[j][i];
    u32 a0, a1, a2, a3, a4, a5, a6, a7;
    {
      const u16* at = (const u16*)(ws + WS_AT) + (size_t)(nt2 * 64 + n0) * L_ + mt2 * 64 + m;
      for (unsigned spins = 0; spins < (1u << 20); ++spins) {
        asm volatile(
            "global_load_ushort %0, %8, off sc1\n\t"
            "global_load_ushort %1, %9, off sc1\n\t"
            "global_load_ushort %2, %10, off sc1\n\t"
            "global_load_ushort %3, %11, off sc1\n\t"
            "global_load_ushort %4, %12, off sc1\n\t"
            "global_load_ushort %5, %13, off sc1\n\t"
            "global_load_ushort %6, %14, off sc1\n\t"
            "global_load_ushort %7, %15, off sc1\n\t"
            "s_waitcnt vmcnt(0)"
            : "=&v"(a0), "=&v"(a1), "=&v"(a2), "=&v"(a3), "=&v"(a4), "=&v"(a5), "=&v"(a6), "=&v"(a7)
            : "v"(at), "v"(at + L_), "v"(at + 2 * L_), "v"(at + 3 * L_), "v"(at + 4 * L_), "v"(at + 5 * L_), "v"(at + 6 * L_), "v"(at + 7 * L_)
            : "memory");
        const bool pend = (a0 == 0xffffu) | (a1 == 0xffffu) | (a2 == 0xffffu) | (a3 == 0xffffu) | (a4 == 0xffffu) |
                          (a5 == 0xffffu) | (a6 == 0xffffu) | (a7 == 0xffffu);
        if (!__any(pend)) break;
        __builtin_amdgcn_s_sleep(2);
      }
    }
    RAW_BAR();
    DIAG_STAMP2(4);
    f32x4 p0 = {}, p1 = {};
#pragma unroll
    for (int w = 0; w < 8; ++w) {
      p0 += *(const f32x4*)(red + (w * 64 + m) * 68 + n0);
      p1 += *(const f32x4*)(red + (w * 64 + m) * 68 + n0 + 4);
    }
    const u32 av[8] = {a0, a1, a2, a3, a4, a5, a6, a7};
    u32x4 o;
#pragma unroll
    for (int j = 0; j < 4; ++j) {
      const f2 xx = unpack2(xv[j]);
      const f2 aa = f2{__uint_as_float(av[2 * j] << 16), __uint_as_float(av[2 * j + 1] << 16)};
      const f2 r1 = xx + aa;
      const f2 bb = unpack2((j < 2) ? pack2(p0[2 * j], p0[2 * j + 1]) : pack2(p1[2 * j - 4], p1[2 * j - 3]));
      const f2 r2 = unpack2(pack2(r1.x, r1.y)) + bb;
      o[j] = pack2(r2.x, r2.y);
    }
    *(u32x4*)(out + go) = o;
  }
}

extern "C" void kernel_launch(void* const* d_in, const int* in_sizes, int n_in, void* d_out, int out_size,
                              void* d_ws, size_t ws_size, hipStream_t stream) {
  const u16* x = (const u16*)d_in[0];
  const u16* logA = (const u16*)d_in[1];
  const u16* Aim = (const u16*)d_in[2];
  const u16* Cre = (const u16*)d_in[3];
  const u16* Cim = (const u16*)d_in[4];
  const u16* logdt = (const u16*)d_in[5];
  const u16* dskip = (const u16*)d_in[6];
  const u16* wg = (const u16*)d_in[8];
  const u16* wu = (const u16*)d_in[9];
  const u16* wd = (const u16*)d_in[10];
  char* ws = (char*)d_ws;
  (void)hipFuncSetAttribute((const void*)k2_kernel, hipFuncAttributeMaxDynamicSharedMemorySize, K2_LDS);
  k1_kernel<<<K1_LN_BLOCKS + K1_WT_BLOCKS + K1_KG_BLOCKS, 256, 0, stream>>>(x, logA, Aim, Cre, Cim, logdt, wg, wu, wd, ws);
  k2_kernel<<<256, 512, K2_LDS, stream>>>(x, dskip, ws, (u16*)d_out);
}
```

```cpp
#include <hip/hip_runtime.h>
#include <hip/hip_bf16.h>
#define WT_STORES
#define CV_PRIO 1
#define G2_PRIO 3

typedef unsigned short u16;
typedef unsigned int u32;
using bf16x8 = __attribute__((ext_vector_type(8))) short;
using f16x8 = __attribute__((ext_vector_type(8))) _Float16;
using f16x4 = __attribute__((ext_vector_type(4))) _Float16;
using f32x4 = __attribute__((ext_vector_type(4))) float;
using f32x16 = __attribute__((ext_vector_type(16))) float;
using u32x4 = __attribute__((ext_vector_type(4))) unsigned int;
using u32x2 = __attribute__((ext_vector_type(2))) unsigned int;
typedef float f2 __attribute__((ext_vector_type(2)));
typedef __attribute__((address_space(1))) unsigned gu32;

constexpr int L_ = 2048, D_ = 512, N_ = 128, F_ = 1792;
constexpr int KC_STRIDE = 648;

constexpr size_t WS_CNT = 0;
constexpr size_t WS_XNT = 4096;
constexpr size_t WS_XNTR = WS_XNT + (size_t)L_ * D_ * 2;
constexpr size_t WS_KC = WS_XNTR + (size_t)L_ * D_ * 2;
constexpr size_t WS_H = WS_KC + (size_t)D_ * KC_STRIDE * 4;
constexpr size_t WS_WG = WS_H + (size_t)L_ * F_ * 2;
constexpr size_t WS_WU = WS_WG + (size_t)D_ * F_ * 2;
constexpr size_t WS_WD = WS_WU + (size_t)D_ * F_ * 2;
constexpr size_t WS_AT = WS_WD + (size_t)D_ * F_ * 2;

#define DIAG_START(k)
#define DIAG_STAMP(i)
#define DIAG_END(wsp)
#define DIAG_STAMP2(i)

__device__ __forceinline__ float bf2f(u16 v) { return __uint_as_float(((u32)v) << 16); }
__device__ __forceinline__ u16 f2bf(float f) {
  __bf16 b = (__bf16)f;
  return __builtin_bit_cast(u16, b);
}
__device__ __forceinline__ float rbf(float f) { return bf2f(f2bf(f)); }
__device__ __forceinline__ u32 pack2(float lo, float hi) {
  typedef __bf16 b2 __attribute__((ext_vector_type(2)));
  b2 r = __builtin_convertvector(f2{lo, hi}, b2);
  return __builtin_bit_cast(u32, r);
}
__device__ __forceinline__ f2 unpack2(u32 w) { return f2{__uint_as_float(w << 16), __uint_as_float(w & 0xffff0000u)}; }

__device__ __forceinline__ void st16_wt(void* p, u32x4 v) { asm volatile("global_store_dwordx4 %0, %1, off sc1\n\ts_nop 1" ::"v"(p), "v"(v) : "memory"); }
__device__ __forceinline__ void st16(void* p, u32x4 v) { st16_wt(p, v); }
__device__ __forceinline__ void st8(void* p, u32x2 v) { asm volatile("global_store_dwordx2 %0, %1, off sc1\n\ts_nop 1" ::"v"(p), "v"(v) : "memory"); }

#define DPP_F(v, ctrl) __builtin_bit_cast(float, __builtin_amdgcn_update_dpp(0, __builtin_bit_cast(int, v), ctrl, 0xf, 0xf, true))
__device__ __forceinline__ float wave_sum(float v) {
  v += DPP_F(v, 0xB1);
  v += DPP_F(v, 0x4E);
  v += DPP_F(v, 0x141);
  v += DPP_F(v, 0x140);
  const int iv = __builtin_bit_cast(int, v);
  return (__builtin_bit_cast(float, __builtin_amdgcn_readlane(iv, 0)) + __builtin_bit_cast(float, __builtin_amdgcn_readlane(iv, 16))) +
         (__builtin_bit_cast(float, __builtin_amdgcn_readlane(iv, 32)) + __builtin_bit_cast(float, __builtin_amdgcn_readlane(iv, 48)));
}
__device__ __forceinline__ float wave_max(float v) {
  v = fmaxf(v, DPP_F(v, 0xB1));
  v = fmaxf(v, DPP_F(v, 0x4E));
  v = fmaxf(v, DPP_F(v, 0x141));
  v = fmaxf(v, DPP_F(v, 0x140));
  const int iv = __builtin_bit_cast(int, v);
  return fmaxf(fmaxf(__builtin_bit_cast(float, __builtin_amdgcn_readlane(iv, 0)), __builtin_bit_cast(float, __builtin_amdgcn_readlane(iv, 16))),
               fmaxf(__builtin_bit_cast(float, __builtin_amdgcn_readlane(iv, 32)), __builtin_bit_cast(float, __builtin_amdgcn_readlane(iv, 48))));
}

constexpr int K1_LN_BLOCKS = 128;
constexpr int K1_WT_BLOCKS = 336;
constexpr int K1_KG_BLOCKS = 256;

__device__ __forceinline__ void cmul6(f2& r0, f2& r1, f2& r2, f2& r3, f2& r4, f2& r5, f2 a0, f2 b0, f2 a1, f2 b1,
                                      f2 a2, f2 b2, f2 a3, f2 b3, f2 a4, f2 b4, f2 a5, f2 b5) {
  asm("v_pk_mul_f32 %0, %6, %7 op_sel:[1,1] op_sel_hi:[1,0] neg_lo:[0,1]\n\t"
      "v_pk_mul_f32 %1, %8, %9 op_sel:[1,1] op_sel_hi:[1,0] neg_lo:[0,1]\n\t"
      "v_pk_mul_f32 %2, %10, %11 op_sel:[1,1] op_sel_hi:[1,0] neg_lo:[0,1]\n\t"
      "v_pk_mul_f32 %3, %12, %13 op_sel:[1,1] op_sel_hi:[1,0] neg_lo:[0,1]\n\t"
      "v_pk_mul_f32 %4, %14, %15 op_sel:[1,1] op_sel_hi:[1,0] neg_lo:[0,1]\n\t"
      "v_pk_mul_f32 %5, %16, %17 op_sel:[1,1] op_sel_hi:[1,0] neg_lo:[0,1]\n\t"
      "v_pk_fma_f32 %0, %6, %7, %0 op_sel:[0,0,0] op_sel_hi:[0,1,1]\n\t"
      "v_pk_fma_f32 %1, %8, %9, %1 op_sel:[0,0,0] op_sel_hi:[0,1,1]\n\t"
      "v_pk_fma_f32 %2, %10, %11, %2 op_sel:[0,0,0] op_sel_hi:[0,1,1]\n\t"
      "v_pk_fma_f32 %3, %12, %13, %3 op_sel:[0,0,0] op_sel_hi:[0,1,1]\n\t"
      "v_pk_fma_f32 %4, %14, %15, %4 op_sel:[0,0,0] op_sel_hi:[0,1,1]\n\t"
      "v_pk_fma_f32 %5, %16, %17, %5 op_sel:[0,0,0] op_sel_hi:[0,1,1]\n\t"
      "s_nop 1"
      : "=&v"(r0), "=&v"(r1), "=&v"(r2), "=&v"(r3), "=&v"(r4), "=&v"(r5)
      : "v"(a0), "v"(b0), "v"(a1), "v"(b1), "v"(a2), "v"(b2), "v"(a3), "v"(b3), "v"(a4), "v"(b4), "v"(a5), "v"(b5));
}
__device__ __forceinline__ void cmul2(f2& r0, f2& r1, f2 a0, f2 b0, f2 a1, f2 b1) {
  asm("v_pk_mul_f32 %0, %2, %3 op_sel:[1,1] op_sel_hi:[1,0] neg_lo:[0,1]\n\t"
      "v_pk_mul_f32 %1, %4, %5 op_sel:[1,1] op_sel_hi:[1,0] neg_lo:[0,1]\n\t"
      "s_nop 0\n\t"
      "v_pk_fma_f32 %0, %2, %3, %0 op_sel:[0,0,0] op_sel_hi:[0,1,1]\n\t"
      "v_pk_fma_f32 %1, %4, %5, %1 op_sel:[0,0,0] op_sel_hi:[0,1,1]\n\t"
      "s_nop 1"
      : "=&v"(r0), "=&v"(r1)
      : "v"(a0), "v"(b0), "v"(a1), "v"(b1));
}
__device__ __forceinline__ f2 cexp_(float zr, float zi, float t) {
  const float m = __builtin_amdgcn_exp2f((zr * t) * 1.44269504088896341f);
  float r = (zi * t) * 0.15915494309189535f;
  r -= rintf(r);
  return f2{m * __builtin_amdgcn_cosf(r), m * __builtin_amdgcn_sinf(r)};
}

__global__ __launch_bounds__(256) void k1_kernel(const u16* __restrict__ x, const u16* __restrict__ logA,
                                                 const u16* __restrict__ Aim, const u16* __restrict__ Cre,
                                                 const u16* __restrict__ Cim, const u16* __restrict__ logdt,
                                                 const u16* __restrict__ wg, const u16* __restrict__ wu,
                                                 const u16* __restrict__ wd, char* __restrict__ ws) {
  __shared__ __attribute__((aligned(16))) char smem[18432];
  const int tid = threadIdx.x, lane = tid & 63, wid = tid >> 6;
  DIAG_START(0)
  int bid = blockIdx.x - K1_KG_BLOCKS;
  if (bid >= 0 && bid < K1_LN_BLOCKS) {
    __builtin_amdgcn_s_setprio(3);
    if (bid == 0 && tid < 16) *(u32*)(ws + WS_CNT + tid * 256) = 0u;
#pragma unroll
    for (int it = 0; it < 4; ++it) st16(ws + WS_AT + ((size_t)bid * 1024 + it * 256 + tid) * 16, u32x4{0xffffffffu, 0xffffffffu, 0xffffffffu, 0xffffffffu});
    u16* tile = (u16*)smem;
    u16* xnt = (u16*)(ws + WS_XNT);
    const int r0 = bid * 16;
    u32x4 v[4];
#pragma unroll
    for (int i = 0; i < 4; ++i) v[i] = *(const u32x4*)(x + (size_t)(r0 + wid * 4 + i) * D_ + lane * 8);
#pragma unroll
    for (int i = 0; i < 4; ++i) {
      const int rl = wid * 4 + i;
      f2 f[4];
#pragma unroll
      for (int j = 0; j < 4; ++j) f[j] = unpack2(v[i][j]);
      const f2 s2 = (f[0] + f[1]) + (f[2] + f[3]);
      const float mean = wave_sum(s2.x + s2.y) * (1.f / D_);
      f2 q2 = {0.f, 0.f};
#pragma unroll
      for (int j = 0; j < 4; ++j) { const f2 t = f[j] - mean; q2 = __builtin_elementwise_fma(t, t, q2); }
      const float q = wave_sum(q2.x + q2.y);
      const float mb = rbf(mean);
      const float vb = rbf(q * (1.f / D_));
      const float sd = rbf(sqrtf(rbf(vb + 1e-5f)));
      const float rs = __builtin_amdgcn_rcpf(sd);
      u32x4 o;
#pragma unroll
      for (int j = 0; j < 4; ++j) {
        const f2 t = f[j] - mb;
        const f2 a = unpack2(pack2(t.x, t.y));
        f2 qq = a * rs;
        const f2 rem = __builtin_elementwise_fma(-qq, f2{sd, sd}, a);
        qq = __builtin_elementwise_fma(rem, f2{rs, rs}, qq);
        o[j] = pack2(qq.x, qq.y);
      }
      *(u32x4*)(tile + rl * 520 + lane * 8) = o;
    }
    DIAG_STAMP(1);
    __syncthreads();
#pragma unroll
    for (int it = 0; it < 4; ++it) {
      const int c = tid + it * 256;
      const u32x4 o = *(const u32x4*)(tile + (c & 15) * 520 + (c >> 4) * 8);
      st16(xnt + ((size_t)bid * 1024 + c) * 8, o);
    }
    u16* xtr = (u16*)(ws + WS_XNTR);
#pragma unroll
    for (int it = 0; it < 4; ++it) {
      const int c = tid + it * 256;
      const int d = c >> 1, g = c & 1;
      u32x4 o;
#pragma unroll
      for (int j = 0; j < 4; ++j)
        o[j] = (u32)tile[(g * 8 + 2 * j) * 520 + d] | ((u32)tile[(g * 8 + 2 * j + 1) * 520 + d] << 16);
      st16(xtr + ((size_t)bid * 1024 + c) * 8, o);
    }
    DIAG_END(ws);
    return;
  }
  bid -= K1_LN_BLOCKS;
  if (bid >= 0) {
    __builtin_amdgcn_s_setprio(2);
    int task = bid * 4 + wid;
    const u16* W; u16* Wt; int Kd, Nd;
    if (task < 448) { W = wg; Wt = (u16*)(ws + WS_WG); Kd = D_; Nd = F_; }
    else if (task < 896) { task -= 448; W = wu; Wt = (u16*)(ws + WS_WU); Kd = D_; Nd = F_; }
    else { task -= 896; W = wd; Wt = (u16*)(ws + WS_WD); Kd = F_; Nd = D_; }
    const int nks = Kd / 32;
    const int ks = task % nks, nb = task / nks;
    const int col = nb * 64 + lane;
    u16 v[32];
#pragma unroll
    for (int k = 0; k < 32; ++k) v[k] = W[(size_t)(ks * 32 + k) * Nd + col];
#pragma unroll
    for (int q = 0; q < 4; ++q) {
      u32x4 o;
#pragma unroll
      for (int j = 0; j < 4; ++j) o[j] = (u32)v[q * 8 + 2 * j] | ((u32)v[q * 8 + 2 * j + 1] << 16);
      st16(Wt + ((size_t)((col >> 4) * nks + ks) * 64 + q * 16 + (col & 15)) * 8, o);
    }
    DIAG_END(ws);
    return;
  }
  bid = blockIdx.x;
  {
    const int d = bid * 2;
    float4* prm = (float4*)(smem + 16384);
    if (tid < 128) {
      const u16 la = logA[d * N_ + tid], ai = Aim[d * N_ + tid], cr = Cre[d * N_ + tid], ci = Cim[d * N_ + tid];
      const float dt = rbf(expf(bf2f(logdt[d])));
      const float ar = -rbf(expf(bf2f(la)));
      prm[tid] = make_float4(ar * dt, bf2f(ai) * dt, bf2f(cr) * dt, bf2f(ci) * dt);
    }
    __syncthreads();
    DIAG_STAMP(1);
    const int kq = lane >> 4, idx = lane & 15;
    f32x4 acc[4] = {};
    const float ty = (float)idx, te = (float)(idx == 0 ? 256 : 16 * idx);
    float4 pr[8];
#pragma unroll
    for (int s = 0; s < 8; ++s) pr[s] = prm[wid * 32 + s * 4 + kq];
#pragma unroll
    for (int s = 0; s < 8; s += 2) {
      const float4 pa = pr[s], pb = pr[s + 1];
      const f2 Ca = f2{pa.z, -pa.w}, Cb = f2{pb.z, -pb.w};
      f2 Ya[4], Yb[4], Xa[4], Xb[4];
      Ya[0] = cexp_(pa.x, pa.y, ty); Yb[0] = cexp_(pb.x, pb.y, ty);
      f2 Ea = cexp_(pa.x, -pa.y, te), Eb = cexp_(pb.x, -pb.y, te);
      f2 Fa = (idx == 0) ? f2{1.f, 0.f} : Ea, Fb = (idx == 0) ? f2{1.f, 0.f} : Eb;
#pragma unroll
      for (int e = 0; e < 3; ++e) {
        f2 nEa, nEb;
        cmul6(Xa[e], Xb[e], Ya[e + 1], Yb[e + 1], nEa, nEb, Ca, Fa, Cb, Fb, Ya[e], Ya[e], Yb[e], Yb[e], Ea, Ea, Eb, Eb);
        Ea = nEa; Eb = nEb; Fa = nEa; Fb = nEb;
      }
      cmul2(Xa[3], Xb[3], Ca, Ea, Cb, Eb);
#pragma unroll
      for (int e = 0; e < 4; ++e) acc[e] = __builtin_amdgcn_mfma_f32_16x16x4f32(Xa[e].x, Ya[e].x, acc[e], 0, 0, 0);
#pragma unroll
      for (int e = 0; e < 4; ++e) acc[e] = __builtin_amdgcn_mfma_f32_16x16x4f32(Xa[e].y, Ya[e].y, acc[e], 0, 0, 0);
#pragma unroll
      for (int e = 0; e < 4; ++e) acc[e] = __builtin_amdgcn_mfma_f32_16x16x4f32(Xb[e].x, Yb[e].x, acc[e], 0, 0, 0);
#pragma unroll
      for (int e = 0; e < 4; ++e) acc[e] = __builtin_amdgcn_mfma_f32_16x16x4f32(Xb[e].y, Yb[e].y, acc[e], 0, 0, 0);
    }
    DIAG_STAMP(2);
    float* red = (float*)smem;
#pragma unroll
    for (int e = 0; e < 4; ++e)
#pragma unroll
      for (int r = 0; r < 4; ++r) red[(wid * 4 + e) * 256 + ((lane >> 4) * 4 + r) * 16 + (lane & 15)] = acc[e][r];
    __syncthreads();
    float* kc = (float*)(ws + WS_KC) + (size_t)d * KC_STRIDE;
#pragma unroll
    for (int e = 0; e < 4; ++e) {
      const float v = 2.f * ((red[(0 * 4 + e) * 256 + tid] + red[(1 * 4 + e) * 256 + tid]) +
                             (red[(2 * 4 + e) * 256 + tid] + red[(3 * 4 + e) * 256 + tid]));
      if (e == 0) kc[tid] = v;
      else if (tid >= 128) kc[128 * e + tid] = v;
      else if (e == 3 && tid == 0) kc[640] = v;
    }
    DIAG_END(ws);
  }
}

constexpr int G1_STAGE = 22 * 1024;
constexpr int G1_NBUF = 5;
constexpr int KG_RED = G1_STAGE * G1_NBUF;
constexpr int KG_PRM = KG_RED + 32768;
constexpr int KG_KRAW = KG_PRM + 2048;
constexpr int K2_LDS = KG_KRAW + 2624;

constexpr int UC_LEN = 2080;
constexpr int KR_LEN = 3840;
constexpr int CV_UCP = 0;
constexpr int CV_KR = CV_UCP + 8 * UC_LEN * 2;
constexpr int CV_URAW = CV_KR + KR_LEN * 2;
constexpr int CV_KRAW = CV_URAW + 4224;
constexpr int CV_MAX = CV_KRAW + 2592;
constexpr int CV_CH_STRIDE = 48128;
constexpr int H_STAGE = 2 * CV_CH_STRIDE;

__device__ __forceinline__ f2 gelu2(f2 g) {
  const float c0 = -2.f * 0.7978845608028654f * 1.44269504088896341f;
  const float c1 = c0 * 0.044715f;
  const f2 t = g * __builtin_elementwise_fma(g * g, f2{c1, c1}, f2{c0, c0});
  const f2 dn = f2{__builtin_amdgcn_exp2f(t.x), __builtin_amdgcn_exp2f(t.y)} + 1.f;
  return g * f2{__builtin_amdgcn_rcpf(dn.x), __builtin_amdgcn_rcpf(dn.y)};
}
__device__ __forceinline__ void glds16(const void* g, void* l) {
  __builtin_amdgcn_global_load_lds((const __attribute__((address_space(1))) u32*)g,
                                   (__attribute__((address_space(3))) u32*)l, 16, 0, 0);
}
__device__ __forceinline__ u32 bf2_to_h2(u32 w) {
  auto r = __builtin_amdgcn_cvt_pkrtz(__uint_as_float(w << 16), __uint_as_float(w & 0xffff0000u));
  return __builtin_bit_cast(u32, r);
}
#define RAW_BAR() do { asm volatile("s_waitcnt lgkmcnt(0)" ::: "memory"); __builtin_amdgcn_s_barrier(); } while (0)

__global__ __launch_bounds__(512, 2) void k2_kernel(const u16* __restrict__ x, const u16* __restrict__ logA,
                                                    const u16* __restrict__ Aim, const u16* __restrict__ Cre,
                                                    const u16* __restrict__ Cim, const u16* __restrict__ logdt,
                                                    const u16* __restrict__ dskip, char* __restrict__ ws,
                                                    u16* __restrict__ out) {
  extern __shared__ __attribute__((aligned(16))) char smem[];
  const int tid = threadIdx.x, lane = tid & 63;
  const int wid = __builtin_amdgcn_readfirstlane(tid >> 6);
  DIAG_START(1)
  const int bid = blockIdx.x;
  const int xcd = bid & 7, loc = bid >> 3;
  const int ch = wid >> 2, w4 = wid & 3;
  const int d = bid * 2 + ch;
  const int t2 = tid & 255;
  const int mt = (xcd >> 1) * 4 + (loc & 3);
  const int nt = (xcd & 1) * 8 + (loc >> 2);

  u32x4 uv0; float kv0, kv1, kv2; u32 pds;
  {
    const u16* xnt = (const u16*)(ws + WS_XNT);
    const u16* wgt = (const u16*)(ws + WS_WG);
    const u16* wut = (const u16*)(ws + WS_WU);
    auto src = [&](int f) -> const u16* {
      if (f < 8) return xnt + (size_t)(mt * 8 + f) * 16 * 512;
      const int s = f - 8;
      return ((s & 1) ? wut : wgt) + (size_t)(nt * 7 + (s >> 1)) * 16 * 512;
    };
    const u16* s0 = src(wid) + lane * 8;
    const u16* s1 = src(wid + 8) + lane * 8;
    const u16* s2 = src(wid < 6 ? wid + 16 : 21) + lane * 8;
    auto stage = [&](int ks) {
      char* base = smem + (ks % G1_NBUF) * G1_STAGE + wid * 1024 + lane * 16;
      glds16(s0 + ks * 512, base);
      glds16(s1 + ks * 512, base + 8192);
      if (wid < 6) glds16(s2 + ks * 512, base + 16384);
    };
#define G1_WAIT(n) do { if (wid < 6) asm volatile("s_waitcnt vmcnt(%0)" ::"n"(3 * (n)) : "memory"); \
                        else asm volatile("s_waitcnt vmcnt(%0)" ::"n"(2 * (n)) : "memory"); } while (0)
    u32 pla, pai, pcr, pci, pdt;
    {
      const int pi = (bid * 2 + 1) * N_ + (tid & 127);
      asm volatile(
          "global_load_ushort %0, %5, off\n\t"
          "global_load_ushort %1, %6, off\n\t"
          "global_load_ushort %2, %7, off\n\t"
          "global_load_ushort %3, %8, off\n\t"
          "global_load_ushort %4, %9, off"
          : "=&v"(pla), "=&v"(pai), "=&v"(pcr), "=&v"(pci), "=&v"(pdt)
          : "v"(logA + pi), "v"(Aim + pi), "v"(Cre + pi), "v"(Cim + pi), "v"(logdt + bid * 2 + 1)
          : "memory");
    }
#pragma unroll
    for (int k = 0; k < G1_NBUF; ++k) stage(k);
    G1_WAIT(G1_NBUF);
    asm volatile("" : "+v"(pla), "+v"(pai), "+v"(pcr), "+v"(pci), "+v"(pdt));
    {
      float4* prm = (float4*)(smem + KG_PRM);
      if (tid < 128) {
        const float dt = rbf(expf(bf2f((u16)pdt)));
        const float ar = -rbf(expf(bf2f((u16)pla)));
        prm[tid] = make_float4(ar * dt, bf2f((u16)pai) * dt, bf2f((u16)pcr) * dt, bf2f((u16)pci) * dt);
      }
      RAW_BAR();
      const int kq = lane >> 4, idx = lane & 15;
      f32x4 kacc[4] = {};
      const float ty = (float)idx, te = (float)(idx == 0 ? 256 : 16 * idx);
      float4 pr[4];
#pragma unroll
      for (int s = 0; s < 4; ++s) pr[s] = prm[wid * 16 + s * 4 + kq];
#pragma unroll
      for (int s = 0; s < 4; s += 2) {
        const float4 pa = pr[s], pb = pr[s + 1];
        const f2 Ca = f2{pa.z, -pa.w}, Cb = f2{pb.z, -pb.w};
        f2 Ya[4], Yb[4], Xa[4], Xb[4];
        Ya[0] = cexp_(pa.x, pa.y, ty); Yb[0] = cexp_(pb.x, pb.y, ty);
        f2 Ea = cexp_(pa.x, -pa.y, te), Eb = cexp_(pb.x, -pb.y, te);
        f2 Fa = (idx == 0) ? f2{1.f, 0.f} : Ea, Fb = (idx == 0) ? f2{1.f, 0.f} : Eb;
#pragma unroll
        for (int e = 0; e < 3; ++e) {
          f2 nEa, nEb;
          cmul6(Xa[e], Xb[e], Ya[e + 1], Yb[e + 1], nEa, nEb, Ca, Fa, Cb, Fb, Ya[e], Ya[e], Yb[e], Yb[e], Ea, Ea, Eb, Eb);
          Ea = nEa; Eb = nEb; Fa = nEa; Fb = nEb;
        }
        cmul2(Xa[3], Xb[3], Ca, Ea, Cb, Eb);
#pragma unroll
        for (int e = 0; e < 4; ++e) kacc[e] = __builtin_amdgcn_mfma_f32_16x16x4f32(Xa[e].x, Ya[e].x, kacc[e], 0, 0, 0);
#pragma unroll
        for (int e = 0; e < 4; ++e) kacc[e] = __builtin_amdgcn_mfma_f32_16x16x4f32(Xa[e].y, Ya[e].y, kacc[e], 0, 0, 0);
#pragma unroll
        for (int e = 0; e < 4; ++e) kacc[e] = __builtin_amdgcn_mfma_f32_16x16x4f32(Xb[e].x, Yb[e].x, kacc[e], 0, 0, 0);
#pragma unroll
        for (int e = 0; e < 4; ++e) kacc[e] = __builtin_amdgcn_mfma_f32_16x16x4f32(Xb[e].y, Yb[e].y, kacc[e], 0, 0, 0);
      }
      float* red = (float*)(smem + KG_RED);
#pragma unroll
      for (int e = 0; e < 4; ++e)
#pragma unroll
        for (int r = 0; r < 4; ++r) red[(wid * 4 + e) * 256 + ((lane >> 4) * 4 + r) * 16 + (lane & 15)] = kacc[e][r];
      RAW_BAR();
      float* kr1 = (float*)(smem + KG_KRAW);
#pragma unroll
      for (int h = 0; h < 2; ++h) {
        const int e = (tid >> 8) * 2 + h, q = tid & 255;
        float v = 0.f;
#pragma unroll
        for (int w = 0; w < 8; ++w) v += red[(w * 4 + e) * 256 + q];
        v *= 2.f;
        if (e == 0) kr1[q] = v;
        else if (q >= 128) kr1[128 * e + q] = v;
        else if (e == 3 && q == 0) kr1[640] = v;
      }
    }
    const int wm = wid >> 1, wn = wid & 1;
    f32x4 acc[7][2] = {};
    bf16x8 xa[2], wa[7], xb[2], wb[7];
    G1_WAIT(G1_NBUF - 1);
    RAW_BAR();
    DIAG_STAMP(1);
    {
      const char* base = smem + lane * 16;
#pragma unroll
      for (int i = 0; i < 2; ++i) xa[i] = *(const bf16x8*)(base + (wm * 2 + i) * 1024);
#pragma unroll
      for (int j = 0; j < 7; ++j) wa[j] = *(const bf16x8*)(base + (8 + wn * 7 + j) * 1024);
    }
#define G1_ITER(k, XC, WC, XN, WN, NLEFT)                                                          \
  do {                                                                                             \
    G1_WAIT(NLEFT);                                                                                \
    RAW_BAR();                                                                                     \
    const char* rb_ = smem + (((k) + 1) % G1_NBUF) * G1_STAGE + lane * 16;                         \
    _Pragma("unroll") for (int j = 0; j < 7; ++j) {                                                \
      acc[j][0] = __builtin_amdgcn_mfma_f32_16x16x32_bf16(WC[j], XC[0], acc[j][0], 0, 0, 0);       \
      if (j < 2) XN[j] = *(const bf16x8*)(rb_ + (wm * 2 + j) * 1024);                              \
      acc[j][1] = __builtin_amdgcn_mfma_f32_16x16x32_bf16(WC[j], XC[1], acc[j][1], 0, 0, 0);       \
      WN[j] = *(const bf16x8*)(rb_ + (8 + wn * 7 + j) * 1024);                                     \
      __builtin_amdgcn_sched_barrier(0);                                                           \
      if (j == 3 && (k) + G1_NBUF < 16) { stage((k) + G1_NBUF); __builtin_amdgcn_sched_barrier(0); } \
    }                                                                                              \
  } while (0)
    G1_ITER(0, xa, wa, xb, wb, 3);
    G1_ITER(1, xb, wb, xa, wa, 3);
    G1_ITER(2, xa, wa, xb, wb, 3);
    G1_ITER(3, xb, wb, xa, wa, 3);
    G1_ITER(4, xa, wa, xb, wb, 3);
    G1_ITER(5, xb, wb, xa, wa, 3);
    G1_ITER(6, xa, wa, xb, wb, 3);
    G1_ITER(7, xb, wb, xa, wa, 3);
    G1_ITER(8, xa, wa, xb, wb, 3);
    G1_ITER(9, xb, wb, xa, wa, 3);
    G1_ITER(10, xa, wa, xb, wb, 3);
    G1_ITER(11, xb, wb, xa, wa, 3);
    G1_ITER(12, xa, wa, xb, wb, 2);
    G1_ITER(13, xb, wb, xa, wa, 1);
    G1_ITER(14, xa, wa, xb, wb, 0);
    {
      const u16* ug = (const u16*)(ws + WS_XNTR) + (size_t)(t2 >> 1) * 8192 + d * 16 + (t2 & 1) * 8;
      const float* kc = (const float*)(ws + WS_KC) + (size_t)d * KC_STRIDE;
      uv0 = *(const u32x4*)ug;
      if (ch == 0) { kv0 = kc[t2]; kv1 = kc[t2 + 256]; kv2 = (t2 < 129) ? kc[t2 + 512] : 0.f; }
      else { const float* kr1 = (const float*)(smem + KG_KRAW); kv0 = kr1[t2]; kv1 = kr1[t2 + 256]; kv2 = (t2 < 129) ? kr1[t2 + 512] : 0.f; }
      pds = dskip[d];
    }
    __builtin_amdgcn_sched_barrier(0);
#pragma unroll
    for (int j = 0; j < 7; ++j)
#pragma unroll
      for (int i = 0; i < 2; ++i) acc[j][i] = __builtin_amdgcn_mfma_f32_16x16x32_bf16(wb[j], xb[i], acc[j][i], 0, 0, 0);
    DIAG_STAMP(2);
    RAW_BAR();
    f32x4* ex = (f32x4*)(smem + H_STAGE + 28672);
    if (wn == 0) {
#pragma unroll
      for (int i = 0; i < 2; ++i) ex[(wm * 2 + i) * 64 + lane] = acc[6][i];
    }
    char* hs = smem + H_STAGE;
    auto emit = [&](int t, const f32x4& g, const f32x4& u, int i) {
      u32x2 o;
#pragma unroll
      for (int p = 0; p < 2; ++p) {
        const f2 gb = unpack2(pack2(g[2 * p], g[2 * p + 1]));
        const f2 ub = unpack2(pack2(u[2 * p], u[2 * p + 1]));
        const f2 ge = gelu2(gb);
        const f2 hv = unpack2(pack2(ge.x, ge.y)) * ub;
        o[p] = pack2(hv.x, hv.y);
      }
      *(u32x2*)(hs + (((wm * 2 + i) * 7 + t) * 32 + (lane >> 5) * 16 + (lane & 15)) * 16 + ((lane >> 4) & 1) * 8) = o;
    };
    if (wn == 0) {
#pragma unroll
      for (int t = 0; t < 3; ++t)
#pragma unroll
        for (int i = 0; i < 2; ++i) emit(t, acc[2 * t][i], acc[2 * t + 1][i], i);
      RAW_BAR();
    } else {
#pragma unroll
      for (int t = 4; t < 7; ++t)
#pragma unroll
        for (int i = 0; i < 2; ++i) emit(t, acc[2 * t - 7][i], acc[2 * t - 6][i], i);
      RAW_BAR();
#pragma unroll
      for (int i = 0; i < 2; ++i) emit(3, ex[(wm * 2 + i) * 64 + lane], acc[0][i], i);
    }
    {
      char* cbase = smem + ch * CV_CH_STRIDE;
      u32x4* uraw = (u32x4*)(cbase + CV_URAW);
      float* kraw = (float*)(cbase + CV_KRAW);
      float* kmax = (float*)(cbase + CV_MAX);
      if (t2 < 4) { uraw[t2] = u32x4{0, 0, 0, 0}; uraw[260 + t2] = u32x4{0, 0, 0, 0}; }
      u32x4 h;
#pragma unroll
      for (int j = 0; j < 4; ++j) h[j] = bf2_to_h2(uv0[j]);
      uraw[4 + t2] = h;
      kraw[t2] = kv0;
      kraw[t2 + 256] = kv1;
      if (t2 < 136) kraw[t2 + 512] = kv2;
      const float mx = wave_max(fmaxf(fmaxf(fabsf(kv0), fabsf(kv1)), fabsf(kv2)));
      if (lane == 0) kmax[w4] = mx;
    }
    RAW_BAR();
    u16* hT = (u16*)(ws + WS_H);
#pragma unroll
    for (int it = 0; it < 4; ++it) {
      const int c = tid + it * 512;
      if (c < 1792) {
        const int ml = c / 224, rem = c - ml * 224;
        st16_wt(hT + ((size_t)(mt * 8 + ml) * 56 * 64 + (size_t)nt * 7 * 32 + rem) * 8, *(const u32x4*)(hs + c * 16));
      }
    }
  }
  DIAG_STAMP(3);

  const int mt2 = (xcd >> 1) * 8 + (xcd & 1) * 4 + (loc & 3);
  const int nt2 = loc >> 2;
  const int vo = lane * 16;
  const __amdgpu_buffer_rsrc_t hr = __builtin_amdgcn_make_buffer_rsrc((void*)((const u16*)(ws + WS_H) + ((size_t)(mt2 * 4) * 56 + wid * 7) * 512), 0, 4 * 56 * 1024, 0x00020000);
  const __amdgpu_buffer_rsrc_t wr = __builtin_amdgcn_make_buffer_rsrc((void*)((const u16*)(ws + WS_WD) + ((size_t)(nt2 * 4) * 56 + wid * 7) * 512), 0, 4 * 56 * 1024, 0x00020000);
#define G2_LD(rs, i, k, aux) __builtin_bit_cast(bf16x8, __builtin_amdgcn_raw_buffer_load_b128(rs, vo, ((i) * 56 + (k)) * 1024, aux))
  bf16x8 wd[7][4];

  {
    char* cbase = smem + ch * CV_CH_STRIDE;
    u16* ucp = (u16*)(cbase + CV_UCP);
    u16* kr = (u16*)(cbase + CV_KR);
    const u32x4* uraw = (const u32x4*)(cbase + CV_URAW);
    const float* kraw = (const float*)(cbase + CV_KRAW);
    const float* kmax = (const float*)(cbase + CV_MAX);
    const float dsk = bf2f((u16)pds);
    float kscale, kinv;
    {
      const float mx = fmaxf(fmaxf(kmax[0], kmax[1]), fmaxf(kmax[2], kmax[3]));
      int e = (int)((__float_as_uint(mx) >> 23) & 0xff);
      e = min(max(e, 20), 240);
      kscale = __uint_as_float((u32)(127 + 13 + 127 - e) << 23);
      kinv = __uint_as_float((u32)(e - 13) << 23);
    }
#pragma unroll
    for (int it = 0; it < 2; ++it) {
      const int tb = it ? 256 + (t2 & 3) : t2;
      u32 w[8];
      u32x4 a = uraw[tb], b = uraw[tb + 1];
#pragma unroll
      for (int j = 0; j < 4; ++j) { w[j] = a[j]; w[4 + j] = b[j]; }
      if (it == 0 || t2 < 4) {
#pragma unroll
        for (int c = 0; c < 8; ++c) {
          u32x4 o;
#pragma unroll
          for (int j = 0; j < 4; ++j)
            o[j] = (c & 1) ? w[j + (c + 1) / 2] : __builtin_amdgcn_alignbit(w[j + c / 2 + 1], w[j + c / 2], 16);
          *(u32x4*)(ucp + c * UC_LEN + tb * 8) = o;
        }
      }
    }
    {
      float kk[8];
#pragma unroll
      for (int j = 0; j < 8; ++j) {
        const int jj = 2047 - (t2 * 8 + j);
        const int cbi = (int)f2bf((float)jj) - 17024;
        kk[j] = kraw[jj < 128 ? jj : cbi] * kscale;
      }
      f16x8 o;
#pragma unroll
      for (int j = 0; j < 8; ++j) o[j] = (_Float16)kk[j];
      *(f16x8*)(kr + t2 * 8 + 8 * (t2 >> 2)) = o;
      if (t2 < 128) { const int tb = t2 + 256; *(u32x4*)(kr + tb * 8 + 8 * (tb >> 2)) = u32x4{0, 0, 0, 0}; }
    }
    asm volatile("s_waitcnt vmcnt(0)" ::: "memory");
    RAW_BAR();
    if (tid == 0) __hip_atomic_fetch_add((gu32*)(ws + WS_CNT + mt * 256), 1u, __ATOMIC_RELAXED, __HIP_MEMORY_SCOPE_AGENT);
    DIAG_STAMP(4);
    const int r = lane & 31, h = lane >> 5;
    const u16* ap = ucp + (r & 7) * UC_LEN + 8 * h + 8 * (r >> 3);
    const int bofs0 = 2048 - 32 * (r + 1) + 8 * h + 8 * (63 - r);
    const int bofs1 = bofs0 - 1024 - 256;
    f32x16 acc0 = {}, acc1 = {};
    unsigned hcnt = 0;
    {
      const u16* apw = ap + 16 * (w4 * 16);
      const u16* bp0 = kr + bofs0 + 16 * (w4 * 16) + 8 * (w4 * 8);
      const u16* bp1 = kr + bofs1 + 16 * (w4 * 16) + 8 * (w4 * 8);
      auto lda = [&](int s) -> f16x8 { return *(const f16x8*)(apw + 16 * s); };
      auto ldb = [&](const u16* b, int s) -> f16x8 { return *(const f16x8*)(b + 16 * s + 8 * (s >> 1)); };
      f16x8 a0 = lda(0), p0 = ldb(bp0, 0), q0 = ldb(bp1, 0), c0 = lda(64);
      f16x8 a1 = lda(1), p1 = ldb(bp0, 1), q1 = ldb(bp1, 1), c1 = lda(65);
#pragma unroll
      for (int it = 0; it < 8; ++it) {
        f16x8 na0, na1, nc0, nc1, np0, np1, nq0, nq1;
        if (it < 7) {
          const int sn = 2 * it + 2;
          na0 = lda(sn); np0 = ldb(bp0, sn); nq0 = ldb(bp1, sn); nc0 = lda(sn + 64);
          na1 = lda(sn + 1); np1 = ldb(bp0, sn + 1); nq1 = ldb(bp1, sn + 1); nc1 = lda(sn + 65);
#pragma unroll
          for (int i = 0; i < 4; ++i) wd[it][i] = G2_LD(wr, i, it, 0);
        }
        if (it == 7) hcnt = __hip_atomic_load((gu32*)(ws + WS_CNT + (mt2 >> 1) * 256), __ATOMIC_RELAXED, __HIP_MEMORY_SCOPE_AGENT);
        __builtin_amdgcn_sched_barrier(0);
        acc0 = __builtin_amdgcn_mfma_f32_32x32x16_f16(a0, p0, acc0, 0, 0, 0);
        acc1 = __builtin_amdgcn_mfma_f32_32x32x16_f16(a0, q0, acc1, 0, 0, 0);
        acc1 = __builtin_amdgcn_mfma_f32_32x32x16_f16(c0, p0, acc1, 0, 0, 0);
        acc0 = __builtin_amdgcn_mfma_f32_32x32x16_f16(a1, p1, acc0, 0, 0, 0);
        acc1 = __builtin_amdgcn_mfma_f32_32x32x16_f16(a1, q1, acc1, 0, 0, 0);
        acc1 = __builtin_amdgcn_mfma_f32_32x32x16_f16(c1, p1, acc1, 0, 0, 0);
        __builtin_amdgcn_sched_barrier(0);
        if (it < 7) { a0 = na0; a1 = na1; c0 = nc0; c1 = nc1; p0 = np0; p1 = np1; q0 = nq0; q1 = nq1; }
      }
    }
    DIAG_STAMP(5);
    RAW_BAR();
    float* red = (float*)cbase;
#pragma unroll
    for (int g = 0; g < 4; ++g) {
      f32x4 v0 = {acc0[4 * g], acc0[4 * g + 1], acc0[4 * g + 2], acc0[4 * g + 3]};
      f32x4 v1 = {acc1[4 * g], acc1[4 * g + 1], acc1[4 * g + 2], acc1[4 * g + 3]};
      *(f32x4*)(red + (w4 * 32 + r) * 36 + 8 * g + 4 * h) = v0;
      *(f32x4*)(red + ((4 + w4) * 32 + r) * 36 + 8 * g + 4 * h) = v1;
    }
    if (tid == 511 && hcnt < 16u) {
      gu32* cnt = (gu32*)(ws + WS_CNT + (mt2 >> 1) * 256);
      for (unsigned spins = 0; spins < (1u << 22); ++spins) {
        if (__hip_atomic_load(cnt, __ATOMIC_RELAXED, __HIP_MEMORY_SCOPE_AGENT) >= 16u) break;
        __builtin_amdgcn_s_sleep(1);
      }
    }
    RAW_BAR();
    DIAG_STAMP2(1);
    u16* at = (u16*)(ws + WS_AT) + (size_t)d * L_;
    {
      const int c = t2;
      const int T = c >> 7, a = (c >> 2) & 31, b0 = (c & 3) * 8;
      f32x4 s0 = {}, s1 = {};
      const float* p = red + (T * 4 * 32 + a) * 36 + b0;
#pragma unroll
      for (int w = 0; w < 4; ++w) { s0 += *(const f32x4*)(p + w * 32 * 36); s1 += *(const f32x4*)(p + w * 32 * 36 + 4); }
      u32x4 o;
#pragma unroll
      for (int j = 0; j < 4; ++j) {
        const f2 y = unpack2((j < 2) ? pack2(s0[2 * j] * kinv, s0[2 * j + 1] * kinv) : pack2(s1[2 * j - 4] * kinv, s1[2 * j - 3] * kinv));
        const f2 us = unpack2(uv0[j]) * dsk;
        const f2 t = unpack2(pack2(us.x, us.y)) + y;
        u32 ov = pack2(t.x, t.y);
        if ((ov & 0xffffu) == 0xffffu) ov &= 0xffffffc0u;
        if ((ov >> 16) == 0xffffu) ov &= 0xffc0ffffu;
        o[j] = ov;
      }
      st16_wt(at + c * 8, o);
    }
  }
  DIAG_STAMP2(2);

  {
    f32x4 acc[4][4] = {};
    bf16x8 hb[4][4];
#pragma unroll
    for (int k = 0; k < 4; ++k)
#pragma unroll
      for (int i = 0; i < 4; ++i) hb[k][i] = G2_LD(hr, i, k, 16);
    const int m = tid >> 3, n0 = (tid & 7) * 8;
    const size_t go = (size_t)(mt2 * 64 + m) * D_ + nt2 * 64 + n0;
    const u32x4 xv = *(const u32x4*)(x + go);
    __builtin_amdgcn_sched_barrier(0);
#pragma unroll
    for (int k = 0; k < 7; ++k) {
#pragma unroll
      for (int j = 0; j < 4; ++j)
#pragma unroll
        for (int i = 0; i < 4; ++i) acc[j][i] = __builtin_amdgcn_mfma_f32_16x16x32_bf16(wd[k][j], hb[k & 3][i], acc[j][i], 0, 0, 0);
      if (k < 3) {
#pragma unroll
        for (int i = 0; i < 4; ++i) hb[k][i] = G2_LD(hr, i, k + 4, 16);
      }
      __builtin_amdgcn_sched_barrier(0);
    }
    DIAG_STAMP2(3);
    u32 a0, a1, a2, a3, a4, a5, a6, a7;
    const u16* at = (const u16*)(ws + WS_AT) + (size_t)(nt2 * 64 + n0) * L_ + mt2 * 64 + m;
#define A_LOADS(tail)                                                                                              \
  asm volatile("global_load_ushort %0, %8, off sc1\n\t"                                                            \
               "global_load_ushort %1, %9, off sc1\n\t"                                                            \
               "global_load_ushort %2, %10, off sc1\n\t"                                                           \
               "global_load_ushort %3, %11, off sc1\n\t"                                                           \
               "global_load_ushort %4, %12, off sc1\n\t"                                                           \
               "global_load_ushort %5, %13, off sc1\n\t"                                                           \
               "global_load_ushort %6, %14, off sc1\n\t"                                                           \
               "global_load_ushort %7, %15, off sc1" tail                                                          \
               : "=&v"(a0), "=&v"(a1), "=&v"(a2), "=&v"(a3), "=&v"(a4), "=&v"(a5), "=&v"(a6), "=&v"(a7)            \
               : "v"(at), "v"(at + L_), "v"(at + 2 * L_), "v"(at + 3 * L_), "v"(at + 4 * L_), "v"(at + 5 * L_),   \
                 "v"(at + 6 * L_), "v"(at + 7 * L_)                                                                \
               : "memory")
    A_LOADS("");
    RAW_BAR();
    float* red = (float*)smem;
    float* rp = red + (wid * 64 + (lane & 15)) * 68 + 4 * (lane >> 4);
#pragma unroll
    for (int j = 0; j < 4; ++j)
#pragma unroll
      for (int i = 0; i < 4; ++i) *(f32x4*)(rp + i * 16 * 68 + j * 16) = acc[j][i];
    asm volatile("s_waitcnt vmcnt(0)" : "+v"(a0), "+v"(a1), "+v"(a2), "+v"(a3), "+v"(a4), "+v"(a5), "+v"(a6), "+v"(a7)::"memory");
    for (unsigned spins = 0; spins < (1u << 20); ++spins) {
      const bool pend = (a0 == 0xffffu) | (a1 == 0xffffu) | (a2 == 0xffffu) | (a3 == 0xffffu) | (a4 == 0xffffu) |
                        (a5 == 0xffffu) | (a6 == 0xffffu) | (a7 == 0xffffu);
      if (!__any(pend)) break;
      __builtin_amdgcn_s_sleep(2);
      A_LOADS("\n\ts_waitcnt vmcnt(0)");
    }
    RAW_BAR();
    DIAG_STAMP2(4);
    f32x4 p0 = {}, p1 = {};
#pragma unroll
    for (int w = 0; w < 8; ++w) {
      p0 += *(const f32x4*)(red + (w * 64 + m) * 68 + n0);
      p1 += *(const f32x4*)(red + (w * 64 + m) * 68 + n0 + 4);
    }
    const u32 av[8] = {a0, a1, a2, a3, a4, a5, a6, a7};
    u32x4 o;
#pragma unroll
    for (int j = 0; j < 4; ++j) {
      const f2 xx = unpack2(xv[j]);
      const f2 aa = f2{__uint_as_float(av[2 * j] << 16), __uint_as_float(av[2 * j + 1] << 16)};
      const f2 r1 = xx + aa;
      const f2 bb = unpack2((j < 2) ? pack2(p0[2 * j], p0[2 * j + 1]) : pack2(p1[2 * j - 4], p1[2 * j - 3]));
      const f2 r2 = unpack2(pack2(r1.x, r1.y)) + bb;
      o[j] = pack2(r2.x, r2.y);
    }
    *(u32x4*)(out + go) = o;
  }
}

extern "C" void kernel_launch(void* const* d_in, const int* in_sizes, int n_in, void* d_out, int out_size,
                              void* d_ws, size_t ws_size, hipStream_t stream) {
  const u16* x = (const u16*)d_in[0];
  const u16* logA = (const u16*)d_in[1];
  const u16* Aim = (const u16*)d_in[2];
  const u16* Cre = (const u16*)d_in[3];
  const u16* Cim = (const u16*)d_in[4];
  const u16* logdt = (const u16*)d_in[5];
  const u16* dskip = (const u16*)d_in[6];
  const u16* wg = (const u16*)d_in[8];
  const u16* wu = (const u16*)d_in[9];
  const u16* wd = (const u16*)d_in[10];
  char* ws = (char*)d_ws;
  (void)hipFuncSetAttribute((const void*)k2_kernel, hipFuncAttributeMaxDynamicSharedMemorySize, K2_LDS);
  k1_kernel<<<K1_LN_BLOCKS + K1_WT_BLOCKS + K1_KG_BLOCKS, 256, 0, stream>>>(x, logA, Aim, Cre, Cim, logdt, wg, wu, wd, ws);
  k2_kernel<<<256, 512, K2_LDS, stream>>>(x, logA, Aim, Cre, Cim, logdt, dskip, ws, (u16*)d_out);
}
```

```cpp
#include <hip/hip_runtime.h>
#include <hip/hip_bf16.h>
#define WT_STORES
#define CV_PRIO 1
#define G2_PRIO 3

typedef unsigned short u16;
typedef unsigned int u32;
using bf16x8 = __attribute__((ext_vector_type(8))) short;
using f16x8 = __attribute__((ext_vector_type(8))) _Float16;
using f16x4 = __attribute__((ext_vector_type(4))) _Float16;
using f32x4 = __attribute__((ext_vector_type(4))) float;
using f32x16 = __attribute__((ext_vector_type(16))) float;
using u32x4 = __attribute__((ext_vector_type(4))) unsigned int;
using u32x2 = __attribute__((ext_vector_type(2))) unsigned int;
typedef float f2 __attribute__((ext_vector_type(2)));
typedef __attribute__((address_space(1))) unsigned gu32;

constexpr int L_ = 2048, D_ = 512, N_ = 128, F_ = 1792;
constexpr int KC_STRIDE = 648;

constexpr size_t WS_CNT = 0;
constexpr size_t WS_XNT = 4096;
constexpr size_t WS_XNTR = WS_XNT + (size_t)L_ * D_ * 2;
constexpr size_t WS_KC = WS_XNTR + (size_t)L_ * D_ * 2;
constexpr size_t WS_H = WS_KC + (size_t)D_ * KC_STRIDE * 4;
constexpr size_t WS_WG = WS_H + (size_t)L_ * F_ * 2;
constexpr size_t WS_WU = WS_WG + (size_t)D_ * F_ * 2;
constexpr size_t WS_WD = WS_WU + (size_t)D_ * F_ * 2;
constexpr size_t WS_AT = WS_WD + (size_t)D_ * F_ * 2;

#define DIAG_START(k)
#define DIAG_STAMP(i)
#define DIAG_END(wsp)
#define DIAG_STAMP2(i)

__device__ __forceinline__ float bf2f(u16 v) { return __uint_as_float(((u32)v) << 16); }
__device__ __forceinline__ u16 f2bf(float f) {
  __bf16 b = (__bf16)f;
  return __builtin_bit_cast(u16, b);
}
__device__ __forceinline__ float rbf(float f) { return bf2f(f2bf(f)); }
__device__ __forceinline__ u32 pack2(float lo, float hi) {
  typedef __bf16 b2 __attribute__((ext_vector_type(2)));
  b2 r = __builtin_convertvector(f2{lo, hi}, b2);
  return __builtin_bit_cast(u32, r);
}
__device__ __forceinline__ f2 unpack2(u32 w) { return f2{__uint_as_float(w << 16), __uint_as_float(w & 0xffff0000u)}; }

__device__ __forceinline__ void st16_wt(void* p, u32x4 v) { asm volatile("global_store_dwordx4 %0, %1, off sc1\n\ts_nop 1" ::"v"(p), "v"(v) : "memory"); }
__device__ __forceinline__ void st16(void* p, u32x4 v) { st16_wt(p, v); }
__device__ __forceinline__ void st8(void* p, u32x2 v) { asm volatile("global_store_dwordx2 %0, %1, off sc1\n\ts_nop 1" ::"v"(p), "v"(v) : "memory"); }

#define DPP_F(v, ctrl) __builtin_bit_cast(float, __builtin_amdgcn_update_dpp(0, __builtin_bit_cast(int, v), ctrl, 0xf, 0xf, true))
__device__ __forceinline__ float wave_sum(float v) {
  v += DPP_F(v, 0xB1);
  v += DPP_F(v, 0x4E);
  v += DPP_F(v, 0x141);
  v += DPP_F(v, 0x140);
  const int iv = __builtin_bit_cast(int, v);
  return (__builtin_bit_cast(float, __builtin_amdgcn_readlane(iv, 0)) + __builtin_bit_cast(float, __builtin_amdgcn_readlane(iv, 16))) +
         (__builtin_bit_cast(float, __builtin_amdgcn_readlane(iv, 32)) + __builtin_bit_cast(float, __builtin_amdgcn_readlane(iv, 48)));
}
__device__ __forceinline__ float wave_max(float v) {
  v = fmaxf(v, DPP_F(v, 0xB1));
  v = fmaxf(v, DPP_F(v, 0x4E));
  v = fmaxf(v, DPP_F(v, 0x141));
  v = fmaxf(v, DPP_F(v, 0x140));
  const int iv = __builtin_bit_cast(int, v);
  return fmaxf(fmaxf(__builtin_bit_cast(float, __builtin_amdgcn_readlane(iv, 0)), __builtin_bit_cast(float, __builtin_amdgcn_readlane(iv, 16))),
               fmaxf(__builtin_bit_cast(float, __builtin_amdgcn_readlane(iv, 32)), __builtin_bit_cast(float, __builtin_amdgcn_readlane(iv, 48))));
}

constexpr int K1_LN_BLOCKS = 128;
constexpr int K1_WT_BLOCKS = 336;
constexpr int K1_KG_BLOCKS = 512;

__device__ __forceinline__ void cmul6(f2& r0, f2& r1, f2& r2, f2& r3, f2& r4, f2& r5, f2 a0, f2 b0, f2 a1, f2 b1,
                                      f2 a2, f2 b2, f2 a3, f2 b3, f2 a4, f2 b4, f2 a5, f2 b5) {
  asm("v_pk_mul_f32 %0, %6, %7 op_sel:[1,1] op_sel_hi:[1,0] neg_lo:[0,1]\n\t"
      "v_pk_mul_f32 %1, %8, %9 op_sel:[1,1] op_sel_hi:[1,0] neg_lo:[0,1]\n\t"
      "v_pk_mul_f32 %2, %10, %11 op_sel:[1,1] op_sel_hi:[1,0] neg_lo:[0,1]\n\t"
      "v_pk_mul_f32 %3, %12, %13 op_sel:[1,1] op_sel_hi:[1,0] neg_lo:[0,1]\n\t"
      "v_pk_mul_f32 %4, %14, %15 op_sel:[1,1] op_sel_hi:[1,0] neg_lo:[0,1]\n\t"
      "v_pk_mul_f32 %5, %16, %17 op_sel:[1,1] op_sel_hi:[1,0] neg_lo:[0,1]\n\t"
      "v_pk_fma_f32 %0, %6, %7, %0 op_sel:[0,0,0] op_sel_hi:[0,1,1]\n\t"
      "v_pk_fma_f32 %1, %8, %9, %1 op_sel:[0,0,0] op_sel_hi:[0,1,1]\n\t"
      "v_pk_fma_f32 %2, %10, %11, %2 op_sel:[0,0,0] op_sel_hi:[0,1,1]\n\t"
      "v_pk_fma_f32 %3, %12, %13, %3 op_sel:[0,0,0] op_sel_hi:[0,1,1]\n\t"
      "v_pk_fma_f32 %4, %14, %15, %4 op_sel:[0,0,0] op_sel_hi:[0,1,1]\n\t"
      "v_pk_fma_f32 %5, %16, %17, %5 op_sel:[0,0,0] op_sel_hi:[0,1,1]\n\t"
      "s_nop 1"
      : "=&v"(r0), "=&v"(r1), "=&v"(r2), "=&v"(r3), "=&v"(r4), "=&v"(r5)
      : "v"(a0), "v"(b0), "v"(a1), "v"(b1), "v"(a2), "v"(b2), "v"(a3), "v"(b3), "v"(a4), "v"(b4), "v"(a5), "v"(b5));
}
__device__ __forceinline__ void cmul2(f2& r0, f2& r1, f2 a0, f2 b0, f2 a1, f2 b1) {
  asm("v_pk_mul_f32 %0, %2, %3 op_sel:[1,1] op_sel_hi:[1,0] neg_lo:[0,1]\n\t"
      "v_pk_mul_f32 %1, %4, %5 op_sel:[1,1] op_sel_hi:[1,0] neg_lo:[0,1]\n\t"
      "s_nop 0\n\t"
      "v_pk_fma_f32 %0, %2, %3, %0 op_sel:[0,0,0] op_sel_hi:[0,1,1]\n\t"
      "v_pk_fma_f32 %1, %4, %5, %1 op_sel:[0,0,0] op_sel_hi:[0,1,1]\n\t"
      "s_nop 1"
      : "=&v"(r0), "=&v"(r1)
      : "v"(a0), "v"(b0), "v"(a1), "v"(b1));
}
__device__ __forceinline__ f2 cexp_(float zr, float zi, float t) {
  const float m = __builtin_amdgcn_exp2f((zr * t) * 1.44269504088896341f);
  float r = (zi * t) * 0.15915494309189535f;
  r -= rintf(r);
  return f2{m * __builtin_amdgcn_cosf(r), m * __builtin_amdgcn_sinf(r)};
}

__global__ __launch_bounds__(256) void k1_kernel(const u16* __restrict__ x, const u16* __restrict__ logA,
                                                 const u16* __restrict__ Aim, const u16* __restrict__ Cre,
                                                 const u16* __restrict__ Cim, const u16* __restrict__ logdt,
                                                 const u16* __restrict__ wg, const u16* __restrict__ wu,
                                                 const u16* __restrict__ wd, char* __restrict__ ws) {
  __shared__ __attribute__((aligned(16))) char smem[18432];
  const int tid = threadIdx.x, lane = tid & 63, wid = tid >> 6;
  DIAG_START(0)
  int bid = blockIdx.x - K1_KG_BLOCKS;
  if (bid >= 0 && bid < K1_LN_BLOCKS) {
    __builtin_amdgcn_s_setprio(3);
    if (bid == 0 && tid < 16) *(u32*)(ws + WS_CNT + tid * 256) = 0u;
#pragma unroll
    for (int it = 0; it < 4; ++it) st16(ws + WS_AT + ((size_t)bid * 1024 + it * 256 + tid) * 16, u32x4{0xffffffffu, 0xffffffffu, 0xffffffffu, 0xffffffffu});
    u16* tile = (u16*)smem;
    u16* xnt = (u16*)(ws + WS_XNT);
    const int r0 = bid * 16;
    u32x4 v[4];
#pragma unroll
    for (int i = 0; i < 4; ++i) v[i] = *(const u32x4*)(x + (size_t)(r0 + wid * 4 + i) * D_ + lane * 8);
#pragma unroll
    for (int i = 0; i < 4; ++i) {
      const int rl = wid * 4 + i;
      f2 f[4];
#pragma unroll
      for (int j = 0; j < 4; ++j) f[j] = unpack2(v[i][j]);
      const f2 s2 = (f[0] + f[1]) + (f[2] + f[3]);
      const float mean = wave_sum(s2.x + s2.y) * (1.f / D_);
      f2 q2 = {0.f, 0.f};
#pragma unroll
      for (int j = 0; j < 4; ++j) { const f2 t = f[j] - mean; q2 = __builtin_elementwise_fma(t, t, q2); }
      const float q = wave_sum(q2.x + q2.y);
      const float mb = rbf(mean);
      const float vb = rbf(q * (1.f / D_));
      const float sd = rbf(sqrtf(rbf(vb + 1e-5f)));
      const float rs = __builtin_amdgcn_rcpf(sd);
      u32x4 o;
#pragma unroll
      for (int j = 0; j < 4; ++j) {
        const f2 t = f[j] - mb;
        const f2 a = unpack2(pack2(t.x, t.y));
        f2 qq = a * rs;
        const f2 rem = __builtin_elementwise_fma(-qq, f2{sd, sd}, a);
        qq = __builtin_elementwise_fma(rem, f2{rs, rs}, qq);
        o[j] = pack2(qq.x, qq.y);
      }
      *(u32x4*)(tile + rl * 520 + lane * 8) = o;
    }
    DIAG_STAMP(1);
    __syncthreads();
#pragma unroll
    for (int it = 0; it < 4; ++it) {
      const int c = tid + it * 256;
      const u32x4 o = *(const u32x4*)(tile + (c & 15) * 520 + (c >> 4) * 8);
      st16(xnt + ((size_t)bid * 1024 + c) * 8, o);
    }
    u16* xtr = (u16*)(ws + WS_XNTR);
#pragma unroll
    for (int it = 0; it < 4; ++it) {
      const int c = tid + it * 256;
      const int d = c >> 1, g = c & 1;
      u32x4 o;
#pragma unroll
      for (int j = 0; j < 4; ++j)
        o[j] = (u32)tile[(g * 8 + 2 * j) * 520 + d] | ((u32)tile[(g * 8 + 2 * j + 1) * 520 + d] << 16);
      st16(xtr + ((size_t)bid * 1024 + c) * 8, o);
    }
    DIAG_END(ws);
    return;
  }
  bid -= K1_LN_BLOCKS;
  if (bid >= 0) {
    __builtin_amdgcn_s_setprio(2);
    int task = bid * 4 + wid;
    const u16* W; u16* Wt; int Kd, Nd;
    if (task < 448) { W = wg; Wt = (u16*)(ws + WS_WG); Kd = D_; Nd = F_; }
    else if (task < 896) { task -= 448; W = wu; Wt = (u16*)(ws + WS_WU); Kd = D_; Nd = F_; }
    else { task -= 896; W = wd; Wt = (u16*)(ws + WS_WD); Kd = F_; Nd = D_; }
    const int nks = Kd / 32;
    const int ks = task % nks, nb = task / nks;
    const int col = nb * 64 + lane;
    u16 v[32];
#pragma unroll
    for (int k = 0; k < 32; ++k) v[k] = W[(size_t)(ks * 32 + k) * Nd + col];
#pragma unroll
    for (int q = 0; q < 4; ++q) {
      u32x4 o;
#pragma unroll
      for (int j = 0; j < 4; ++j) o[j] = (u32)v[q * 8 + 2 * j] | ((u32)v[q * 8 + 2 * j + 1] << 16);
      st16(Wt + ((size_t)((col >> 4) * nks + ks) * 64 + q * 16 + (col & 15)) * 8, o);
    }
    DIAG_END(ws);
    return;
  }
  bid = blockIdx.x;
  {
    const int d = bid;
    float4* prm = (float4*)(smem + 16384);
    if (tid < 128) {
      const u16 la = logA[d * N_ + tid], ai = Aim[d * N_ + tid], cr = Cre[d * N_ + tid], ci = Cim[d * N_ + tid];
      const float dt = rbf(expf(bf2f(logdt[d])));
      const float ar = -rbf(expf(bf2f(la)));
      prm[tid] = make_float4(ar * dt, bf2f(ai) * dt, bf2f(cr) * dt, bf2f(ci) * dt);
    }
    __syncthreads();
    DIAG_STAMP(1);
    const int kq = lane >> 4, idx = lane & 15;
    f32x4 acc[4] = {};
    const float ty = (float)idx, te = (float)(idx == 0 ? 256 : 16 * idx);
    float4 pr[8];
#pragma unroll
    for (int s = 0; s < 8; ++s) pr[s] = prm[wid * 32 + s * 4 + kq];
#pragma unroll
    for (int s = 0; s < 8; s += 2) {
      const float4 pa = pr[s], pb = pr[s + 1];
      const f2 Ca = f2{pa.z, -pa.w}, Cb = f2{pb.z, -pb.w};
      f2 Ya[4], Yb[4], Xa[4], Xb[4];
      Ya[0] = cexp_(pa.x, pa.y, ty); Yb[0] = cexp_(pb.x, pb.y, ty);
      f2 Ea = cexp_(pa.x, -pa.y, te), Eb = cexp_(pb.x, -pb.y, te);
      f2 Fa = (idx == 0) ? f2{1.f, 0.f} : Ea, Fb = (idx == 0) ? f2{1.f, 0.f} : Eb;
#pragma unroll
      for (int e = 0; e < 3; ++e) {
        f2 nEa, nEb;
        cmul6(Xa[e], Xb[e], Ya[e + 1], Yb[e + 1], nEa, nEb, Ca, Fa, Cb, Fb, Ya[e], Ya[e], Yb[e], Yb[e], Ea, Ea, Eb, Eb);
        Ea = nEa; Eb = nEb; Fa = nEa; Fb = nEb;
      }
      cmul2(Xa[3], Xb[3], Ca, Ea, Cb, Eb);
#pragma unroll
      for (int e = 0; e < 4; ++e) acc[e] = __builtin_amdgcn_mfma_f32_16x16x4f32(Xa[e].x, Ya[e].x, acc[e], 0, 0, 0);
#pragma unroll
      for (int e = 0; e < 4; ++e) acc[e] = __builtin_amdgcn_mfma_f32_16x16x4f32(Xa[e].y, Ya[e].y, acc[e], 0, 0, 0);
#pragma unroll
      for (int e = 0; e < 4; ++e) acc[e] = __builtin_amdgcn_mfma_f32_16x16x4f32(Xb[e].x, Yb[e].x, acc[e], 0, 0, 0);
#pragma unroll
      for (int e = 0; e < 4; ++e) acc[e] = __builtin_amdgcn_mfma_f32_16x16x4f32(Xb[e].y, Yb[e].y, acc[e], 0, 0, 0);
    }
    DIAG_STAMP(2);
    float* red = (float*)smem;
#pragma unroll
    for (int e = 0; e < 4; ++e)
#pragma unroll
      for (int r = 0; r < 4; ++r) red[(wid * 4 + e) * 256 + ((lane >> 4) * 4 + r) * 16 + (lane & 15)] = acc[e][r];
    __syncthreads();
    float* kc = (float*)(ws + WS_KC) + (size_t)d * KC_STRIDE;
#pragma unroll
    for (int e = 0; e < 4; ++e) {
      const float v = 2.f * ((red[(0 * 4 + e) * 256 + tid] + red[(1 * 4 + e) * 256 + tid]) +
                             (red[(2 * 4 + e) * 256 + tid] + red[(3 * 4 + e) * 256 + tid]));
      if (e == 0) kc[tid] = v;
      else if (tid >= 128) kc[128 * e + tid] = v;
      else if (e == 3 && tid == 0) kc[640] = v;
    }
    DIAG_END(ws);
  }
}

constexpr int G1_STAGE = 22 * 1024;
constexpr int G1_NBUF = 7;
constexpr int K2_LDS = G1_STAGE * G1_NBUF;

constexpr int UC_LEN = 2080;
constexpr int KR_LEN = 3840;
constexpr int CV_UCP = 0;
constexpr int CV_KR = CV_UCP + 8 * UC_LEN * 2;
constexpr int CV_URAW = CV_KR + KR_LEN * 2;
constexpr int CV_KRAW = CV_URAW + 4224;
constexpr int CV_MAX = CV_KRAW + 2592;
constexpr int CV_CH_STRIDE = 48128;
constexpr int H_STAGE = 2 * CV_CH_STRIDE;

__device__ __forceinline__ f2 gelu2(f2 g) {
  const float c0 = -2.f * 0.7978845608028654f * 1.44269504088896341f;
  const float c1 = c0 * 0.044715f;
  const f2 t = g * __builtin_elementwise_fma(g * g, f2{c1, c1}, f2{c0, c0});
  const f2 dn = f2{__builtin_amdgcn_exp2f(t.x), __builtin_amdgcn_exp2f(t.y)} + 1.f;
  return g * f2{__builtin_amdgcn_rcpf(dn.x), __builtin_amdgcn_rcpf(dn.y)};
}
__device__ __forceinline__ void glds16(const void* g, void* l) {
  __builtin_amdgcn_global_load_lds((const __attribute__((address_space(1))) u32*)g,
                                   (__attribute__((address_space(3))) u32*)l, 16, 0, 0);
}
__device__ __forceinline__ u32 bf2_to_h2(u32 w) {
  auto r = __builtin_amdgcn_cvt_pkrtz(__uint_as_float(w << 16), __uint_as_float(w & 0xffff0000u));
  return __builtin_bit_cast(u32, r);
}
#define RAW_BAR() do { asm volatile("s_waitcnt lgkmcnt(0)" ::: "memory"); __builtin_amdgcn_s_barrier(); } while (0)

__global__ __launch_bounds__(512, 2) void k2_kernel(const u16* __restrict__ x, const u16* __restrict__ dskip,
                                                    char* __restrict__ ws, u16* __restrict__ out) {
  extern __shared__ __attribute__((aligned(16))) char smem[];
  const int tid = threadIdx.x, lane = tid & 63;
  const int wid = __builtin_amdgcn_readfirstlane(tid >> 6);
  DIAG_START(1)
  const int bid = blockIdx.x;
  const int xcd = bid & 7, loc = bid >> 3;
  const int ch = wid >> 2, w4 = wid & 3;
  const int d = bid * 2 + ch;
  const int t2 = tid & 255;
  const int mt = (xcd >> 1) * 4 + (loc & 3);
  const int nt = (xcd & 1) * 8 + (loc >> 2);

  u32x4 uv0; float kv0, kv1, kv2; u32 pds;
  {
    const u16* xnt = (const u16*)(ws + WS_XNT);
    const u16* wgt = (const u16*)(ws + WS_WG);
    const u16* wut = (const u16*)(ws + WS_WU);
    auto src = [&](int f) -> const u16* {
      if (f < 8) return xnt + (size_t)(mt * 8 + f) * 16 * 512;
      const int s = f - 8;
      return ((s & 1) ? wut : wgt) + (size_t)(nt * 7 + (s >> 1)) * 16 * 512;
    };
    const u16* s0 = src(wid) + lane * 8;
    const u16* s1 = src(wid + 8) + lane * 8;
    const u16* s2 = src(wid < 6 ? wid + 16 : 21) + lane * 8;
    auto stage = [&](int ks) {
      char* base = smem + (ks % G1_NBUF) * G1_STAGE + wid * 1024 + lane * 16;
      glds16(s0 + ks * 512, base);
      glds16(s1 + ks * 512, base + 8192);
      if (wid < 6) glds16(s2 + ks * 512, base + 16384);
    };
#define G1_WAIT(n) do { if (wid < 6) asm volatile("s_waitcnt vmcnt(%0)" ::"n"(3 * (n)) : "memory"); \
                        else asm volatile("s_waitcnt vmcnt(%0)" ::"n"(2 * (n)) : "memory"); } while (0)
#pragma unroll
    for (int k = 0; k < G1_NBUF; ++k) stage(k);
    const int wm = wid >> 1, wn = wid & 1;
    f32x4 acc[7][2] = {};
    bf16x8 xa[2], wa[7], xb[2], wb[7];
    G1_WAIT(G1_NBUF - 1);
    RAW_BAR();
    DIAG_STAMP(1);
    {
      const char* base = smem + lane * 16;
#pragma unroll
      for (int i = 0; i < 2; ++i) xa[i] = *(const bf16x8*)(base + (wm * 2 + i) * 1024);
#pragma unroll
      for (int j = 0; j < 7; ++j) wa[j] = *(const bf16x8*)(base + (8 + wn * 7 + j) * 1024);
    }
#define G1_ITER(k, XC, WC, XN, WN, NLEFT)                                                          \
  do {                                                                                             \
    G1_WAIT(NLEFT);                                                                                \
    RAW_BAR();                                                                                     \
    const char* rb_ = smem + (((k) + 1) % G1_NBUF) * G1_STAGE + lane * 16;                         \
    _Pragma("unroll") for (int j = 0; j < 7; ++j) {                                                \
      acc[j][0] = __builtin_amdgcn_mfma_f32_16x16x32_bf16(WC[j], XC[0], acc[j][0], 0, 0, 0);       \
      if (j < 2) XN[j] = *(const bf16x8*)(rb_ + (wm * 2 + j) * 1024);                              \
      acc[j][1] = __builtin_amdgcn_mfma_f32_16x16x32_bf16(WC[j], XC[1], acc[j][1], 0, 0, 0);       \
      WN[j] = *(const bf16x8*)(rb_ + (8 + wn * 7 + j) * 1024);                                     \
      __builtin_amdgcn_sched_barrier(0);                                                           \
      if (j == 3 && (k) + G1_NBUF < 16) { stage((k) + G1_NBUF); __builtin_amdgcn_sched_barrier(0); } \
    }                                                                                              \
  } while (0)
    G1_ITER(0, xa, wa, xb, wb, 5);
    G1_ITER(1, xb, wb, xa, wa, 5);
    G1_ITER(2, xa, wa, xb, wb, 5);
    G1_ITER(3, xb, wb, xa, wa, 5);
    G1_ITER(4, xa, wa, xb, wb, 5);
    G1_ITER(5, xb, wb, xa, wa, 5);
    G1_ITER(6, xa, wa, xb, wb, 5);
    G1_ITER(7, xb, wb, xa, wa, 5);
    G1_ITER(8, xa, wa, xb, wb, 5);
    G1_ITER(9, xb, wb, xa, wa, 5);
    G1_ITER(10, xa, wa, xb, wb, 4);
    G1_ITER(11, xb, wb, xa, wa, 3);
    G1_ITER(12, xa, wa, xb, wb, 2);
    G1_ITER(13, xb, wb, xa, wa, 1);
    G1_ITER(14, xa, wa, xb, wb, 0);
    {
      const u16* ug = (const u16*)(ws + WS_XNTR) + (size_t)(t2 >> 1) * 8192 + d * 16 + (t2 & 1) * 8;
      const float* kc = (const float*)(ws + WS_KC) + (size_t)d * KC_STRIDE;
      uv0 = *(const u32x4*)ug;
      kv0 = kc[t2]; kv1 = kc[t2 + 256]; kv2 = (t2 < 129) ? kc[t2 + 512] : 0.f;
      pds = dskip[d];
    }
    __builtin_amdgcn_sched_barrier(0);
#pragma unroll
    for (int j = 0; j < 7; ++j)
#pragma unroll
      for (int i = 0; i < 2; ++i) acc[j][i] = __builtin_amdgcn_mfma_f32_16x16x32_bf16(wb[j], xb[i], acc[j][i], 0, 0, 0);
    DIAG_STAMP(2);
    RAW_BAR();
    f32x4* ex = (f32x4*)(smem + H_STAGE + 28672);
    if (wn == 0) {
#pragma unroll
      for (int i = 0; i < 2; ++i) ex[(wm * 2 + i) * 64 + lane] = acc[6][i];
    }
    char* hs = smem + H_STAGE;
    auto emit = [&](int t, const f32x4& g, const f32x4& u, int i) {
      u32x2 o;
#pragma unroll
      for (int p = 0; p < 2; ++p) {
        const f2 gb = unpack2(pack2(g[2 * p], g[2 * p + 1]));
        const f2 ub = unpack2(pack2(u[2 * p], u[2 * p + 1]));
        const f2 ge = gelu2(gb);
        const f2 hv = unpack2(pack2(ge.x, ge.y)) * ub;
        o[p] = pack2(hv.x, hv.y);
      }
      *(u32x2*)(hs + (((wm * 2 + i) * 7 + t) * 32 + (lane >> 5) * 16 + (lane & 15)) * 16 + ((lane >> 4) & 1) * 8) = o;
    };
    if (wn == 0) {
#pragma unroll
      for (int t = 0; t < 3; ++t)
#pragma unroll
        for (int i = 0; i < 2; ++i) emit(t, acc[2 * t][i], acc[2 * t + 1][i], i);
      RAW_BAR();
    } else {
#pragma unroll
      for (int t = 4; t < 7; ++t)
#pragma unroll
        for (int i = 0; i < 2; ++i) emit(t, acc[2 * t - 7][i], acc[2 * t - 6][i], i);
      RAW_BAR();
#pragma unroll
      for (int i = 0; i < 2; ++i) emit(3, ex[(wm * 2 + i) * 64 + lane], acc[0][i], i);
    }
    {
      char* cbase = smem + ch * CV_CH_STRIDE;
      u32x4* uraw = (u32x4*)(cbase + CV_URAW);
      float* kraw = (float*)(cbase + CV_KRAW);
      float* kmax = (float*)(cbase + CV_MAX);
      if (t2 < 4) { uraw[t2] = u32x4{0, 0, 0, 0}; uraw[260 + t2] = u32x4{0, 0, 0, 0}; }
      u32x4 h;
#pragma unroll
      for (int j = 0; j < 4; ++j) h[j] = bf2_to_h2(uv0[j]);
      uraw[4 + t2] = h;
      kraw[t2] = kv0;
      kraw[t2 + 256] = kv1;
      if (t2 < 136) kraw[t2 + 512] = kv2;
      const float mx = wave_max(fmaxf(fmaxf(fabsf(kv0), fabsf(kv1)), fabsf(kv2)));
      if (lane == 0) kmax[w4] = mx;
    }
    RAW_BAR();
    u16* hT = (u16*)(ws + WS_H);
#pragma unroll
    for (int it = 0; it < 4; ++it) {
      const int c = tid + it * 512;
      if (c < 1792) {
        const int ml = c / 224, rem = c - ml * 224;
        st16_wt(hT + ((size_t)(mt * 8 + ml) * 56 * 64 + (size_t)nt * 7 * 32 + rem) * 8, *(const u32x4*)(hs + c * 16));
      }
    }
  }
  DIAG_STAMP(3);

  const int mt2 = (xcd >> 1) * 8 + (xcd & 1) * 4 + (loc & 3);
  const int nt2 = loc >> 2;
  const int vo = lane * 16;
  const __amdgpu_buffer_rsrc_t hr = __builtin_amdgcn_make_buffer_rsrc((void*)((const u16*)(ws + WS_H) + ((size_t)(mt2 * 4) * 56 + wid * 7) * 512), 0, 4 * 56 * 1024, 0x00020000);
  const __amdgpu_buffer_rsrc_t wr = __builtin_amdgcn_make_buffer_rsrc((void*)((const u16*)(ws + WS_WD) + ((size_t)(nt2 * 4) * 56 + wid * 7) * 512), 0, 4 * 56 * 1024, 0x00020000);
#define G2_LD(rs, i, k, aux) __builtin_bit_cast(bf16x8, __builtin_amdgcn_raw_buffer_load_b128(rs, vo, ((i) * 56 + (k)) * 1024, aux))
  bf16x8 wd[7][4];

  {
    char* cbase = smem + ch * CV_CH_STRIDE;
    u16* ucp = (u16*)(cbase + CV_UCP);
    u16* kr = (u16*)(cbase + CV_KR);
    const u32x4* uraw = (const u32x4*)(cbase + CV_URAW);
    const float* kraw = (const float*)(cbase + CV_KRAW);
    const float* kmax = (const float*)(cbase + CV_MAX);
    const float dsk = bf2f((u16)pds);
    float kscale, kinv;
    {
      const float mx = fmaxf(fmaxf(kmax[0], kmax[1]), fmaxf(kmax[2], kmax[3]));
      int e = (int)((__float_as_uint(mx) >> 23) & 0xff);
      e = min(max(e, 20), 240);
      kscale = __uint_as_float((u32)(127 + 13 + 127 - e) << 23);
      kinv = __uint_as_float((u32)(e - 13) << 23);
    }
#pragma unroll
    for (int it = 0; it < 2; ++it) {
      const int tb = it ? 256 + (t2 & 3) : t2;
      u32 w[8];
      u32x4 a = uraw[tb], b = uraw[tb + 1];
#pragma unroll
      for (int j = 0; j < 4; ++j) { w[j] = a[j]; w[4 + j] = b[j]; }
      if (it == 0 || t2 < 4) {
#pragma unroll
        for (int c = 0; c < 8; ++c) {
          u32x4 o;
#pragma unroll
          for (int j = 0; j < 4; ++j)
            o[j] = (c & 1) ? w[j + (c + 1) / 2] : __builtin_amdgcn_alignbit(w[j + c / 2 + 1], w[j + c / 2], 16);
          *(u32x4*)(ucp + c * UC_LEN + tb * 8) = o;
        }
      }
    }
    {
      float kk[8];
#pragma unroll
      for (int j = 0; j < 8; ++j) {
        const int jj = 2047 - (t2 * 8 + j);
        const int cbi = (int)f2bf((float)jj) - 17024;
        kk[j] = kraw[jj < 128 ? jj : cbi] * kscale;
      }
      f16x8 o;
#pragma unroll
      for (int j = 0; j < 8; ++j) o[j] = (_Float16)kk[j];
      *(f16x8*)(kr + t2 * 8 + 8 * (t2 >> 2)) = o;
      if (t2 < 128) { const int tb = t2 + 256; *(u32x4*)(kr + tb * 8 + 8 * (tb >> 2)) = u32x4{0, 0, 0, 0}; }
    }
    asm volatile("s_waitcnt vmcnt(0)" ::: "memory");
    RAW_BAR();
    if (tid == 0) __hip_atomic_fetch_add((gu32*)(ws + WS_CNT + mt * 256), 1u, __ATOMIC_RELAXED, __HIP_MEMORY_SCOPE_AGENT);
    DIAG_STAMP(4);
    const int r = lane & 31, h = lane >> 5;
    const u16* ap = ucp + (r & 7) * UC_LEN + 8 * h + 8 * (r >> 3);
    const int bofs0 = 2048 - 32 * (r + 1) + 8 * h + 8 * (63 - r);
    const int bofs1 = bofs0 - 1024 - 256;
    f32x16 acc0 = {}, acc1 = {};
    unsigned hcnt = 0;
    {
      const u16* apw = ap + 16 * (w4 * 16);
      const u16* bp0 = kr + bofs0 + 16 * (w4 * 16) + 8 * (w4 * 8);
      const u16* bp1 = kr + bofs1 + 16 * (w4 * 16) + 8 * (w4 * 8);
      auto lda = [&](int s) -> f16x8 { return *(const f16x8*)(apw + 16 * s); };
      auto ldb = [&](const u16* b, int s) -> f16x8 { return *(const f16x8*)(b + 16 * s + 8 * (s >> 1)); };
      f16x8 a0 = lda(0), p0 = ldb(bp0, 0), q0 = ldb(bp1, 0), c0 = lda(64);
      f16x8 a1 = lda(1), p1 = ldb(bp0, 1), q1 = ldb(bp1, 1), c1 = lda(65);
#pragma unroll
      for (int it = 0; it < 8; ++it) {
        f16x8 na0, na1, nc0, nc1, np0, np1, nq0, nq1;
        if (it < 7) {
          const int sn = 2 * it + 2;
          na0 = lda(sn); np0 = ldb(bp0, sn); nq0 = ldb(bp1, sn); nc0 = lda(sn + 64);
          na1 = lda(sn + 1); np1 = ldb(bp0, sn + 1); nq1 = ldb(bp1, sn + 1); nc1 = lda(sn + 65);
#pragma unroll
          for (int i = 0; i < 4; ++i) wd[it][i] = G2_LD(wr, i, it, 0);
        }
        if (it == 7) hcnt = __hip_atomic_load((gu32*)(ws + WS_CNT + (mt2 >> 1) * 256), __ATOMIC_RELAXED, __HIP_MEMORY_SCOPE_AGENT);
        __builtin_amdgcn_sched_barrier(0);
        acc0 = __builtin_amdgcn_mfma_f32_32x32x16_f16(a0, p0, acc0, 0, 0, 0);
        acc1 = __builtin_amdgcn_mfma_f32_32x32x16_f16(a0, q0, acc1, 0, 0, 0);
        acc1 = __builtin_amdgcn_mfma_f32_32x32x16_f16(c0, p0, acc1, 0, 0, 0);
        acc0 = __builtin_amdgcn_mfma_f32_32x32x16_f16(a1, p1, acc0, 0, 0, 0);
        acc1 = __builtin_amdgcn_mfma_f32_32x32x16_f16(a1, q1, acc1, 0, 0, 0);
        acc1 = __builtin_amdgcn_mfma_f32_32x32x16_f16(c1, p1, acc1, 0, 0, 0);
        __builtin_amdgcn_sched_barrier(0);
        if (it < 7) { a0 = na0; a1 = na1; c0 = nc0; c1 = nc1; p0 = np0; p1 = np1; q0 = nq0; q1 = nq1; }
      }
    }
    DIAG_STAMP(5);
    RAW_BAR();
    float* red = (float*)cbase;
#pragma unroll
    for (int g = 0; g < 4; ++g) {
      f32x4 v0 = {acc0[4 * g], acc0[4 * g + 1], acc0[4 * g + 2], acc0[4 * g + 3]};
      f32x4 v1 = {acc1[4 * g], acc1[4 * g + 1], acc1[4 * g + 2], acc1[4 * g + 3]};
      *(f32x4*)(red + (w4 * 32 + r) * 36 + 8 * g + 4 * h) = v0;
      *(f32x4*)(red + ((4 + w4) * 32 + r) * 36 + 8 * g + 4 * h) = v1;
    }
    if (tid == 511 && hcnt < 16u) {
      gu32* cnt = (gu32*)(ws + WS_CNT + (mt2 >> 1) * 256);
      for (unsigned spins = 0; spins < (1u << 22); ++spins) {
        if (__hip_atomic_load(cnt, __ATOMIC_RELAXED, __HIP_MEMORY_SCOPE_AGENT) >= 16u) break;
        __builtin_amdgcn_s_sleep(1);
      }
    }
    RAW_BAR();
    DIAG_STAMP2(1);
    u16* at = (u16*)(ws + WS_AT) + (size_t)d * L_;
    {
      const int c = t2;
      const int T = c >> 7, a = (c >> 2) & 31, b0 = (c & 3) * 8;
      f32x4 s0 = {}, s1 = {};
      const float* p = red + (T * 4 * 32 + a) * 36 + b0;
#pragma unroll
      for (int w = 0; w < 4; ++w) { s0 += *(const f32x4*)(p + w * 32 * 36); s1 += *(const f32x4*)(p + w * 32 * 36 + 4); }
      u32x4 o;
#pragma unroll
      for (int j = 0; j < 4; ++j) {
        const f2 y = unpack2((j < 2) ? pack2(s0[2 * j] * kinv, s0[2 * j + 1] * kinv) : pack2(s1[2 * j - 4] * kinv, s1[2 * j - 3] * kinv));
        const f2 us = unpack2(uv0[j]) * dsk;
        const f2 t = unpack2(pack2(us.x, us.y)) + y;
        u32 ov = pack2(t.x, t.y);
        if ((ov & 0xffffu) == 0xffffu) ov &= 0xffffffc0u;
        if ((ov >> 16) == 0xffffu) ov &= 0xffc0ffffu;
        o[j] = ov;
      }
      st16_wt(at + c * 8, o);
    }
  }
  DIAG_STAMP2(2);

  {
    f32x4 acc[4][4] = {};
    bf16x8 hb[4][4];
#pragma unroll
    for (int k = 0; k < 4; ++k)
#pragma unroll
      for (int i = 0; i < 4; ++i) hb[k][i] = G2_LD(hr, i, k, 16);
    const int m = tid >> 3, n0 = (tid & 7) * 8;
    const size_t go = (size_t)(mt2 * 64 + m) * D_ + nt2 * 64 + n0;
    const u32x4 xv = *(const u32x4*)(x + go);
    __builtin_amdgcn_sched_barrier(0);
#pragma unroll
    for (int k = 0; k < 7; ++k) {
#pragma unroll
      for (int j = 0; j < 4; ++j)
#pragma unroll
        for (int i = 0; i < 4; ++i) acc[j][i] = __builtin_amdgcn_mfma_f32_16x16x32_bf16(wd[k][j], hb[k & 3][i], acc[j][i], 0, 0, 0);
      if (k < 3) {
#pragma unroll
        for (int i = 0; i < 4; ++i) hb[k][i] = G2_LD(hr, i, k + 4, 16);
      }
      __builtin_amdgcn_sched_barrier(0);
    }
    DIAG_STAMP2(3);
    u32 a0, a1, a2, a3, a4, a5, a6, a7;
    const u16* at = (const u16*)(ws + WS_AT) + (size_t)(nt2 * 64 + n0) * L_ + mt2 * 64 + m;
#define A_LOADS(tail)                                                                                              \
  asm volatile("global_load_ushort %0, %8, off sc1\n\t"                                                            \
               "global_load_ushort %1, %9, off sc1\n\t"                                                            \
               "global_load_ushort %2, %10, off sc1\n\t"                                                           \
               "global_load_ushort %3, %11, off sc1\n\t"                                                           \
               "global_load_ushort %4, %12, off sc1\n\t"                                                           \
               "global_load_ushort %5, %13, off sc1\n\t"                                                           \
               "global_load_ushort %6, %14, off sc1\n\t"                                                           \
               "global_load_ushort %7, %15, off sc1" tail                                                          \
               : "=&v"(a0), "=&v"(a1), "=&v"(a2), "=&v"(a3), "=&v"(a4), "=&v"(a5), "=&v"(a6), "=&v"(a7)            \
               : "v"(at), "v"(at + L_), "v"(at + 2 * L_), "v"(at + 3 * L_), "v"(at + 4 * L_), "v"(at + 5 * L_),   \
                 "v"(at + 6 * L_), "v"(at + 7 * L_)                                                                \
               : "memory")
    A_LOADS("");
    RAW_BAR();
    float* red = (float*)smem;
    float* rp = red + (wid * 64 + (lane & 15)) * 68 + 4 * (lane >> 4);
#pragma unroll
    for (int j = 0; j < 4; ++j)
#pragma unroll
      for (int i = 0; i < 4; ++i) *(f32x4*)(rp + i * 16 * 68 + j * 16) = acc[j][i];
    asm volatile("s_waitcnt vmcnt(0)" : "+v"(a0), "+v"(a1), "+v"(a2), "+v"(a3), "+v"(a4), "+v"(a5), "+v"(a6), "+v"(a7)::"memory");
    for (unsigned spins = 0; spins < (1u << 20); ++spins) {
      const bool pend = (a0 == 0xffffu) | (a1 == 0xffffu) | (a2 == 0xffffu) | (a3 == 0xffffu) | (a4 == 0xffffu) |
                        (a5 == 0xffffu) | (a6 == 0xffffu) | (a7 == 0xffffu);
      if (!__any(pend)) break;
      __builtin_amdgcn_s_sleep(2);
      A_LOADS("\n\ts_waitcnt vmcnt(0)");
    }
    RAW_BAR();
    DIAG_STAMP2(4);
    f32x4 p0 = {}, p1 = {};
#pragma unroll
    for (int w = 0; w < 8; ++w) {
      p0 += *(const f32x4*)(red + (w * 64 + m) * 68 + n0);
      p1 += *(const f32x4*)(red + (w * 64 + m) * 68 + n0 + 4);
    }
    const u32 av[8] = {a0, a1, a2, a3, a4, a5, a6, a7};
    u32x4 o;
#pragma unroll
    for (int j = 0; j < 4; ++j) {
      const f2 xx = unpack2(xv[j]);
      const f2 aa = f2{__uint_as_float(av[2 * j] << 16), __uint_as_float(av[2 * j + 1] << 16)};
      const f2 r1 = xx + aa;
      const f2 bb = unpack2((j < 2) ? pack2(p0[2 * j], p0[2 * j + 1]) : pack2(p1[2 * j - 4], p1[2 * j - 3]));
      const f2 r2 = unpack2(pack2(r1.x, r1.y)) + bb;
      o[j] = pack2(r2.x, r2.y);
    }
    *(u32x4*)(out + go) = o;
  }
}

extern "C" void kernel_launch(void* const* d_in, const int* in_sizes, int n_in, void* d_out, int out_size,
                              void* d_ws, size_t ws_size, hipStream_t stream) {
  const u16* x = (const u16*)d_in[0];
  const u16* logA = (const u16*)d_in[1];
  const u16* Aim = (const u16*)d_in[2];
  const u16* Cre = (const u16*)d_in[3];
  const u16* Cim = (const u16*)d_in[4];
  const u16* logdt = (const u16*)d_in[5];
  const u16* dskip = (const u16*)d_in[6];
  const u16* wg = (const u16*)d_in[8];
  const u16* wu = (const u16*)d_in[9];
  const u16* wd = (const u16*)d_in[10];
  char* ws = (char*)d_ws;
  (void)hipFuncSetAttribute((const void*)k2_kernel, hipFuncAttributeMaxDynamicSharedMemorySize, K2_LDS);
  k1_kernel<<<K1_LN_BLOCKS + K1_WT_BLOCKS + K1_KG_BLOCKS, 256, 0, stream>>>(x, logA, Aim, Cre, Cim, logdt, wg, wu, wd, ws);
  k2_kernel<<<256, 512, K2_LDS, stream>>>(x, dskip, ws, (u16*)d_out);
}
```

```cpp
#include <hip/hip_runtime.h>
#include <hip/hip_bf16.h>
#define WT_STORES
#define CV_PRIO 1
#define G2_PRIO 3

typedef unsigned short u16;
typedef unsigned int u32;
using bf16x8 = __attribute__((ext_vector_type(8))) short;
using f16x8 = __attribute__((ext_vector_type(8))) _Float16;
using f16x4 = __attribute__((ext_vector_type(4))) _Float16;
using f32x4 = __attribute__((ext_vector_type(4))) float;
using f32x16 = __attribute__((ext_vector_type(16))) float;
using u32x4 = __attribute__((ext_vector_type(4))) unsigned int;
using u32x2 = __attribute__((ext_vector_type(2))) unsigned int;
typedef float f2 __attribute__((ext_vector_type(2)));
typedef __attribute__((address_space(1))) unsigned gu32;

constexpr int L_ = 2048, D_ = 512, N_ = 128, F_ = 1792;
constexpr int KC_STRIDE = 648;

constexpr size_t WS_CNT = 0;
constexpr size_t WS_XNT = 4096;
constexpr size_t WS_XNTR = WS_XNT + (size_t)L_ * D_ * 2;
constexpr size_t WS_KC = WS_XNTR + (size_t)L_ * D_ * 2;
constexpr size_t WS_H = WS_KC + (size_t)D_ * KC_STRIDE * 4;
constexpr size_t WS_WG = WS_H + (size_t)L_ * F_ * 2;
constexpr size_t WS_WU = WS_WG + (size_t)D_ * F_ * 2;
constexpr size_t WS_WD = WS_WU + (size_t)D_ * F_ * 2;
constexpr size_t WS_AT = WS_WD + (size_t)D_ * F_ * 2;

#define DIAG_START(k)
#define DIAG_STAMP(i)
#define DIAG_END(wsp)
#define DIAG_STAMP2(i)

__device__ __forceinline__ float bf2f(u16 v) { return __uint_as_float(((u32)v) << 16); }
__device__ __forceinline__ u16 f2bf(float f) {
  __bf16 b = (__bf16)f;
  return __builtin_bit_cast(u16, b);
}
__device__ __forceinline__ float rbf(float f) { return bf2f(f2bf(f)); }
__device__ __forceinline__ u32 pack2(float lo, float hi) {
  typedef __bf16 b2 __attribute__((ext_vector_type(2)));
  b2 r = __builtin_convertvector(f2{lo, hi}, b2);
  return __builtin_bit_cast(u32, r);
}
__device__ __forceinline__ f2 unpack2(u32 w) { return f2{__uint_as_float(w << 16), __uint_as_float(w & 0xffff0000u)}; }

__device__ __forceinline__ void st16_wt(void* p, u32x4 v) { asm volatile("global_store_dwordx4 %0, %1, off sc1\n\ts_nop 1" ::"v"(p), "v"(v) : "memory"); }
__device__ __forceinline__ void st16(void* p, u32x4 v) { st16_wt(p, v); }
__device__ __forceinline__ void st8(void* p, u32x2 v) { asm volatile("global_store_dwordx2 %0, %1, off sc1\n\ts_nop 1" ::"v"(p), "v"(v) : "memory"); }

#define DPP_F(v, ctrl) __builtin_bit_cast(float, __builtin_amdgcn_update_dpp(0, __builtin_bit_cast(int, v), ctrl, 0xf, 0xf, true))
__device__ __forceinline__ float wave_sum(float v) {
  v += DPP_F(v, 0xB1);
  v += DPP_F(v, 0x4E);
  v += DPP_F(v, 0x141);
  v += DPP_F(v, 0x140);
  const int iv = __builtin_bit_cast(int, v);
  return (__builtin_bit_cast(float, __builtin_amdgcn_readlane(iv, 0)) + __builtin_bit_cast(float, __builtin_amdgcn_readlane(iv, 16))) +
         (__builtin_bit_cast(float, __builtin_amdgcn_readlane(iv, 32)) + __builtin_bit_cast(float, __builtin_amdgcn_readlane(iv, 48)));
}
__device__ __forceinline__ float wave_max(float v) {
  v = fmaxf(v, DPP_F(v, 0xB1));
  v = fmaxf(v, DPP_F(v, 0x4E));
  v = fmaxf(v, DPP_F(v, 0x141));
  v = fmaxf(v, DPP_F(v, 0x140));
  const int iv = __builtin_bit_cast(int, v);
  return fmaxf(fmaxf(__builtin_bit_cast(float, __builtin_amdgcn_readlane(iv, 0)), __builtin_bit_cast(float, __builtin_amdgcn_readlane(iv, 16))),
               fmaxf(__builtin_bit_cast(float, __builtin_amdgcn_readlane(iv, 32)), __builtin_bit_cast(float, __builtin_amdgcn_readlane(iv, 48))));
}

constexpr int K1_LN_BLOCKS = 128;
constexpr int K1_WT_BLOCKS = 336;
constexpr int K1_KG_BLOCKS = 512;

__device__ __forceinline__ void cmul6(f2& r0, f2& r1, f2& r2, f2& r3, f2& r4, f2& r5, f2 a0, f2 b0, f2 a1, f2 b1,
                                      f2 a2, f2 b2, f2 a3, f2 b3, f2 a4, f2 b4, f2 a5, f2 b5) {
  asm("v_pk_mul_f32 %0, %6, %7 op_sel:[1,1] op_sel_hi:[1,0] neg_lo:[0,1]\n\t"
      "v_pk_mul_f32 %1, %8, %9 op_sel:[1,1] op_sel_hi:[1,0] neg_lo:[0,1]\n\t"
      "v_pk_mul_f32 %2, %10, %11 op_sel:[1,1] op_sel_hi:[1,0] neg_lo:[0,1]\n\t"
      "v_pk_mul_f32 %3, %12, %13 op_sel:[1,1] op_sel_hi:[1,0] neg_lo:[0,1]\n\t"
      "v_pk_mul_f32 %4, %14, %15 op_sel:[1,1] op_sel_hi:[1,0] neg_lo:[0,1]\n\t"
      "v_pk_mul_f32 %5, %16, %17 op_sel:[1,1] op_sel_hi:[1,0] neg_lo:[0,1]\n\t"
      "v_pk_fma_f32 %0, %6, %7, %0 op_sel:[0,0,0] op_sel_hi:[0,1,1]\n\t"
      "v_pk_fma_f32 %1, %8, %9, %1 op_sel:[0,0,0] op_sel_hi:[0,1,1]\n\t"
      "v_pk_fma_f32 %2, %10, %11, %2 op_sel:[0,0,0] op_sel_hi:[0,1,1]\n\t"
      "v_pk_fma_f32 %3, %12, %13, %3 op_sel:[0,0,0] op_sel_hi:[0,1,1]\n\t"
      "v_pk_fma_f32 %4, %14, %15, %4 op_sel:[0,0,0] op_sel_hi:[0,1,1]\n\t"
      "v_pk_fma_f32 %5, %16, %17, %5 op_sel:[0,0,0] op_sel_hi:[0,1,1]\n\t"
      "s_nop 1"
      : "=&v"(r0), "=&v"(r1), "=&v"(r2), "=&v"(r3), "=&v"(r4), "=&v"(r5)
      : "v"(a0), "v"(b0), "v"(a1), "v"(b1), "v"(a2), "v"(b2), "v"(a3), "v"(b3), "v"(a4), "v"(b4), "v"(a5), "v"(b5));
}
__device__ __forceinline__ void cmul2(f2& r0, f2& r1, f2 a0, f2 b0, f2 a1, f2 b1) {
  asm("v_pk_mul_f32 %0, %2, %3 op_sel:[1,1] op_sel_hi:[1,0] neg_lo:[0,1]\n\t"
      "v_pk_mul_f32 %1, %4, %5 op_sel:[1,1] op_sel_hi:[1,0] neg_lo:[0,1]\n\t"
      "s_nop 0\n\t"
      "v_pk_fma_f32 %0, %2, %3, %0 op_sel:[0,0,0] op_sel_hi:[0,1,1]\n\t"
      "v_pk_fma_f32 %1, %4, %5, %1 op_sel:[0,0,0] op_sel_hi:[0,1,1]\n\t"
      "s_nop 1"
      : "=&v"(r0), "=&v"(r1)
      : "v"(a0), "v"(b0), "v"(a1), "v"(b1));
}
__device__ __forceinline__ f2 cexp_(float zr, float zi, float t) {
  const float m = __builtin_amdgcn_exp2f((zr * t) * 1.44269504088896341f);
  float r = (zi * t) * 0.15915494309189535f;
  r -= rintf(r);
  return f2{m * __builtin_amdgcn_cosf(r), m * __builtin_amdgcn_sinf(r)};
}

__global__ __launch_bounds__(256) void k1_kernel(const u16* __restrict__ x, const u16* __restrict__ logA,
                                                 const u16* __restrict__ Aim, const u16* __restrict__ Cre,
                                                 const u16* __restrict__ Cim, const u16* __restrict__ logdt,
                                                 const u16* __restrict__ wg, const u16* __restrict__ wu,
                                                 const u16* __restrict__ wd, char* __restrict__ ws) {
  __shared__ __attribute__((aligned(16))) char smem[18432];
  const int tid = threadIdx.x, lane = tid & 63, wid = tid >> 6;
  DIAG_START(0)
  int bid = blockIdx.x - K1_KG_BLOCKS;
  if (bid >= 0 && bid < K1_LN_BLOCKS) {
    __builtin_amdgcn_s_setprio(3);
    if (bid == 0 && tid < 16) *(u32*)(ws + WS_CNT + tid * 256) = 0u;
#pragma unroll
    for (int it = 0; it < 4; ++it) st16(ws + WS_AT + ((size_t)bid * 1024 + it * 256 + tid) * 16, u32x4{0xffffffffu, 0xffffffffu, 0xffffffffu, 0xffffffffu});
    u16* tile = (u16*)smem;
    u16* xnt = (u16*)(ws + WS_XNT);
    const int r0 = bid * 16;
    u32x4 v[4];
#pragma unroll
    for (int i = 0; i < 4; ++i) v[i] = *(const u32x4*)(x + (size_t)(r0 + wid * 4 + i) * D_ + lane * 8);
#pragma unroll
    for (int i = 0; i < 4; ++i) {
      const int rl = wid * 4 + i;
      f2 f[4];
#pragma unroll
      for (int j = 0; j < 4; ++j) f[j] = unpack2(v[i][j]);
      const f2 s2 = (f[0] + f[1]) + (f[2] + f[3]);
      const float mean = wave_sum(s2.x + s2.y) * (1.f / D_);
      f2 q2 = {0.f, 0.f};
#pragma unroll
      for (int j = 0; j < 4; ++j) { const f2 t = f[j] - mean; q2 = __builtin_elementwise_fma(t, t, q2); }
      const float q = wave_sum(q2.x + q2.y);
      const float mb = rbf(mean);
      const float vb = rbf(q * (1.f / D_));
      const float sd = rbf(sqrtf(rbf(vb + 1e-5f)));
      const float rs = __builtin_amdgcn_rcpf(sd);
      u32x4 o;
#pragma unroll
      for (int j = 0; j < 4; ++j) {
        const f2 t = f[j] - mb;
        const f2 a = unpack2(pack2(t.x, t.y));
        f2 qq = a * rs;
        const f2 rem = __builtin_elementwise_fma(-qq, f2{sd, sd}, a);
        qq = __builtin_elementwise_fma(rem, f2{rs, rs}, qq);
        o[j] = pack2(qq.x, qq.y);
      }
      *(u32x4*)(tile + rl * 520 + lane * 8) = o;
    }
    DIAG_STAMP(1);
    __syncthreads();
#pragma unroll
    for (int it = 0; it < 4; ++it) {
      const int c = tid + it * 256;
      const u32x4 o = *(const u32x4*)(tile + (c & 15) * 520 + (c >> 4) * 8);
      st16(xnt + ((size_t)bid * 1024 + c) * 8, o);
    }
    u16* xtr = (u16*)(ws + WS_XNTR);
#pragma unroll
    for (int it = 0; it < 4; ++it) {
      const int c = tid + it * 256;
      const int d = c >> 1, g = c & 1;
      u32x4 o;
#pragma unroll
      for (int j = 0; j < 4; ++j)
        o[j] = (u32)tile[(g * 8 + 2 * j) * 520 + d] | ((u32)tile[(g * 8 + 2 * j + 1) * 520 + d] << 16);
      st16(xtr + ((size_t)bid * 1024 + c) * 8, o);
    }
    DIAG_END(ws);
    return;
  }
  bid -= K1_LN_BLOCKS;
  if (bid >= 0) {
    __builtin_amdgcn_s_setprio(2);
    int task = bid * 4 + wid;
    const u16* W; u16* Wt; int Kd, Nd;
    if (task < 448) { W = wg; Wt = (u16*)(ws + WS_WG); Kd = D_; Nd = F_; }
    else if (task < 896) { task -= 448; W = wu; Wt = (u16*)(ws + WS_WU); Kd = D_; Nd = F_; }
    else { task -= 896; W = wd; Wt = (u16*)(ws + WS_WD); Kd = F_; Nd = D_; }
    const int nks = Kd / 32;
    const int ks = task % nks, nb = task / nks;
    const int col = nb * 64 + lane;
    u16 v[32];
#pragma unroll
    for (int k = 0; k < 32; ++k) v[k] = W[(size_t)(ks * 32 + k) * Nd + col];
#pragma unroll
    for (int q = 0; q < 4; ++q) {
      u32x4 o;
#pragma unroll
      for (int j = 0; j < 4; ++j) o[j] = (u32)v[q * 8 + 2 * j] | ((u32)v[q * 8 + 2 * j + 1] << 16);
      st16(Wt + ((size_t)((col >> 4) * nks + ks) * 64 + q * 16 + (col & 15)) * 8, o);
    }
    DIAG_END(ws);
    return;
  }
  bid = blockIdx.x;
  {
    const int d = bid;
    float4* prm = (float4*)(smem + 16384);
    if (tid < 128) {
      const u16 la = logA[d * N_ + tid], ai = Aim[d * N_ + tid], cr = Cre[d * N_ + tid], ci = Cim[d * N_ + tid];
      const float dt = rbf(expf(bf2f(logdt[d])));
      const float ar = -rbf(expf(bf2f(la)));
      prm[tid] = make_float4(ar * dt, bf2f(ai) * dt, bf2f(cr) * dt, bf2f(ci) * dt);
    }
    __syncthreads();
    DIAG_STAMP(1);
    const int kq = lane >> 4, idx = lane & 15;
    f32x4 acc[4] = {};
    const float ty = (float)idx, te = (float)(idx == 0 ? 256 : 16 * idx);
    float4 pr[8];
#pragma unroll
    for (int s = 0; s < 8; ++s) pr[s] = prm[wid * 32 + s * 4 + kq];
#pragma unroll
    for (int s = 0; s < 8; s += 2) {
      const float4 pa = pr[s], pb = pr[s + 1];
      const f2 Ca = f2{pa.z, -pa.w}, Cb = f2{pb.z, -pb.w};
      f2 Ya[4], Yb[4], Xa[4], Xb[4];
      Ya[0] = cexp_(pa.x, pa.y, ty); Yb[0] = cexp_(pb.x, pb.y, ty);
      f2 Ea = cexp_(pa.x, -pa.y, te), Eb = cexp_(pb.x, -pb.y, te);
      f2 Fa = (idx == 0) ? f2{1.f, 0.f} : Ea, Fb = (idx == 0) ? f2{1.f, 0.f} : Eb;
#pragma unroll
      for (int e = 0; e < 3; ++e) {
        f2 nEa, nEb;
        cmul6(Xa[e], Xb[e], Ya[e + 1], Yb[e + 1], nEa, nEb, Ca, Fa, Cb, Fb, Ya[e], Ya[e], Yb[e], Yb[e], Ea, Ea, Eb, Eb);
        Ea = nEa; Eb = nEb; Fa = nEa; Fb = nEb;
      }
      cmul2(Xa[3], Xb[3], Ca, Ea, Cb, Eb);
#pragma unroll
      for (int e = 0; e < 4; ++e) acc[e] = __builtin_amdgcn_mfma_f32_16x16x4f32(Xa[e].x, Ya[e].x, acc[e], 0, 0, 0);
#pragma unroll
      for (int e = 0; e < 4; ++e) acc[e] = __builtin_amdgcn_mfma_f32_16x16x4f32(Xa[e].y, Ya[e].y, acc[e], 0, 0, 0);
#pragma unroll
      for (int e = 0; e < 4; ++e) acc[e] = __builtin_amdgcn_mfma_f32_16x16x4f32(Xb[e].x, Yb[e].x, acc[e], 0, 0, 0);
#pragma unroll
      for (int e = 0; e < 4; ++e) acc[e] = __builtin_amdgcn_mfma_f32_16x16x4f32(Xb[e].y, Yb[e].y, acc[e], 0, 0, 0);
    }
    DIAG_STAMP(2);
    float* red = (float*)smem;
#pragma unroll
    for (int e = 0; e < 4; ++e)
#pragma unroll
      for (int r = 0; r < 4; ++r) red[(wid * 4 + e) * 256 + ((lane >> 4) * 4 + r) * 16 + (lane & 15)] = acc[e][r];
    __syncthreads();
    float* kc = (float*)(ws + WS_KC) + (size_t)d * KC_STRIDE;
#pragma unroll
    for (int e = 0; e < 4; ++e) {
      const float v = 2.f * ((red[(0 * 4 + e) * 256 + tid] + red[(1 * 4 + e) * 256 + tid]) +
                             (red[(2 * 4 + e) * 256 + tid] + red[(3 * 4 + e) * 256 + tid]));
      if (e == 0) kc[tid] = v;
      else if (tid >= 128) kc[128 * e + tid] = v;
      else if (e == 3 && tid == 0) kc[640] = v;
    }
    DIAG_END(ws);
  }
}

constexpr int G1_STAGE = 22 * 1024;
constexpr int G1_NBUF = 7;
constexpr int G1_PRE = 3;
constexpr int K2_LDS = G1_STAGE * G1_NBUF;

constexpr int UC_LEN = 2080;
constexpr int KR_LEN = 3840;
constexpr int CV_BASE = G1_PRE * G1_STAGE;
constexpr int CV_UCP = 0;
constexpr int CV_KR = CV_UCP + 8 * UC_LEN * 2;
constexpr int CV_URAW = CV_UCP + 6 * UC_LEN * 2;
constexpr int CV_KRAW = CV_URAW + 4224;
constexpr int CV_MAX = CV_KRAW + 2592;
constexpr int CV_CH_STRIDE = 40960;
constexpr int H_STAGE = 0;
constexpr int CV_RED = 40960;

__device__ __forceinline__ f2 gelu2(f2 g) {
  const float c0 = -2.f * 0.7978845608028654f * 1.44269504088896341f;
  const float c1 = c0 * 0.044715f;
  const f2 t = g * __builtin_elementwise_fma(g * g, f2{c1, c1}, f2{c0, c0});
  const f2 dn = f2{__builtin_amdgcn_exp2f(t.x), __builtin_amdgcn_exp2f(t.y)} + 1.f;
  return g * f2{__builtin_amdgcn_rcpf(dn.x), __builtin_amdgcn_rcpf(dn.y)};
}
__device__ __forceinline__ void glds16(const void* g, void* l) {
  __builtin_amdgcn_global_load_lds((const __attribute__((address_space(1))) u32*)g,
                                   (__attribute__((address_space(3))) u32*)l, 16, 0, 0);
}
__device__ __forceinline__ u32 bf2_to_h2(u32 w) {
  auto r = __builtin_amdgcn_cvt_pkrtz(__uint_as_float(w << 16), __uint_as_float(w & 0xffff0000u));
  return __builtin_bit_cast(u32, r);
}
#define RAW_BAR() do { asm volatile("s_waitcnt lgkmcnt(0)" ::: "memory"); __builtin_amdgcn_s_barrier(); } while (0)

__global__ __launch_bounds__(512, 2) void k2_kernel(const u16* __restrict__ x, const u16* __restrict__ dskip,
                                                    char* __restrict__ ws, u16* __restrict__ out) {
  extern __shared__ __attribute__((aligned(16))) char smem[];
  const int tid = threadIdx.x, lane = tid & 63;
  const int wid = __builtin_amdgcn_readfirstlane(tid >> 6);
  DIAG_START(1)
  const int bid = blockIdx.x;
  const int xcd = bid & 7, loc = bid >> 3;
  const int ch = wid >> 2, w4 = wid & 3;
  const int d = bid * 2 + ch;
  const int t2 = tid & 255;
  const int mt = (xcd >> 1) * 4 + (loc & 3);
  const int nt = (xcd & 1) * 8 + (loc >> 2);

  u32x4 uv0; float kv0, kv1, kv2; u32 pds;
  {
    const u16* ug = (const u16*)(ws + WS_XNTR) + (size_t)(t2 >> 1) * 8192 + d * 16 + (t2 & 1) * 8;
    const float* kc = (const float*)(ws + WS_KC) + (size_t)d * KC_STRIDE + t2;
    asm volatile(
        "global_load_dwordx4 %0, %5, off\n\t"
        "global_load_dword %1, %6, off\n\t"
        "global_load_dword %2, %6, off offset:1024\n\t"
        "global_load_dword %3, %7, off\n\t"
        "global_load_ushort %4, %8, off"
        : "=&v"(uv0), "=&v"(kv0), "=&v"(kv1), "=&v"(kv2), "=&v"(pds)
        : "v"(ug), "v"(kc), "v"(kc + (t2 < 129 ? 512 : 0)), "v"(dskip + d)
        : "memory");
  }
  const u16* xnt = (const u16*)(ws + WS_XNT);
  const u16* wgt = (const u16*)(ws + WS_WG);
  const u16* wut = (const u16*)(ws + WS_WU);
  auto src = [&](int f) -> const u16* {
    if (f < 8) return xnt + (size_t)(mt * 8 + f) * 16 * 512;
    const int s = f - 8;
    return ((s & 1) ? wut : wgt) + (size_t)(nt * 7 + (s >> 1)) * 16 * 512;
  };
  const u16* s0 = src(wid) + lane * 8;
  const u16* s1 = src(wid + 8) + lane * 8;
  const u16* s2 = src(wid < 6 ? wid + 16 : 21) + lane * 8;
  auto stage = [&](int ks) {
    char* base = smem + (ks % G1_NBUF) * G1_STAGE + wid * 1024 + lane * 16;
    glds16(s0 + ks * 512, base);
    glds16(s1 + ks * 512, base + 8192);
    if (wid < 6) glds16(s2 + ks * 512, base + 16384);
  };
#define G1_WAIT(n) do { if (wid < 6) asm volatile("s_waitcnt vmcnt(%0)" ::"n"(3 * (n)) : "memory"); \
                        else asm volatile("s_waitcnt vmcnt(%0)" ::"n"(2 * (n)) : "memory"); } while (0)
#pragma unroll
  for (int k = 0; k < G1_PRE; ++k) stage(k);
  G1_WAIT(G1_PRE);
  asm volatile("" : "+v"(uv0), "+v"(kv0), "+v"(kv1), "+v"(kv2), "+v"(pds));
  if (t2 >= 129) kv2 = 0.f;

  f32x16 acc0 = {}, acc1 = {};
  float kinv;
  {
    char* cbase = smem + CV_BASE + ch * CV_CH_STRIDE;
    u16* ucp = (u16*)(cbase + CV_UCP);
    u16* kr = (u16*)(cbase + CV_KR);
    u32x4* uraw = (u32x4*)(cbase + CV_URAW);
    float* kraw = (float*)(cbase + CV_KRAW);
    float* kmax = (float*)(cbase + CV_MAX);
    if (t2 < 4) { uraw[t2] = u32x4{0, 0, 0, 0}; uraw[260 + t2] = u32x4{0, 0, 0, 0}; }
    {
      u32x4 h;
#pragma unroll
      for (int j = 0; j < 4; ++j) h[j] = bf2_to_h2(uv0[j]);
      uraw[4 + t2] = h;
    }
    kraw[t2] = kv0;
    kraw[t2 + 256] = kv1;
    if (t2 < 136) kraw[t2 + 512] = kv2;
    {
      const float mx = wave_max(fmaxf(fmaxf(fabsf(kv0), fabsf(kv1)), fabsf(kv2)));
      if (lane == 0) kmax[w4] = mx;
    }
    RAW_BAR();
    DIAG_STAMP(1);
    float kscale;
    {
      const float mx = fmaxf(fmaxf(kmax[0], kmax[1]), fmaxf(kmax[2], kmax[3]));
      int e = (int)((__float_as_uint(mx) >> 23) & 0xff);
      e = min(max(e, 20), 240);
      kscale = __uint_as_float((u32)(127 + 13 + 127 - e) << 23);
      kinv = __uint_as_float((u32)(e - 13) << 23);
    }
    u32x4 c6[2], c7[2];
#pragma unroll
    for (int it = 0; it < 2; ++it) {
      const int tb = it ? 256 + (t2 & 3) : t2;
      u32 w[8];
      u32x4 a = uraw[tb], b = uraw[tb + 1];
#pragma unroll
      for (int j = 0; j < 4; ++j) { w[j] = a[j]; w[4 + j] = b[j]; }
#pragma unroll
      for (int c = 0; c < 8; ++c) {
        u32x4 o;
#pragma unroll
        for (int j = 0; j < 4; ++j)
          o[j] = (c & 1) ? w[j + (c + 1) / 2] : __builtin_amdgcn_alignbit(w[j + c / 2 + 1], w[j + c / 2], 16);
        if (c < 6) { if (it == 0 || t2 < 4) *(u32x4*)(ucp + c * UC_LEN + tb * 8) = o; }
        else if (c == 6) c6[it] = o;
        else c7[it] = o;
      }
    }
    {
      float kk[8];
#pragma unroll
      for (int j = 0; j < 8; ++j) {
        const int jj = 2047 - (t2 * 8 + j);
        const int cbi = (int)f2bf((float)jj) - 17024;
        kk[j] = kraw[jj < 128 ? jj : cbi] * kscale;
      }
      f16x8 o;
#pragma unroll
      for (int j = 0; j < 8; ++j) o[j] = (_Float16)kk[j];
      *(f16x8*)(kr + t2 * 8 + 8 * (t2 >> 2)) = o;
      if (t2 < 128) { const int tb = t2 + 256; *(u32x4*)(kr + tb * 8 + 8 * (tb >> 2)) = u32x4{0, 0, 0, 0}; }
    }
    RAW_BAR();
    *(u32x4*)(ucp + 6 * UC_LEN + t2 * 8) = c6[0];
    *(u32x4*)(ucp + 7 * UC_LEN + t2 * 8) = c7[0];
    if (t2 < 4) {
      *(u32x4*)(ucp + 6 * UC_LEN + (256 + t2) * 8) = c6[1];
      *(u32x4*)(ucp + 7 * UC_LEN + (256 + t2) * 8) = c7[1];
    }
    RAW_BAR();
    DIAG_STAMP(2);
    const int r = lane & 31, h = lane >> 5;
    const u16* ap = ucp + (r & 7) * UC_LEN + 8 * h + 8 * (r >> 3);
    const int bofs0 = 2048 - 32 * (r + 1) + 8 * h + 8 * (63 - r);
    const int bofs1 = bofs0 - 1024 - 256;
    const u16* apw = ap + 16 * (w4 * 16);
    const u16* bp0 = kr + bofs0 + 16 * (w4 * 16) + 8 * (w4 * 8);
    const u16* bp1 = kr + bofs1 + 16 * (w4 * 16) + 8 * (w4 * 8);
    auto lda = [&](int s) -> f16x8 { return *(const f16x8*)(apw + 16 * s); };
    auto ldb = [&](const u16* b, int s) -> f16x8 { return *(const f16x8*)(b + 16 * s + 8 * (s >> 1)); };
    f16x8 a0 = lda(0), p0 = ldb(bp0, 0), q0 = ldb(bp1, 0), c0 = lda(64);
    f16x8 a1 = lda(1), p1 = ldb(bp0, 1), q1 = ldb(bp1, 1), c1 = lda(65);
#pragma unroll
    for (int it = 0; it < 8; ++it) {
      f16x8 na0, na1, nc0, nc1, np0, np1, nq0, nq1;
      if (it < 7) {
        const int sn = 2 * it + 2;
        na0 = lda(sn); np0 = ldb(bp0, sn); nq0 = ldb(bp1, sn); nc0 = lda(sn + 64);
        na1 = lda(sn + 1); np1 = ldb(bp0, sn + 1); nq1 = ldb(bp1, sn + 1); nc1 = lda(sn + 65);
      }
      __builtin_amdgcn_sched_barrier(0);
      acc0 = __builtin_amdgcn_mfma_f32_32x32x16_f16(a0, p0, acc0, 0, 0, 0);
      acc1 = __builtin_amdgcn_mfma_f32_32x32x16_f16(a0, q0, acc1, 0, 0, 0);
      acc1 = __builtin_amdgcn_mfma_f32_32x32x16_f16(c0, p0, acc1, 0, 0, 0);
      acc0 = __builtin_amdgcn_mfma_f32_32x32x16_f16(a1, p1, acc0, 0, 0, 0);
      acc1 = __builtin_amdgcn_mfma_f32_32x32x16_f16(a1, q1, acc1, 0, 0, 0);
      acc1 = __builtin_amdgcn_mfma_f32_32x32x16_f16(c1, p1, acc1, 0, 0, 0);
      __builtin_amdgcn_sched_barrier(0);
      if (it < 7) { a0 = na0; a1 = na1; c0 = nc0; c1 = nc1; p0 = np0; p1 = np1; q0 = nq0; q1 = nq1; }
    }
  }
  RAW_BAR();
  DIAG_STAMP(3);
#pragma unroll
  for (int k = G1_PRE; k < G1_NBUF; ++k) stage(k);

  const int mt2 = (xcd >> 1) * 8 + (xcd & 1) * 4 + (loc & 3);
  const int nt2 = loc >> 2;
  const int vo = lane * 16;
  const __amdgpu_buffer_rsrc_t hr = __builtin_amdgcn_make_buffer_rsrc((void*)((const u16*)(ws + WS_H) + ((size_t)(mt2 * 4) * 56 + wid * 7) * 512), 0, 4 * 56 * 1024, 0x00020000);
  const __amdgpu_buffer_rsrc_t wr = __builtin_amdgcn_make_buffer_rsrc((void*)((const u16*)(ws + WS_WD) + ((size_t)(nt2 * 4) * 56 + wid * 7) * 512), 0, 4 * 56 * 1024, 0x00020000);
#define G2_LD(rs, i, k, aux) __builtin_bit_cast(bf16x8, __builtin_amdgcn_raw_buffer_load_b128(rs, vo, ((i) * 56 + (k)) * 1024, aux))
  bf16x8 wd[7][4];

  {
    const int wm = wid >> 1, wn = wid & 1;
    f32x4 acc[7][2] = {};
    bf16x8 xa[2], wa[7], xb[2], wb[7];
    G1_WAIT(G1_NBUF - 1);
    RAW_BAR();
    {
      const char* base = smem + lane * 16;
#pragma unroll
      for (int i = 0; i < 2; ++i) xa[i] = *(const bf16x8*)(base + (wm * 2 + i) * 1024);
#pragma unroll
      for (int j = 0; j < 7; ++j) wa[j] = *(const bf16x8*)(base + (8 + wn * 7 + j) * 1024);
    }
#define G1_ITER(k, XC, WC, XN, WN, NLEFT)                                                          \
  do {                                                                                             \
    G1_WAIT(NLEFT);                                                                                \
    RAW_BAR();                                                                                     \
    const char* rb_ = smem + (((k) + 1) % G1_NBUF) * G1_STAGE + lane * 16;                         \
    _Pragma("unroll") for (int j = 0; j < 7; ++j) {                                                \
      acc[j][0] = __builtin_amdgcn_mfma_f32_16x16x32_bf16(WC[j], XC[0], acc[j][0], 0, 0, 0);       \
      if (j < 2) XN[j] = *(const bf16x8*)(rb_ + (wm * 2 + j) * 1024);                              \
      acc[j][1] = __builtin_amdgcn_mfma_f32_16x16x32_bf16(WC[j], XC[1], acc[j][1], 0, 0, 0);       \
      WN[j] = *(const bf16x8*)(rb_ + (8 + wn * 7 + j) * 1024);                                     \
      __builtin_amdgcn_sched_barrier(0);                                                           \
      if (j == 3 && (k) + G1_NBUF < 16) { stage((k) + G1_NBUF); __builtin_amdgcn_sched_barrier(0); } \
    }                                                                                              \
  } while (0)
    G1_ITER(0, xa, wa, xb, wb, 5);
    G1_ITER(1, xb, wb, xa, wa, 5);
    G1_ITER(2, xa, wa, xb, wb, 5);
    G1_ITER(3, xb, wb, xa, wa, 5);
    G1_ITER(4, xa, wa, xb, wb, 5);
    G1_ITER(5, xb, wb, xa, wa, 5);
    G1_ITER(6, xa, wa, xb, wb, 5);
    G1_ITER(7, xb, wb, xa, wa, 5);
    G1_ITER(8, xa, wa, xb, wb, 5);
    G1_ITER(9, xb, wb, xa, wa, 5);
    G1_ITER(10, xa, wa, xb, wb, 4);
    G1_ITER(11, xb, wb, xa, wa, 3);
    G1_ITER(12, xa, wa, xb, wb, 2);
    G1_ITER(13, xb, wb, xa, wa, 1);
    G1_ITER(14, xa, wa, xb, wb, 0);
#pragma unroll
    for (int j = 0; j < 7; ++j)
#pragma unroll
      for (int i = 0; i < 2; ++i) acc[j][i] = __builtin_amdgcn_mfma_f32_16x16x32_bf16(wb[j], xb[i], acc[j][i], 0, 0, 0);
    DIAG_STAMP(4);
    RAW_BAR();
    f32x4* ex = (f32x4*)(smem + H_STAGE + 28672);
    if (wn == 0) {
#pragma unroll
      for (int i = 0; i < 2; ++i) ex[(wm * 2 + i) * 64 + lane] = acc[6][i];
    }
    char* hs = smem + H_STAGE;
    auto emit = [&](int t, const f32x4& g, const f32x4& u, int i) {
      u32x2 o;
#pragma unroll
      for (int p = 0; p < 2; ++p) {
        const f2 gb = unpack2(pack2(g[2 * p], g[2 * p + 1]));
        const f2 ub = unpack2(pack2(u[2 * p], u[2 * p + 1]));
        const f2 ge = gelu2(gb);
        const f2 hv = unpack2(pack2(ge.x, ge.y)) * ub;
        o[p] = pack2(hv.x, hv.y);
      }
      *(u32x2*)(hs + (((wm * 2 + i) * 7 + t) * 32 + (lane >> 5) * 16 + (lane & 15)) * 16 + ((lane >> 4) & 1) * 8) = o;
    };
    if (wn == 0) {
#pragma unroll
      for (int t = 0; t < 3; ++t)
#pragma unroll
        for (int i = 0; i < 2; ++i) emit(t, acc[2 * t][i], acc[2 * t + 1][i], i);
      RAW_BAR();
    } else {
#pragma unroll
      for (int t = 4; t < 7; ++t)
#pragma unroll
        for (int i = 0; i < 2; ++i) emit(t, acc[2 * t - 7][i], acc[2 * t - 6][i], i);
      RAW_BAR();
#pragma unroll
      for (int i = 0; i < 2; ++i) emit(3, ex[(wm * 2 + i) * 64 + lane], acc[0][i], i);
    }
    RAW_BAR();
    u16* hT = (u16*)(ws + WS_H);
#pragma unroll
    for (int it = 0; it < 4; ++it) {
      const int c = tid + it * 512;
      if (c < 1792) {
        const int ml = c / 224, rem = c - ml * 224;
        st16_wt(hT + ((size_t)(mt * 8 + ml) * 56 * 64 + (size_t)nt * 7 * 32 + rem) * 8, *(const u32x4*)(hs + c * 16));
      }
    }
  }
  DIAG_STAMP(5);

  {
    const int r = lane & 31, h = lane >> 5;
    float* red = (float*)(smem + CV_RED + ch * 36864);
#pragma unroll
    for (int g = 0; g < 4; ++g) {
      f32x4 v0 = {acc0[4 * g], acc0[4 * g + 1], acc0[4 * g + 2], acc0[4 * g + 3]};
      f32x4 v1 = {acc1[4 * g], acc1[4 * g + 1], acc1[4 * g + 2], acc1[4 * g + 3]};
      *(f32x4*)(red + (w4 * 32 + r) * 36 + 8 * g + 4 * h) = v0;
      *(f32x4*)(red + ((4 + w4) * 32 + r) * 36 + 8 * g + 4 * h) = v1;
    }
    asm volatile("s_waitcnt vmcnt(0)" ::: "memory");
    RAW_BAR();
    if (tid == 0) __hip_atomic_fetch_add((gu32*)(ws + WS_CNT + mt * 256), 1u, __ATOMIC_RELAXED, __HIP_MEMORY_SCOPE_AGENT);
#pragma unroll
    for (int k = 0; k < 7; ++k)
#pragma unroll
      for (int i = 0; i < 4; ++i) wd[k][i] = G2_LD(wr, i, k, 0);
    __builtin_amdgcn_sched_barrier(0);
    DIAG_STAMP2(1);
    u16* at = (u16*)(ws + WS_AT) + (size_t)d * L_;
    {
      const float dsk = bf2f((u16)pds);
      const int c = t2;
      const int T = c >> 7, a = (c >> 2) & 31, b0 = (c & 3) * 8;
      f32x4 s0 = {}, s1 = {};
      const float* p = red + (T * 4 * 32 + a) * 36 + b0;
#pragma unroll
      for (int w = 0; w < 4; ++w) { s0 += *(const f32x4*)(p + w * 32 * 36); s1 += *(const f32x4*)(p + w * 32 * 36 + 4); }
      u32x4 o;
#pragma unroll
      for (int j = 0; j < 4; ++j) {
        const f2 y = unpack2((j < 2) ? pack2(s0[2 * j] * kinv, s0[2 * j + 1] * kinv) : pack2(s1[2 * j - 4] * kinv, s1[2 * j - 3] * kinv));
        const f2 us = unpack2(uv0[j]) * dsk;
        const f2 t = unpack2(pack2(us.x, us.y)) + y;
        u32 ov = pack2(t.x, t.y);
        if ((ov & 0xffffu) == 0xffffu) ov &= 0xffffffc0u;
        if ((ov >> 16) == 0xffffu) ov &= 0xffc0ffffu;
        o[j] = ov;
      }
      st16_wt(at + c * 8, o);
    }
    if (tid == 511) {
      gu32* cnt = (gu32*)(ws + WS_CNT + (mt2 >> 1) * 256);
      for (unsigned spins = 0; spins < (1u << 22); ++spins) {
        if (__hip_atomic_load(cnt, __ATOMIC_RELAXED, __HIP_MEMORY_SCOPE_AGENT) >= 16u) break;
        __builtin_amdgcn_s_sleep(1);
      }
    }
    RAW_BAR();
  }
  DIAG_STAMP2(2);

  {
    f32x4 acc[4][4] = {};
    bf16x8 hb[4][4];
#pragma unroll
    for (int k = 0; k < 4; ++k)
#pragma unroll
      for (int i = 0; i < 4; ++i) hb[k][i] = G2_LD(hr, i, k, 16);
    const int m = tid >> 3, n0 = (tid & 7) * 8;
    const size_t go = (size_t)(mt2 * 64 + m) * D_ + nt2 * 64 + n0;
    const u32x4 xv = *(const u32x4*)(x + go);
    __builtin_amdgcn_sched_barrier(0);
#pragma unroll
    for (int k = 0; k < 7; ++k) {
#pragma unroll
      for (int j = 0; j < 4; ++j)
#pragma unroll
        for (int i = 0; i < 4; ++i) acc[j][i] = __builtin_amdgcn_mfma_f32_16x16x32_bf16(wd[k][j], hb[k & 3][i], acc[j][i], 0, 0, 0);
      if (k < 3) {
#pragma unroll
        for (int i = 0; i < 4; ++i) hb[k][i] = G2_LD(hr, i, k + 4, 16);
      }
      __builtin_amdgcn_sched_barrier(0);
    }
    DIAG_STAMP2(3);
    u32 a0, a1, a2, a3, a4, a5, a6, a7;
    const u16* at = (const u16*)(ws + WS_AT) + (size_t)(nt2 * 64 + n0) * L_ + mt2 * 64 + m;
#define A_LOADS(tail)                                                                                              \
  asm volatile("global_load_ushort %0, %8, off sc1\n\t"                                                            \
               "global_load_ushort %1, %9, off sc1\n\t"                                                            \
               "global_load_ushort %2, %10, off sc1\n\t"                                                           \
               "global_load_ushort %3, %11, off sc1\n\t"                                                           \
               "global_load_ushort %4, %12, off sc1\n\t"                                                           \
               "global_load_ushort %5, %13, off sc1\n\t"                                                           \
               "global_load_ushort %6, %14, off sc1\n\t"                                                           \
               "global_load_ushort %7, %15, off sc1" tail                                                          \
               : "=&v"(a0), "=&v"(a1), "=&v"(a2), "=&v"(a3), "=&v"(a4), "=&v"(a5), "=&v"(a6), "=&v"(a7)            \
               : "v"(at), "v"(at + L_), "v"(at + 2 * L_), "v"(at + 3 * L_), "v"(at + 4 * L_), "v"(at + 5 * L_),   \
                 "v"(at + 6 * L_), "v"(at + 7 * L_)                                                                \
               : "memory")
    A_LOADS("");
    float* red = (float*)smem;
    float* rp = red + (wid * 64 + (lane & 15)) * 68 + 4 * (lane >> 4);
#pragma unroll
    for (int j = 0; j < 4; ++j)
#pragma unroll
      for (int i = 0; i < 4; ++i) *(f32x4*)(rp + i * 16 * 68 + j * 16) = acc[j][i];
    asm volatile("s_waitcnt vmcnt(0)" : "+v"(a0), "+v"(a1), "+v"(a2), "+v"(a3), "+v"(a4), "+v"(a5), "+v"(a6), "+v"(a7)::"memory");
    for (unsigned spins = 0; spins < (1u << 20); ++spins) {
      const bool pend = (a0 == 0xffffu) | (a1 == 0xffffu) | (a2 == 0xffffu) | (a3 == 0xffffu) | (a4 == 0xffffu) |
                        (a5 == 0xffffu) | (a6 == 0xffffu) | (a7 == 0xffffu);
      if (!__any(pend)) break;
      __builtin_amdgcn_s_sleep(2);
      A_LOADS("\n\ts_waitcnt vmcnt(0)");
    }
    RAW_BAR();
    DIAG_STAMP2(4);
    f32x4 p0 = {}, p1 = {};
#pragma unroll
    for (int w = 0; w < 8; ++w) {
      p0 += *(const f32x4*)(red + (w * 64 + m) * 68 + n0);
      p1 += *(const f32x4*)(red + (w * 64 + m) * 68 + n0 + 4);
    }
    const u32 av[8] = {a0, a1, a2, a3, a4, a5, a6, a7};
    u32x4 o;
#pragma unroll
    for (int j = 0; j < 4; ++j) {
      const f2 xx = unpack2(xv[j]);
      const f2 aa = f2{__uint_as_float(av[2 * j] << 16), __uint_as_float(av[2 * j + 1] << 16)};
      const f2 r1 = xx + aa;
      const f2 bb = unpack2((j < 2) ? pack2(p0[2 * j], p0[2 * j + 1]) : pack2(p1[2 * j - 4], p1[2 * j - 3]));
      const f2 r2 = unpack2(pack2(r1.x, r1.y)) + bb;
      o[j] = pack2(r2.x, r2.y);
    }
    *(u32x4*)(out + go) = o;
  }
}

extern "C" void kernel_launch(void* const* d_in, const int* in_sizes, int n_in, void* d_out, int out_size,
                              void* d_ws, size_t ws_size, hipStream_t stream) {
  const u16* x = (const u16*)d_in[0];
  const u16* logA = (const u16*)d_in[1];
  const u16* Aim = (const u16*)d_in[2];
  const u16* Cre = (const u16*)d_in[3];
  const u16* Cim = (const u16*)d_in[4];
  const u16* logdt = (const u16*)d_in[5];
  const u16* dskip = (const u16*)d_in[6];
  const u16* wg = (const u16*)d_in[8];
  const u16* wu = (const u16*)d_in[9];
  const u16* wd = (const u16*)d_in[10];
  char* ws = (char*)d_ws;
  (void)hipFuncSetAttribute((const void*)k2_kernel, hipFuncAttributeMaxDynamicSharedMemorySize, K2_LDS);
  k1_kernel<<<K1_LN_BLOCKS + K1_WT_BLOCKS + K1_KG_BLOCKS, 256, 0, stream>>>(x, logA, Aim, Cre, Cim, logdt, wg, wu, wd, ws);
  k2_kernel<<<256, 512, K2_LDS, stream>>>(x, dskip, ws, (u16*)d_out);
}
```

```cpp
#include <hip/hip_runtime.h>
#include <hip/hip_bf16.h>
#define WT_STORES
#define CV_PRIO 1
#define G2_PRIO 3

typedef unsigned short u16;
typedef unsigned int u32;
using bf16x8 = __attribute__((ext_vector_type(8))) short;
using f16x8 = __attribute__((ext_vector_type(8))) _Float16;
using f16x4 = __attribute__((ext_vector_type(4))) _Float16;
using f32x4 = __attribute__((ext_vector_type(4))) float;
using f32x16 = __attribute__((ext_vector_type(16))) float;
using u32x4 = __attribute__((ext_vector_type(4))) unsigned int;
using u32x2 = __attribute__((ext_vector_type(2))) unsigned int;
typedef float f2 __attribute__((ext_vector_type(2)));
typedef __attribute__((address_space(1))) unsigned gu32;

constexpr int L_ = 2048, D_ = 512, N_ = 128, F_ = 1792;
constexpr int KC_STRIDE = 648;

constexpr size_t WS_CNT = 0;
constexpr size_t WS_XNT = 4096;
constexpr size_t WS_XNTR = WS_XNT + (size_t)L_ * D_ * 2;
constexpr size_t WS_KC = WS_XNTR + (size_t)L_ * D_ * 2;
constexpr size_t WS_H = WS_KC + (size_t)D_ * KC_STRIDE * 4;
constexpr size_t WS_WG = WS_H + (size_t)L_ * F_ * 2;
constexpr size_t WS_WU = WS_WG + (size_t)D_ * F_ * 2;
constexpr size_t WS_WD = WS_WU + (size_t)D_ * F_ * 2;
constexpr size_t WS_AT = WS_WD + (size_t)D_ * F_ * 2;

#define DIAG_START(k)
#define DIAG_STAMP(i)
#define DIAG_END(wsp)
#define DIAG_STAMP2(i)

__device__ __forceinline__ float bf2f(u16 v) { return __uint_as_float(((u32)v) << 16); }
__device__ __forceinline__ u16 f2bf(float f) {
  __bf16 b = (__bf16)f;
  return __builtin_bit_cast(u16, b);
}
__device__ __forceinline__ float rbf(float f) { return bf2f(f2bf(f)); }
__device__ __forceinline__ u32 pack2(float lo, float hi) {
  typedef __bf16 b2 __attribute__((ext_vector_type(2)));
  b2 r = __builtin_convertvector(f2{lo, hi}, b2);
  return __builtin_bit_cast(u32, r);
}
__device__ __forceinline__ f2 unpack2(u32 w) { return f2{__uint_as_float(w << 16), __uint_as_float(w & 0xffff0000u)}; }

__device__ __forceinline__ void st16_wt(void* p, u32x4 v) { asm volatile("global_store_dwordx4 %0, %1, off sc1\n\ts_nop 1" ::"v"(p), "v"(v) : "memory"); }
__device__ __forceinline__ void st16(void* p, u32x4 v) { st16_wt(p, v); }
__device__ __forceinline__ void st8(void* p, u32x2 v) { asm volatile("global_store_dwordx2 %0, %1, off sc1\n\ts_nop 1" ::"v"(p), "v"(v) : "memory"); }

#define DPP_F(v, ctrl) __builtin_bit_cast(float, __builtin_amdgcn_update_dpp(0, __builtin_bit_cast(int, v), ctrl, 0xf, 0xf, true))
__device__ __forceinline__ float wave_sum(float v) {
  v += DPP_F(v, 0xB1);
  v += DPP_F(v, 0x4E);
  v += DPP_F(v, 0x141);
  v += DPP_F(v, 0x140);
  const int iv = __builtin_bit_cast(int, v);
  return (__builtin_bit_cast(float, __builtin_amdgcn_readlane(iv, 0)) + __builtin_bit_cast(float, __builtin_amdgcn_readlane(iv, 16))) +
         (__builtin_bit_cast(float, __builtin_amdgcn_readlane(iv, 32)) + __builtin_bit_cast(float, __builtin_amdgcn_readlane(iv, 48)));
}
__device__ __forceinline__ float wave_max(float v) {
  v = fmaxf(v, DPP_F(v, 0xB1));
  v = fmaxf(v, DPP_F(v, 0x4E));
  v = fmaxf(v, DPP_F(v, 0x141));
  v = fmaxf(v, DPP_F(v, 0x140));
  const int iv = __builtin_bit_cast(int, v);
  return fmaxf(fmaxf(__builtin_bit_cast(float, __builtin_amdgcn_readlane(iv, 0)), __builtin_bit_cast(float, __builtin_amdgcn_readlane(iv, 16))),
               fmaxf(__builtin_bit_cast(float, __builtin_amdgcn_readlane(iv, 32)), __builtin_bit_cast(float, __builtin_amdgcn_readlane(iv, 48))));
}

constexpr int K1_LN_BLOCKS = 128;
constexpr int K1_WT_BLOCKS = 112;
constexpr int K1_TC_BLOCKS = 256;
constexpr int K1_KG_BLOCKS = 512;

__device__ __forceinline__ void cmul6(f2& r0, f2& r1, f2& r2, f2& r3, f2& r4, f2& r5, f2 a0, f2 b0, f2 a1, f2 b1,
                                      f2 a2, f2 b2, f2 a3, f2 b3, f2 a4, f2 b4, f2 a5, f2 b5) {
  asm("v_pk_mul_f32 %0, %6, %7 op_sel:[1,1] op_sel_hi:[1,0] neg_lo:[0,1]\n\t"
      "v_pk_mul_f32 %1, %8, %9 op_sel:[1,1] op_sel_hi:[1,0] neg_lo:[0,1]\n\t"
      "v_pk_mul_f32 %2, %10, %11 op_sel:[1,1] op_sel_hi:[1,0] neg_lo:[0,1]\n\t"
      "v_pk_mul_f32 %3, %12, %13 op_sel:[1,1] op_sel_hi:[1,0] neg_lo:[0,1]\n\t"
      "v_pk_mul_f32 %4, %14, %15 op_sel:[1,1] op_sel_hi:[1,0] neg_lo:[0,1]\n\t"
      "v_pk_mul_f32 %5, %16, %17 op_sel:[1,1] op_sel_hi:[1,0] neg_lo:[0,1]\n\t"
      "v_pk_fma_f32 %0, %6, %7, %0 op_sel:[0,0,0] op_sel_hi:[0,1,1]\n\t"
      "v_pk_fma_f32 %1, %8, %9, %1 op_sel:[0,0,0] op_sel_hi:[0,1,1]\n\t"
      "v_pk_fma_f32 %2, %10, %11, %2 op_sel:[0,0,0] op_sel_hi:[0,1,1]\n\t"
      "v_pk_fma_f32 %3, %12, %13, %3 op_sel:[0,0,0] op_sel_hi:[0,1,1]\n\t"
      "v_pk_fma_f32 %4, %14, %15, %4 op_sel:[0,0,0] op_sel_hi:[0,1,1]\n\t"
      "v_pk_fma_f32 %5, %16, %17, %5 op_sel:[0,0,0] op_sel_hi:[0,1,1]\n\t"
      "s_nop 1"
      : "=&v"(r0), "=&v"(r1), "=&v"(r2), "=&v"(r3), "=&v"(r4), "=&v"(r5)
      : "v"(a0), "v"(b0), "v"(a1), "v"(b1), "v"(a2), "v"(b2), "v"(a3), "v"(b3), "v"(a4), "v"(b4), "v"(a5), "v"(b5));
}
__device__ __forceinline__ void cmul2(f2& r0, f2& r1, f2 a0, f2 b0, f2 a1, f2 b1) {
  asm("v_pk_mul_f32 %0, %2, %3 op_sel:[1,1] op_sel_hi:[1,0] neg_lo:[0,1]\n\t"
      "v_pk_mul_f32 %1, %4, %5 op_sel:[1,1] op_sel_hi:[1,0] neg_lo:[0,1]\n\t"
      "s_nop 0\n\t"
      "v_pk_fma_f32 %0, %2, %3, %0 op_sel:[0,0,0] op_sel_hi:[0,1,1]\n\t"
      "v_pk_fma_f32 %1, %4, %5, %1 op_sel:[0,0,0] op_sel_hi:[0,1,1]\n\t"
      "s_nop 1"
      : "=&v"(r0), "=&v"(r1)
      : "v"(a0), "v"(b0), "v"(a1), "v"(b1));
}
__device__ __forceinline__ f2 cexp_(float zr, float zi, float t) {
  const float m = __builtin_amdgcn_exp2f((zr * t) * 1.44269504088896341f);
  float r = (zi * t) * 0.15915494309189535f;
  r -= rintf(r);
  return f2{m * __builtin_amdgcn_cosf(r), m * __builtin_amdgcn_sinf(r)};
}

__global__ __launch_bounds__(256) void k1_kernel(const u16* __restrict__ x, const u16* __restrict__ logA,
                                                 const u16* __restrict__ Aim, const u16* __restrict__ Cre,
                                                 const u16* __restrict__ Cim, const u16* __restrict__ logdt,
                                                 const u16* __restrict__ wg, const u16* __restrict__ wu,
                                                 const u16* __restrict__ wd, char* __restrict__ ws) {
  __shared__ __attribute__((aligned(16))) char smem[18432];
  const int tid = threadIdx.x, lane = tid & 63, wid = tid >> 6;
  DIAG_START(0)
  int bid = blockIdx.x - K1_KG_BLOCKS;
  if (bid >= 0 && bid < K1_LN_BLOCKS) {
    __builtin_amdgcn_s_setprio(3);
    if (bid == 0 && tid < 16) *(u32*)(ws + WS_CNT + tid * 256) = 0u;
#pragma unroll
    for (int it = 0; it < 4; ++it) st16(ws + WS_AT + ((size_t)bid * 1024 + it * 256 + tid) * 16, u32x4{0xffffffffu, 0xffffffffu, 0xffffffffu, 0xffffffffu});
    u16* tile = (u16*)smem;
    u16* xnt = (u16*)(ws + WS_XNT);
    const int r0 = bid * 16;
    u32x4 v[4];
#pragma unroll
    for (int i = 0; i < 4; ++i) v[i] = *(const u32x4*)(x + (size_t)(r0 + wid * 4 + i) * D_ + lane * 8);
#pragma unroll
    for (int i = 0; i < 4; ++i) {
      const int rl = wid * 4 + i;
      f2 f[4];
#pragma unroll
      for (int j = 0; j < 4; ++j) f[j] = unpack2(v[i][j]);
      const f2 s2 = (f[0] + f[1]) + (f[2] + f[3]);
      const float mean = wave_sum(s2.x + s2.y) * (1.f / D_);
      f2 q2 = {0.f, 0.f};
#pragma unroll
      for (int j = 0; j < 4; ++j) { const f2 t = f[j] - mean; q2 = __builtin_elementwise_fma(t, t, q2); }
      const float q = wave_sum(q2.x + q2.y);
      const float mb = rbf(mean);
      const float vb = rbf(q * (1.f / D_));
      const float sd = rbf(sqrtf(rbf(vb + 1e-5f)));
      const float rs = __builtin_amdgcn_rcpf(sd);
      u32x4 o;
#pragma unroll
      for (int j = 0; j < 4; ++j) {
        const f2 t = f[j] - mb;
        const f2 a = unpack2(pack2(t.x, t.y));
        f2 qq = a * rs;
        const f2 rem = __builtin_elementwise_fma(-qq, f2{sd, sd}, a);
        qq = __builtin_elementwise_fma(rem, f2{rs, rs}, qq);
        o[j] = pack2(qq.x, qq.y);
      }
      *(u32x4*)(tile + rl * 520 + lane * 8) = o;
    }
    DIAG_STAMP(1);
    __syncthreads();
#pragma unroll
    for (int it = 0; it < 4; ++it) {
      const int c = tid + it * 256;
      const u32x4 o = *(const u32x4*)(tile + (c & 15) * 520 + (c >> 4) * 8);
      st16(xnt + ((size_t)bid * 1024 + c) * 8, o);
    }
    u16* xtr = (u16*)(ws + WS_XNTR);
#pragma unroll
    for (int it = 0; it < 4; ++it) {
      const int c = tid + it * 256;
      const int d = c >> 1, g = c & 1;
      u32x4 o;
#pragma unroll
      for (int j = 0; j < 4; ++j)
        o[j] = (u32)tile[(g * 8 + 2 * j) * 520 + d] | ((u32)tile[(g * 8 + 2 * j + 1) * 520 + d] << 16);
      st16(xtr + ((size_t)bid * 1024 + c) * 8, o);
    }
    DIAG_END(ws);
    return;
  }
  bid -= K1_LN_BLOCKS;
  if (bid >= 0) {
    __builtin_amdgcn_s_setprio(2);
    if (bid >= K1_WT_BLOCKS) {
      const int tb = bid - K1_WT_BLOCKS;
      const int half = (blockIdx.x & 7) & 1, part = tb >> 3;
      u32x4 acc = {0, 0, 0, 0};
#pragma unroll
      for (int it = 0; it < 14; ++it) {
        const int c = tid + it * 256;
        const int mtx = c / 1792, rem = c - mtx * 1792, row = rem / 112, ck = rem - row * 112;
        acc ^= *(const u32x4*)((mtx ? wu : wg) + (size_t)(part * 16 + row) * F_ + half * 896 + ck * 8);
      }
      if (acc[0] == 0x9e3779b9u && acc[1] == 0x7f4a7c15u && acc[2] == 0x12345678u && acc[3] == 0x0badf00du) *(u32x4*)(ws + WS_WG) = acc;
      DIAG_END(ws);
      return;
    }
    int task = bid * 4 + wid;
    const u16* W = wd; u16* Wt = (u16*)(ws + WS_WD); const int Kd = F_, Nd = D_;
    const int nks = Kd / 32;
    const int ks = task % nks, nb = task / nks;
    const int col = nb * 64 + lane;
    u16 v[32];
#pragma unroll
    for (int k = 0; k < 32; ++k) v[k] = W[(size_t)(ks * 32 + k) * Nd + col];
#pragma unroll
    for (int q = 0; q < 4; ++q) {
      u32x4 o;
#pragma unroll
      for (int j = 0; j < 4; ++j) o[j] = (u32)v[q * 8 + 2 * j] | ((u32)v[q * 8 + 2 * j + 1] << 16);
      st16(Wt + ((size_t)((col >> 4) * nks + ks) * 64 + q * 16 + (col & 15)) * 8, o);
    }
    DIAG_END(ws);
    return;
  }
  bid = blockIdx.x;
  {
    const int d = bid;
    float4* prm = (float4*)(smem + 16384);
    if (tid < 128) {
      const u16 la = logA[d * N_ + tid], ai = Aim[d * N_ + tid], cr = Cre[d * N_ + tid], ci = Cim[d * N_ + tid];
      const float dt = rbf(expf(bf2f(logdt[d])));
      const float ar = -rbf(expf(bf2f(la)));
      prm[tid] = make_float4(ar * dt, bf2f(ai) * dt, bf2f(cr) * dt, bf2f(ci) * dt);
    }
    __syncthreads();
    DIAG_STAMP(1);
    const int kq = lane >> 4, idx = lane & 15;
    f32x4 acc[4] = {};
    const float ty = (float)idx, te = (float)(idx == 0 ? 256 : 16 * idx);
    float4 pr[8];
#pragma unroll
    for (int s = 0; s < 8; ++s) pr[s] = prm[wid * 32 + s * 4 + kq];
#pragma unroll
    for (int s = 0; s < 8; s += 2) {
      const float4 pa = pr[s], pb = pr[s + 1];
      const f2 Ca = f2{pa.z, -pa.w}, Cb = f2{pb.z, -pb.w};
      f2 Ya[4], Yb[4], Xa[4], Xb[4];
      Ya[0] = cexp_(pa.x, pa.y, ty); Yb[0] = cexp_(pb.x, pb.y, ty);
      f2 Ea = cexp_(pa.x, -pa.y, te), Eb = cexp_(pb.x, -pb.y, te);
      f2 Fa = (idx == 0) ? f2{1.f, 0.f} : Ea, Fb = (idx == 0) ? f2{1.f, 0.f} : Eb;
#pragma unroll
      for (int e = 0; e < 3; ++e) {
        f2 nEa, nEb;
        cmul6(Xa[e], Xb[e], Ya[e + 1], Yb[e + 1], nEa, nEb, Ca, Fa, Cb, Fb, Ya[e], Ya[e], Yb[e], Yb[e], Ea, Ea, Eb, Eb);
        Ea = nEa; Eb = nEb; Fa = nEa; Fb = nEb;
      }
      cmul2(Xa[3], Xb[3], Ca, Ea, Cb, Eb);
#pragma unroll
      for (int e = 0; e < 4; ++e) acc[e] = __builtin_amdgcn_mfma_f32_16x16x4f32(Xa[e].x, Ya[e].x, acc[e], 0, 0, 0);
#pragma unroll
      for (int e = 0; e < 4; ++e) acc[e] = __builtin_amdgcn_mfma_f32_16x16x4f32(Xa[e].y, Ya[e].y, acc[e], 0, 0, 0);
#pragma unroll
      for (int e = 0; e < 4; ++e) acc[e] = __builtin_amdgcn_mfma_f32_16x16x4f32(Xb[e].x, Yb[e].x, acc[e], 0, 0, 0);
#pragma unroll
      for (int e = 0; e < 4; ++e) acc[e] = __builtin_amdgcn_mfma_f32_16x16x4f32(Xb[e].y, Yb[e].y, acc[e], 0, 0, 0);
    }
    DIAG_STAMP(2);
    float* red = (float*)smem;
#pragma unroll
    for (int e = 0; e < 4; ++e)
#pragma unroll
      for (int r = 0; r < 4; ++r) red[(wid * 4 + e) * 256 + ((lane >> 4) * 4 + r) * 16 + (lane & 15)] = acc[e][r];
    __syncthreads();
    float* kc = (float*)(ws + WS_KC) + (size_t)d * KC_STRIDE;
#pragma unroll
    for (int e = 0; e < 4; ++e) {
      const float v = 2.f * ((red[(0 * 4 + e) * 256 + tid] + red[(1 * 4 + e) * 256 + tid]) +
                             (red[(2 * 4 + e) * 256 + tid] + red[(3 * 4 + e) * 256 + tid]));
      if (e == 0) kc[tid] = v;
      else if (tid >= 128) kc[128 * e + tid] = v;
      else if (e == 3 && tid == 0) kc[640] = v;
    }
    DIAG_END(ws);
  }
}

constexpr int G1_STAGE = 24 * 1024;
constexpr int G1_NBUF = 6;
constexpr int K2_LDS = G1_STAGE * G1_NBUF;

constexpr int UC_LEN = 2080;
constexpr int KR_LEN = 3840;
constexpr int CV_UCP = 0;
constexpr int CV_KR = CV_UCP + 8 * UC_LEN * 2;
constexpr int CV_URAW = CV_KR + KR_LEN * 2;
constexpr int CV_KRAW = CV_URAW + 4224;
constexpr int CV_MAX = CV_KRAW + 2592;
constexpr int CV_CH_STRIDE = 48128;
constexpr int H_STAGE = 2 * CV_CH_STRIDE;

__device__ __forceinline__ f2 gelu2(f2 g) {
  const float c0 = -2.f * 0.7978845608028654f * 1.44269504088896341f;
  const float c1 = c0 * 0.044715f;
  const f2 t = g * __builtin_elementwise_fma(g * g, f2{c1, c1}, f2{c0, c0});
  const f2 dn = f2{__builtin_amdgcn_exp2f(t.x), __builtin_amdgcn_exp2f(t.y)} + 1.f;
  return g * f2{__builtin_amdgcn_rcpf(dn.x), __builtin_amdgcn_rcpf(dn.y)};
}
__device__ __forceinline__ void glds16(const void* g, void* l) {
  __builtin_amdgcn_global_load_lds((const __attribute__((address_space(1))) u32*)g,
                                   (__attribute__((address_space(3))) u32*)l, 16, 0, 0);
}
__device__ __forceinline__ u32 bf2_to_h2(u32 w) {
  auto r = __builtin_amdgcn_cvt_pkrtz(__uint_as_float(w << 16), __uint_as_float(w & 0xffff0000u));
  return __builtin_bit_cast(u32, r);
}
#define RAW_BAR() do { asm volatile("s_waitcnt lgkmcnt(0)" ::: "memory"); __builtin_amdgcn_s_barrier(); } while (0)

__global__ __launch_bounds__(512, 2) void k2_kernel(const u16* __restrict__ x, const u16* __restrict__ dskip,
                                                    const u16* __restrict__ wg, const u16* __restrict__ wu,
                                                    char* __restrict__ ws, u16* __restrict__ out) {
  extern __shared__ __attribute__((aligned(16))) char smem[];
  const int tid = threadIdx.x, lane = tid & 63;
  const int wid = __builtin_amdgcn_readfirstlane(tid >> 6);
  DIAG_START(1)
  const int bid = blockIdx.x;
  const int xcd = bid & 7, loc = bid >> 3;
  const int ch = wid >> 2, w4 = wid & 3;
  const int d = bid * 2 + ch;
  const int t2 = tid & 255;
  const int mt = (xcd >> 1) * 4 + (loc & 3);
  const int nt = (xcd & 1) * 8 + (loc >> 2);

  u32x4 uv0; float kv0, kv1, kv2; u32 pds;
  {
    const u16* xnt = (const u16*)(ws + WS_XNT);
    const u16* s0 = xnt + (size_t)(mt * 8 + wid) * 16 * 512 + lane * 8;
    const u16 *s1, *s2;
    {
      const int row = wid * 4 + (lane >> 4), c = lane & 15;
      const int v = (row & 3) | (((row >> 3) & 1) << 2);
      int T = (c >> 1) ^ v;
      T = T > 6 ? 6 : T;
      const size_t o = (size_t)row * F_ + nt * 112 + T * 16 + (c & 1) * 8;
      s1 = wg + o;
      s2 = wu + o;
    }
    auto stage = [&](int ks) {
      char* base = smem + (ks % G1_NBUF) * G1_STAGE + wid * 1024 + lane * 16;
      glds16(s0 + ks * 512, base);
      glds16(s1 + (size_t)ks * 32 * F_, base + 8192);
      glds16(s2 + (size_t)ks * 32 * F_, base + 16384);
    };
#define G1_WAIT(n) asm volatile("s_waitcnt vmcnt(%0)" ::"n"(3 * (n)) : "memory")
#pragma unroll
    for (int k = 0; k < G1_NBUF; ++k) stage(k);
    const int wm = wid >> 1, wn = wid & 1;
    f32x4 acc[7][2] = {};
    bf16x8 xa[2], wa[7], xb[2], wb[7];
    int wofs[7];
    {
      const int g = lane >> 4, q = (lane & 15) >> 2, pp = lane & 3;
      const int v = q | ((g & 1) << 2);
#pragma unroll
      for (int j = 0; j < 7; ++j) {
        const int sl = wn * 7 + j;
        wofs[j] = 8192 + (sl & 1) * 8192 + (8 * g + q) * 256 + (((sl >> 1) ^ v) * 32) + pp * 8;
      }
    }
    typedef short s16x4 __attribute__((ext_vector_type(4)));
    auto rdw = [&](const char* sb, int j) -> bf16x8 {
      const s16x4 lo = __builtin_amdgcn_ds_read_tr16_b64_v4i16((__attribute__((address_space(3))) s16x4*)(sb + wofs[j]));
      const s16x4 hi = __builtin_amdgcn_ds_read_tr16_b64_v4i16((__attribute__((address_space(3))) s16x4*)(sb + wofs[j] + 1024));
      return __builtin_shufflevector(lo, hi, 0, 1, 2, 3, 4, 5, 6, 7);
    };
    G1_WAIT(G1_NBUF - 1);
    RAW_BAR();
    DIAG_STAMP(1);
    {
#pragma unroll
      for (int i = 0; i < 2; ++i) xa[i] = *(const bf16x8*)(smem + lane * 16 + (wm * 2 + i) * 1024);
#pragma unroll
      for (int j = 0; j < 7; ++j) wa[j] = rdw(smem, j);
    }
#define G1_ITER(k, XC, WC, XN, WN, NLEFT)                                                          \
  do {                                                                                             \
    G1_WAIT(NLEFT);                                                                                \
    RAW_BAR();                                                                                     \
    const char* sb_ = smem + (((k) + 1) % G1_NBUF) * G1_STAGE;                                     \
    _Pragma("unroll") for (int j = 0; j < 7; ++j) {                                                \
      acc[j][0] = __builtin_amdgcn_mfma_f32_16x16x32_bf16(WC[j], XC[0], acc[j][0], 0, 0, 0);       \
      if (j < 2) XN[j] = *(const bf16x8*)(sb_ + lane * 16 + (wm * 2 + j) * 1024);                  \
      acc[j][1] = __builtin_amdgcn_mfma_f32_16x16x32_bf16(WC[j], XC[1], acc[j][1], 0, 0, 0);       \
      WN[j] = rdw(sb_, j);                                                                         \
      __builtin_amdgcn_sched_barrier(0);                                                           \
      if (j == 3 && (k) + G1_NBUF < 16) { stage((k) + G1_NBUF); __builtin_amdgcn_sched_barrier(0); } \
    }                                                                                              \
  } while (0)
    G1_ITER(0, xa, wa, xb, wb, 4);
    G1_ITER(1, xb, wb, xa, wa, 4);
    G1_ITER(2, xa, wa, xb, wb, 4);
    G1_ITER(3, xb, wb, xa, wa, 4);
    G1_ITER(4, xa, wa, xb, wb, 4);
    G1_ITER(5, xb, wb, xa, wa, 4);
    G1_ITER(6, xa, wa, xb, wb, 4);
    G1_ITER(7, xb, wb, xa, wa, 4);
    G1_ITER(8, xa, wa, xb, wb, 4);
    G1_ITER(9, xb, wb, xa, wa, 4);
    G1_ITER(10, xa, wa, xb, wb, 4);
    G1_ITER(11, xb, wb, xa, wa, 3);
    G1_ITER(12, xa, wa, xb, wb, 2);
    G1_ITER(13, xb, wb, xa, wa, 1);
    G1_ITER(14, xa, wa, xb, wb, 0);
    {
      const u16* ug = (const u16*)(ws + WS_XNTR) + (size_t)(t2 >> 1) * 8192 + d * 16 + (t2 & 1) * 8;
      const float* kc = (const float*)(ws + WS_KC) + (size_t)d * KC_STRIDE;
      uv0 = *(const u32x4*)ug;
      kv0 = kc[t2]; kv1 = kc[t2 + 256]; kv2 = (t2 < 129) ? kc[t2 + 512] : 0.f;
      pds = dskip[d];
    }
    __builtin_amdgcn_sched_barrier(0);
#pragma unroll
    for (int j = 0; j < 7; ++j)
#pragma unroll
      for (int i = 0; i < 2; ++i) acc[j][i] = __builtin_amdgcn_mfma_f32_16x16x32_bf16(wb[j], xb[i], acc[j][i], 0, 0, 0);
    DIAG_STAMP(2);
    RAW_BAR();
    f32x4* ex = (f32x4*)(smem + H_STAGE + 28672);
    if (wn == 0) {
#pragma unroll
      for (int i = 0; i < 2; ++i) ex[(wm * 2 + i) * 64 + lane] = acc[6][i];
    }
    char* hs = smem + H_STAGE;
    auto emit = [&](int t, const f32x4& g, const f32x4& u, int i) {
      u32x2 o;
#pragma unroll
      for (int p = 0; p < 2; ++p) {
        const f2 gb = unpack2(pack2(g[2 * p], g[2 * p + 1]));
        const f2 ub = unpack2(pack2(u[2 * p], u[2 * p + 1]));
        const f2 ge = gelu2(gb);
        const f2 hv = unpack2(pack2(ge.x, ge.y)) * ub;
        o[p] = pack2(hv.x, hv.y);
      }
      *(u32x2*)(hs + (((wm * 2 + i) * 7 + t) * 32 + (lane >> 5) * 16 + (lane & 15)) * 16 + ((lane >> 4) & 1) * 8) = o;
    };
    if (wn == 0) {
#pragma unroll
      for (int t = 0; t < 3; ++t)
#pragma unroll
        for (int i = 0; i < 2; ++i) emit(t, acc[2 * t][i], acc[2 * t + 1][i], i);
      RAW_BAR();
    } else {
#pragma unroll
      for (int t = 4; t < 7; ++t)
#pragma unroll
        for (int i = 0; i < 2; ++i) emit(t, acc[2 * t - 7][i], acc[2 * t - 6][i], i);
      RAW_BAR();
#pragma unroll
      for (int i = 0; i < 2; ++i) emit(3, ex[(wm * 2 + i) * 64 + lane], acc[0][i], i);
    }
    {
      char* cbase = smem + ch * CV_CH_STRIDE;
      u32x4* uraw = (u32x4*)(cbase + CV_URAW);
      float* kraw = (float*)(cbase + CV_KRAW);
      float* kmax = (float*)(cbase + CV_MAX);
      if (t2 < 4) { uraw[t2] = u32x4{0, 0, 0, 0}; uraw[260 + t2] = u32x4{0, 0, 0, 0}; }
      u32x4 h;
#pragma unroll
      for (int j = 0; j < 4; ++j) h[j] = bf2_to_h2(uv0[j]);
      uraw[4 + t2] = h;
      kraw[t2] = kv0;
      kraw[t2 + 256] = kv1;
      if (t2 < 136) kraw[t2 + 512] = kv2;
      const float mx = wave_max(fmaxf(fmaxf(fabsf(kv0), fabsf(kv1)), fabsf(kv2)));
      if (lane == 0) kmax[w4] = mx;
    }
    RAW_BAR();
    u16* hT = (u16*)(ws + WS_H);
#pragma unroll
    for (int it = 0; it < 4; ++it) {
      const int c = tid + it * 512;
      if (c < 1792) {
        const int ml = c / 224, rem = c - ml * 224;
        st16_wt(hT + ((size_t)(mt * 8 + ml) * 56 * 64 + (size_t)nt * 7 * 32 + rem) * 8, *(const u32x4*)(hs + c * 16));
      }
    }
  }
  DIAG_STAMP(3);

  const int mt2 = (xcd >> 1) * 8 + (xcd & 1) * 4 + (loc & 3);
  const int nt2 = loc >> 2;
  const int vo = lane * 16;
  const __amdgpu_buffer_rsrc_t hr = __builtin_amdgcn_make_buffer_rsrc((void*)((const u16*)(ws + WS_H) + ((size_t)(mt2 * 4) * 56 + wid * 7) * 512), 0, 4 * 56 * 1024, 0x00020000);
  const __amdgpu_buffer_rsrc_t wr = __builtin_amdgcn_make_buffer_rsrc((void*)((const u16*)(ws + WS_WD) + ((size_t)(nt2 * 4) * 56 + wid * 7) * 512), 0, 4 * 56 * 1024, 0x00020000);
#define G2_LD(rs, i, k, aux) __builtin_bit_cast(bf16x8, __builtin_amdgcn_raw_buffer_load_b128(rs, vo, ((i) * 56 + (k)) * 1024, aux))
  bf16x8 wd[7][4];

  {
    char* cbase = smem + ch * CV_CH_STRIDE;
    u16* ucp = (u16*)(cbase + CV_UCP);
    u16* kr = (u16*)(cbase + CV_KR);
    const u32x4* uraw = (const u32x4*)(cbase + CV_URAW);
    const float* kraw = (const float*)(cbase + CV_KRAW);
    const float* kmax = (const float*)(cbase + CV_MAX);
    const float dsk = bf2f((u16)pds);
    float kscale, kinv;
    {
      const float mx = fmaxf(fmaxf(kmax[0], kmax[1]), fmaxf(kmax[2], kmax[3]));
      int e = (int)((__float_as_uint(mx) >> 23) & 0xff);
      e = min(max(e, 20), 240);
      kscale = __uint_as_float((u32)(127 + 13 + 127 - e) << 23);
      kinv = __uint_as_float((u32)(e - 13) << 23);
    }
#pragma unroll
    for (int it = 0; it < 2; ++it) {
      const int tb = it ? 256 + (t2 & 3) : t2;
      u32 w[8];
      u32x4 a = uraw[tb], b = uraw[tb + 1];
#pragma unroll
      for (int j = 0; j < 4; ++j) { w[j] = a[j]; w[4 + j] = b[j]; }
      if (it == 0 || t2 < 4) {
#pragma unroll
        for (int c = 0; c < 8; ++c) {
          u32x4 o;
#pragma unroll
          for (int j = 0; j < 4; ++j)
            o[j] = (c & 1) ? w[j + (c + 1) / 2] : __builtin_amdgcn_alignbit(w[j + c / 2 + 1], w[j + c / 2], 16);
          *(u32x4*)(ucp + c * UC_LEN + tb * 8) = o;
        }
      }
    }
    {
      float kk[8];
#pragma unroll
      for (int j = 0; j < 8; ++j) {
        const int jj = 2047 - (t2 * 8 + j);
        const int cbi = (int)f2bf((float)jj) - 17024;
        kk[j] = kraw[jj < 128 ? jj : cbi] * kscale;
      }
      f16x8 o;
#pragma unroll
      for (int j = 0; j < 8; ++j) o[j] = (_Float16)kk[j];
      *(f16x8*)(kr + t2 * 8 + 8 * (t2 >> 2)) = o;
      if (t2 < 128) { const int tb = t2 + 256; *(u32x4*)(kr + tb * 8 + 8 * (tb >> 2)) = u32x4{0, 0, 0, 0}; }
    }
    asm volatile("s_waitcnt vmcnt(0)" ::: "memory");
    RAW_BAR();
    if (tid == 0) __hip_atomic_fetch_add((gu32*)(ws + WS_CNT + mt * 256), 1u, __ATOMIC_RELAXED, __HIP_MEMORY_SCOPE_AGENT);
    DIAG_STAMP(4);
    const int r = lane & 31, h = lane >> 5;
    const u16* ap = ucp + (r & 7) * UC_LEN + 8 * h + 8 * (r >> 3);
    const int bofs0 = 2048 - 32 * (r + 1) + 8 * h + 8 * (63 - r);
    const int bofs1 = bofs0 - 1024 - 256;
    f32x16 acc0 = {}, acc1 = {};
    unsigned hcnt = 0;
    {
      const u16* apw = ap + 16 * (w4 * 16);
      const u16* bp0 = kr + bofs0 + 16 * (w4 * 16) + 8 * (w4 * 8);
      const u16* bp1 = kr + bofs1 + 16 * (w4 * 16) + 8 * (w4 * 8);
      auto lda = [&](int s) -> f16x8 { return *(const f16x8*)(apw + 16 * s); };
      auto ldb = [&](const u16* b, int s) -> f16x8 { return *(const f16x8*)(b + 16 * s + 8 * (s >> 1)); };
      f16x8 a0 = lda(0), p0 = ldb(bp0, 0), q0 = ldb(bp1, 0), c0 = lda(64);
      f16x8 a1 = lda(1), p1 = ldb(bp0, 1), q1 = ldb(bp1, 1), c1 = lda(65);
#pragma unroll
      for (int it = 0; it < 8; ++it) {
        f16x8 na0, na1, nc0, nc1, np0, np1, nq0, nq1;
        if (it < 7) {
          const int sn = 2 * it + 2;
          na0 = lda(sn); np0 = ldb(bp0, sn); nq0 = ldb(bp1, sn); nc0 = lda(sn + 64);
          na1 = lda(sn + 1); np1 = ldb(bp0, sn + 1); nq1 = ldb(bp1, sn + 1); nc1 = lda(sn + 65);
#pragma unroll
          for (int i = 0; i < 4; ++i) wd[it][i] = G2_LD(wr, i, it, 0);
        }
        if (it == 7) hcnt = __hip_atomic_load((gu32*)(ws + WS_CNT + (mt2 >> 1) * 256), __ATOMIC_RELAXED, __HIP_MEMORY_SCOPE_AGENT);
        __builtin_amdgcn_sched_barrier(0);
        acc0 = __builtin_amdgcn_mfma_f32_32x32x16_f16(a0, p0, acc0, 0, 0, 0);
        acc1 = __builtin_amdgcn_mfma_f32_32x32x16_f16(a0, q0, acc1, 0, 0, 0);
        acc1 = __builtin_amdgcn_mfma_f32_32x32x16_f16(c0, p0, acc1, 0, 0, 0);
        acc0 = __builtin_amdgcn_mfma_f32_32x32x16_f16(a1, p1, acc0, 0, 0, 0);
        acc1 = __builtin_amdgcn_mfma_f32_32x32x16_f16(a1, q1, acc1, 0, 0, 0);
        acc1 = __builtin_amdgcn_mfma_f32_32x32x16_f16(c1, p1, acc1, 0, 0, 0);
        __builtin_amdgcn_sched_barrier(0);
        if (it < 7) { a0 = na0; a1 = na1; c0 = nc0; c1 = nc1; p0 = np0; p1 = np1; q0 = nq0; q1 = nq1; }
      }
    }
    DIAG_STAMP(5);
    RAW_BAR();
    float* red = (float*)cbase;
#pragma unroll
    for (int g = 0; g < 4; ++g) {
      f32x4 v0 = {acc0[4 * g], acc0[4 * g + 1], acc0[4 * g + 2], acc0[4 * g + 3]};
      f32x4 v1 = {acc1[4 * g], acc1[4 * g + 1], acc1[4 * g + 2], acc1[4 * g + 3]};
      *(f32x4*)(red + (w4 * 32 + r) * 36 + 8 * g + 4 * h) = v0;
      *(f32x4*)(red + ((4 + w4) * 32 + r) * 36 + 8 * g + 4 * h) = v1;
    }
    if (tid == 511 && hcnt < 16u) {
      gu32* cnt = (gu32*)(ws + WS_CNT + (mt2 >> 1) * 256);
      for (unsigned spins = 0; spins < (1u << 22); ++spins) {
        if (__hip_atomic_load(cnt, __ATOMIC_RELAXED, __HIP_MEMORY_SCOPE_AGENT) >= 16u) break;
        __builtin_amdgcn_s_sleep(1);
      }
    }
    RAW_BAR();
    DIAG_STAMP2(1);
    u16* at = (u16*)(ws + WS_AT) + (size_t)d * L_;
    {
      const int c = t2;
      const int T = c >> 7, a = (c >> 2) & 31, b0 = (c & 3) * 8;
      f32x4 s0 = {}, s1 = {};
      const float* p = red + (T * 4 * 32 + a) * 36 + b0;
#pragma unroll
      for (int w = 0; w < 4; ++w) { s0 += *(const f32x4*)(p + w * 32 * 36); s1 += *(const f32x4*)(p + w * 32 * 36 + 4); }
      u32x4 o;
#pragma unroll
      for (int j = 0; j < 4; ++j) {
        const f2 y = unpack2((j < 2) ? pack2(s0[2 * j] * kinv, s0[2 * j + 1] * kinv) : pack2(s1[2 * j - 4] * kinv, s1[2 * j - 3] * kinv));
        const f2 us = unpack2(uv0[j]) * dsk;
        const f2 t = unpack2(pack2(us.x, us.y)) + y;
        u32 ov = pack2(t.x, t.y);
        if ((ov & 0xffffu) == 0xffffu) ov &= 0xffffffc0u;
        if ((ov >> 16) == 0xffffu) ov &= 0xffc0ffffu;
        o[j] = ov;
      }
      st16_wt(at + c * 8, o);
    }
  }
  DIAG_STAMP2(2);

  {
    f32x4 acc[4][4] = {};
    bf16x8 hb[4][4];
#pragma unroll
    for (int k = 0; k < 4; ++k)
#pragma unroll
      for (int i = 0; i < 4; ++i) hb[k][i] = G2_LD(hr, i, k, 16);
    const int m = tid >> 3, n0 = (tid & 7) * 8;
    const size_t go = (size_t)(mt2 * 64 + m) * D_ + nt2 * 64 + n0;
    const u32x4 xv = *(const u32x4*)(x + go);
    __builtin_amdgcn_sched_barrier(0);
#pragma unroll
    for (int k = 0; k < 7; ++k) {
#pragma unroll
      for (int j = 0; j < 4; ++j)
#pragma unroll
        for (int i = 0; i < 4; ++i) acc[j][i] = __builtin_amdgcn_mfma_f32_16x16x32_bf16(wd[k][j], hb[k & 3][i], acc[j][i], 0, 0, 0);
      if (k < 3) {
#pragma unroll
        for (int i = 0; i < 4; ++i) hb[k][i] = G2_LD(hr, i, k + 4, 16);
      }
      __builtin_amdgcn_sched_barrier(0);
    }
    DIAG_STAMP2(3);
    u32 a0, a1, a2, a3, a4, a5, a6, a7;
    const u16* at = (const u16*)(ws + WS_AT) + (size_t)(nt2 * 64 + n0) * L_ + mt2 * 64 + m;
#define A_LOADS(tail)                                                                                              \
  asm volatile("global_load_ushort %0, %8, off sc1\n\t"                                                            \
               "global_load_ushort %1, %9, off sc1\n\t"                                                            \
               "global_load_ushort %2, %10, off sc1\n\t"                                                           \
               "global_load_ushort %3, %11, off sc1\n\t"                                                           \
               "global_load_ushort %4, %12, off sc1\n\t"                                                           \
               "global_load_ushort %5, %13, off sc1\n\t"                                                           \
               "global_load_ushort %6, %14, off sc1\n\t"                                                           \
               "global_load_ushort %7, %15, off sc1" tail                                                          \
               : "=&v"(a0), "=&v"(a1), "=&v"(a2), "=&v"(a3), "=&v"(a4), "=&v"(a5), "=&v"(a6), "=&v"(a7)            \
               : "v"(at), "v"(at + L_), "v"(at + 2 * L_), "v"(at + 3 * L_), "v"(at + 4 * L_), "v"(at + 5 * L_),   \
                 "v"(at + 6 * L_), "v"(at + 7 * L_)                                                                \
               : "memory")
    A_LOADS("");
    RAW_BAR();
    float* red = (float*)smem;
    float* rp = red + (wid * 64 + (lane & 15)) * 68 + 4 * (lane >> 4);
#pragma unroll
    for (int j = 0; j < 4; ++j)
#pragma unroll
      for (int i = 0; i < 4; ++i) *(f32x4*)(rp + i * 16 * 68 + j * 16) = acc[j][i];
    asm volatile("s_waitcnt vmcnt(0)" : "+v"(a0), "+v"(a1), "+v"(a2), "+v"(a3), "+v"(a4), "+v"(a5), "+v"(a6), "+v"(a7)::"memory");
    for (unsigned spins = 0; spins < (1u << 20); ++spins) {
      const bool pend = (a0 == 0xffffu) | (a1 == 0xffffu) | (a2 == 0xffffu) | (a3 == 0xffffu) | (a4 == 0xffffu) |
                        (a5 == 0xffffu) | (a6 == 0xffffu) | (a7 == 0xffffu);
      if (!__any(pend)) break;
      __builtin_amdgcn_s_sleep(2);
      A_LOADS("\n\ts_waitcnt vmcnt(0)");
    }
    RAW_BAR();
    DIAG_STAMP2(4);
    f32x4 p0 = {}, p1 = {};
#pragma unroll
    for (int w = 0; w < 8; ++w) {
      p0 += *(const f32x4*)(red + (w * 64 + m) * 68 + n0);
      p1 += *(const f32x4*)(red + (w * 64 + m) * 68 + n0 + 4);
    }
    const u32 av[8] = {a0, a1, a2, a3, a4, a5, a6, a7};
    u32x4 o;
#pragma unroll
    for (int j = 0; j < 4; ++j) {
      const f2 xx = unpack2(xv[j]);
      const f2 aa = f2{__uint_as_float(av[2 * j] << 16), __uint_as_float(av[2 * j + 1] << 16)};
      const f2 r1 = xx + aa;
      const f2 bb = unpack2((j < 2) ? pack2(p0[2 * j], p0[2 * j + 1]) : pack2(p1[2 * j - 4], p1[2 * j - 3]));
      const f2 r2 = unpack2(pack2(r1.x, r1.y)) + bb;
      o[j] = pack2(r2.x, r2.y);
    }
    *(u32x4*)(out + go) = o;
  }
}

extern "C" void kernel_launch(void* const* d_in, const int* in_sizes, int n_in, void* d_out, int out_size,
                              void* d_ws, size_t ws_size, hipStream_t stream) {
  const u16* x = (const u16*)d_in[0];
  const u16* logA = (const u16*)d_in[1];
  const u16* Aim = (const u16*)d_in[2];
  const u16* Cre = (const u16*)d_in[3];
  const u16* Cim = (const u16*)d_in[4];
  const u16* logdt = (const u16*)d_in[5];
  const u16* dskip = (const u16*)d_in[6];
  const u16* wg = (const u16*)d_in[8];
  const u16* wu = (const u16*)d_in[9];
  const u16* wd = (const u16*)d_in[10];
  char* ws = (char*)d_ws;
  (void)hipFuncSetAttribute((const void*)k2_kernel, hipFuncAttributeMaxDynamicSharedMemorySize, K2_LDS);
  k1_kernel<<<K1_LN_BLOCKS + K1_WT_BLOCKS + K1_TC_BLOCKS + K1_KG_BLOCKS, 256, 0, stream>>>(x, logA, Aim, Cre, Cim, logdt, wg, wu, wd, ws);
  k2_kernel<<<256, 512, K2_LDS, stream>>>(x, dskip, wg, wu, ws, (u16*)d_out);
}
```

```cpp
#include <hip/hip_runtime.h>
#include <hip/hip_bf16.h>
#define WT_STORES
#define CV_PRIO 1
#define G2_PRIO 3

typedef unsigned short u16;
typedef unsigned int u32;
using bf16x8 = __attribute__((ext_vector_type(8))) short;
using f16x8 = __attribute__((ext_vector_type(8))) _Float16;
using f16x4 = __attribute__((ext_vector_type(4))) _Float16;
using f32x4 = __attribute__((ext_vector_type(4))) float;
using f32x16 = __attribute__((ext_vector_type(16))) float;
using u32x4 = __attribute__((ext_vector_type(4))) unsigned int;
using u32x2 = __attribute__((ext_vector_type(2))) unsigned int;
typedef float f2 __attribute__((ext_vector_type(2)));
typedef __attribute__((address_space(1))) unsigned gu32;

constexpr int L_ = 2048, D_ = 512, N_ = 128, F_ = 1792;
constexpr int KC_STRIDE = 648;

constexpr size_t WS_CNT = 0;
constexpr size_t WS_XNT = 4096;
constexpr size_t WS_XNTR = WS_XNT + (size_t)L_ * D_ * 2;
constexpr size_t WS_KC = WS_XNTR + (size_t)L_ * D_ * 2;
constexpr size_t WS_H = WS_KC + (size_t)D_ * KC_STRIDE * 4;
constexpr size_t WS_WG = WS_H + (size_t)L_ * F_ * 2;
constexpr size_t WS_WU = WS_WG + (size_t)D_ * F_ * 2;
constexpr size_t WS_WD = WS_WU + (size_t)D_ * F_ * 2;
constexpr size_t WS_AT = WS_WD + (size_t)D_ * F_ * 2;

#define DIAG_START(k)
#define DIAG_STAMP(i)
#define DIAG_END(wsp)
#define DIAG_STAMP2(i)

__device__ __forceinline__ float bf2f(u16 v) { return __uint_as_float(((u32)v) << 16); }
__device__ __forceinline__ u16 f2bf(float f) {
  __bf16 b = (__bf16)f;
  return __builtin_bit_cast(u16, b);
}
__device__ __forceinline__ float rbf(float f) { return bf2f(f2bf(f)); }
__device__ __forceinline__ u32 pack2(float lo, float hi) {
  typedef __bf16 b2 __attribute__((ext_vector_type(2)));
  b2 r = __builtin_convertvector(f2{lo, hi}, b2);
  return __builtin_bit_cast(u32, r);
}
__device__ __forceinline__ f2 unpack2(u32 w) { return f2{__uint_as_float(w << 16), __uint_as_float(w & 0xffff0000u)}; }

__device__ __forceinline__ void st16_wt(void* p, u32x4 v) { asm volatile("global_store_dwordx4 %0, %1, off sc1\n\ts_nop 1" ::"v"(p), "v"(v) : "memory"); }
__device__ __forceinline__ void st16(void* p, u32x4 v) { st16_wt(p, v); }
__device__ __forceinline__ void st8(void* p, u32x2 v) { asm volatile("global_store_dwordx2 %0, %1, off sc1\n\ts_nop 1" ::"v"(p), "v"(v) : "memory"); }

#define DPP_F(v, ctrl) __builtin_bit_cast(float, __builtin_amdgcn_update_dpp(0, __builtin_bit_cast(int, v), ctrl, 0xf, 0xf, true))
__device__ __forceinline__ float wave_sum(float v) {
  v += DPP_F(v, 0xB1);
  v += DPP_F(v, 0x4E);
  v += DPP_F(v, 0x141);
  v += DPP_F(v, 0x140);
  const int iv = __builtin_bit_cast(int, v);
  return (__builtin_bit_cast(float, __builtin_amdgcn_readlane(iv, 0)) + __builtin_bit_cast(float, __builtin_amdgcn_readlane(iv, 16))) +
         (__builtin_bit_cast(float, __builtin_amdgcn_readlane(iv, 32)) + __builtin_bit_cast(float, __builtin_amdgcn_readlane(iv, 48)));
}
__device__ __forceinline__ float wave_max(float v) {
  v = fmaxf(v, DPP_F(v, 0xB1));
  v = fmaxf(v, DPP_F(v, 0x4E));
  v = fmaxf(v, DPP_F(v, 0x141));
  v = fmaxf(v, DPP_F(v, 0x140));
  const int iv = __builtin_bit_cast(int, v);
  return fmaxf(fmaxf(__builtin_bit_cast(float, __builtin_amdgcn_readlane(iv, 0)), __builtin_bit_cast(float, __builtin_amdgcn_readlane(iv, 16))),
               fmaxf(__builtin_bit_cast(float, __builtin_amdgcn_readlane(iv, 32)), __builtin_bit_cast(float, __builtin_amdgcn_readlane(iv, 48))));
}

constexpr int K1_LN_BLOCKS = 128;
constexpr int K1_WT_BLOCKS = 112;
constexpr int K1_TC_BLOCKS = 256;
constexpr int K1_KG_BLOCKS = 512;

__device__ __forceinline__ void cmul6(f2& r0, f2& r1, f2& r2, f2& r3, f2& r4, f2& r5, f2 a0, f2 b0, f2 a1, f2 b1,
                                      f2 a2, f2 b2, f2 a3, f2 b3, f2 a4, f2 b4, f2 a5, f2 b5) {
  asm("v_pk_mul_f32 %0, %6, %7 op_sel:[1,1] op_sel_hi:[1,0] neg_lo:[0,1]\n\t"
      "v_pk_mul_f32 %1, %8, %9 op_sel:[1,1] op_sel_hi:[1,0] neg_lo:[0,1]\n\t"
      "v_pk_mul_f32 %2, %10, %11 op_sel:[1,1] op_sel_hi:[1,0] neg_lo:[0,1]\n\t"
      "v_pk_mul_f32 %3, %12, %13 op_sel:[1,1] op_sel_hi:[1,0] neg_lo:[0,1]\n\t"
      "v_pk_mul_f32 %4, %14, %15 op_sel:[1,1] op_sel_hi:[1,0] neg_lo:[0,1]\n\t"
      "v_pk_mul_f32 %5, %16, %17 op_sel:[1,1] op_sel_hi:[1,0] neg_lo:[0,1]\n\t"
      "v_pk_fma_f32 %0, %6, %7, %0 op_sel:[0,0,0] op_sel_hi:[0,1,1]\n\t"
      "v_pk_fma_f32 %1, %8, %9, %1 op_sel:[0,0,0] op_sel_hi:[0,1,1]\n\t"
      "v_pk_fma_f32 %2, %10, %11, %2 op_sel:[0,0,0] op_sel_hi:[0,1,1]\n\t"
      "v_pk_fma_f32 %3, %12, %13, %3 op_sel:[0,0,0] op_sel_hi:[0,1,1]\n\t"
      "v_pk_fma_f32 %4, %14, %15, %4 op_sel:[0,0,0] op_sel_hi:[0,1,1]\n\t"
      "v_pk_fma_f32 %5, %16, %17, %5 op_sel:[0,0,0] op_sel_hi:[0,1,1]\n\t"
      "s_nop 1"
      : "=&v"(r0), "=&v"(r1), "=&v"(r2), "=&v"(r3), "=&v"(r4), "=&v"(r5)
      : "v"(a0), "v"(b0), "v"(a1), "v"(b1), "v"(a2), "v"(b2), "v"(a3), "v"(b3), "v"(a4), "v"(b4), "v"(a5), "v"(b5));
}
__device__ __forceinline__ void cmul2(f2& r0, f2& r1, f2 a0, f2 b0, f2 a1, f2 b1) {
  asm("v_pk_mul_f32 %0, %2, %3 op_sel:[1,1] op_sel_hi:[1,0] neg_lo:[0,1]\n\t"
      "v_pk_mul_f32 %1, %4, %5 op_sel:[1,1] op_sel_hi:[1,0] neg_lo:[0,1]\n\t"
      "s_nop 0\n\t"
      "v_pk_fma_f32 %0, %2, %3, %0 op_sel:[0,0,0] op_sel_hi:[0,1,1]\n\t"
      "v_pk_fma_f32 %1, %4, %5, %1 op_sel:[0,0,0] op_sel_hi:[0,1,1]\n\t"
      "s_nop 1"
      : "=&v"(r0), "=&v"(r1)
      : "v"(a0), "v"(b0), "v"(a1), "v"(b1));
}
__device__ __forceinline__ f2 cexp_(float zr, float zi, float t) {
  const float m = __builtin_amdgcn_exp2f((zr * t) * 1.44269504088896341f);
  float r = (zi * t) * 0.15915494309189535f;
  r -= rintf(r);
  return f2{m * __builtin_amdgcn_cosf(r), m * __builtin_amdgcn_sinf(r)};
}

__global__ __launch_bounds__(256) void k1_kernel(const u16* __restrict__ x, const u16* __restrict__ logA,
                                                 const u16* __restrict__ Aim, const u16* __restrict__ Cre,
                                                 const u16* __restrict__ Cim, const u16* __restrict__ logdt,
                                                 const u16* __restrict__ wg, const u16* __restrict__ wu,
                                                 const u16* __restrict__ wd, char* __restrict__ ws) {
  __shared__ __attribute__((aligned(16))) char smem[18432];
  const int tid = threadIdx.x, lane = tid & 63, wid = tid >> 6;
  DIAG_START(0)
  int bid = blockIdx.x - K1_KG_BLOCKS;
  if (bid >= 0 && bid < K1_LN_BLOCKS) {
    __builtin_amdgcn_s_setprio(3);
    if (bid == 0 && tid < 16) *(u32*)(ws + WS_CNT + tid * 256) = 0u;
#pragma unroll
    for (int it = 0; it < 4; ++it) st16(ws + WS_AT + ((size_t)bid * 1024 + it * 256 + tid) * 16, u32x4{0xffffffffu, 0xffffffffu, 0xffffffffu, 0xffffffffu});
    u16* tile = (u16*)smem;
    u16* xnt = (u16*)(ws + WS_XNT);
    const int r0 = bid * 16;
    u32x4 v[4];
#pragma unroll
    for (int i = 0; i < 4; ++i) v[i] = *(const u32x4*)(x + (size_t)(r0 + wid * 4 + i) * D_ + lane * 8);
#pragma unroll
    for (int i = 0; i < 4; ++i) {
      const int rl = wid * 4 + i;
      f2 f[4];
#pragma unroll
      for (int j = 0; j < 4; ++j) f[j] = unpack2(v[i][j]);
      const f2 s2 = (f[0] + f[1]) + (f[2] + f[3]);
      const float mean = wave_sum(s2.x + s2.y) * (1.f / D_);
      f2 q2 = {0.f, 0.f};
#pragma unroll
      for (int j = 0; j < 4; ++j) { const f2 t = f[j] - mean; q2 = __builtin_elementwise_fma(t, t, q2); }
      const float q = wave_sum(q2.x + q2.y);
      const float mb = rbf(mean);
      const float vb = rbf(q * (1.f / D_));
      const float sd = rbf(sqrtf(rbf(vb + 1e-5f)));
      const float rs = __builtin_amdgcn_rcpf(sd);
      u32x4 o;
#pragma unroll
      for (int j = 0; j < 4; ++j) {
        const f2 t = f[j] - mb;
        const f2 a = unpack2(pack2(t.x, t.y));
        f2 qq = a * rs;
        const f2 rem = __builtin_elementwise_fma(-qq, f2{sd, sd}, a);
        qq = __builtin_elementwise_fma(rem, f2{rs, rs}, qq);
        o[j] = pack2(qq.x, qq.y);
      }
      *(u32x4*)(tile + rl * 520 + lane * 8) = o;
    }
    DIAG_STAMP(1);
    __syncthreads();
#pragma unroll
    for (int it = 0; it < 4; ++it) {
      const int c = tid + it * 256;
      const u32x4 o = *(const u32x4*)(tile + (c & 15) * 520 + (c >> 4) * 8);
      st16(xnt + ((size_t)bid * 1024 + c) * 8, o);
    }
    u16* xtr = (u16*)(ws + WS_XNTR);
#pragma unroll
    for (int it = 0; it < 4; ++it) {
      const int c = tid + it * 256;
      const int d = c >> 1, g = c & 1;
      u32x4 o;
#pragma unroll
      for (int j = 0; j < 4; ++j)
        o[j] = (u32)tile[(g * 8 + 2 * j) * 520 + d] | ((u32)tile[(g * 8 + 2 * j + 1) * 520 + d] << 16);
      st16(xtr + ((size_t)bid * 1024 + c) * 8, o);
    }
    DIAG_END(ws);
    return;
  }
  bid -= K1_LN_BLOCKS;
  if (bid >= 0) {
    __builtin_amdgcn_s_setprio(2);
    if (bid >= K1_WT_BLOCKS) {
      const int tb = bid - K1_WT_BLOCKS;
      const int half = (blockIdx.x & 7) & 1, part = tb >> 3;
      u32x4 acc = {0, 0, 0, 0};
#pragma unroll
      for (int it = 0; it < 14; ++it) {
        const int c = tid + it * 256;
        const int mtx = c / 1792, rem = c - mtx * 1792, row = rem / 112, ck = rem - row * 112;
        acc ^= *(const u32x4*)((mtx ? wu : wg) + (size_t)(part * 16 + row) * F_ + half * 896 + ck * 8);
      }
      if (acc[0] == 0x9e3779b9u && acc[1] == 0x7f4a7c15u && acc[2] == 0x12345678u && acc[3] == 0x0badf00du) *(u32x4*)(ws + WS_WG) = acc;
      DIAG_END(ws);
      return;
    }
    int task = bid * 4 + wid;
    const u16* W = wd; u16* Wt = (u16*)(ws + WS_WD); const int Kd = F_, Nd = D_;
    const int nks = Kd / 32;
    const int ks = task % nks, nb = task / nks;
    const int col = nb * 64 + lane;
    u16 v[32];
#pragma unroll
    for (int k = 0; k < 32; ++k) v[k] = W[(size_t)(ks * 32 + k) * Nd + col];
#pragma unroll
    for (int q = 0; q < 4; ++q) {
      u32x4 o;
#pragma unroll
      for (int j = 0; j < 4; ++j) o[j] = (u32)v[q * 8 + 2 * j] | ((u32)v[q * 8 + 2 * j + 1] << 16);
      st16(Wt + ((size_t)((col >> 4) * nks + ks) * 64 + q * 16 + (col & 15)) * 8, o);
    }
    DIAG_END(ws);
    return;
  }
  bid = blockIdx.x;
  {
    const int d = bid;
    float4* prm = (float4*)(smem + 16384);
    if (tid < 128) {
      const u16 la = logA[d * N_ + tid], ai = Aim[d * N_ + tid], cr = Cre[d * N_ + tid], ci = Cim[d * N_ + tid];
      const float dt = rbf(expf(bf2f(logdt[d])));
      const float ar = -rbf(expf(bf2f(la)));
      prm[tid] = make_float4(ar * dt, bf2f(ai) * dt, bf2f(cr) * dt, bf2f(ci) * dt);
    }
    __syncthreads();
    DIAG_STAMP(1);
    const int kq = lane >> 4, idx = lane & 15;
    f32x4 acc[4] = {};
    const float ty = (float)idx, te = (float)(idx == 0 ? 256 : 16 * idx);
    float4 pr[8];
#pragma unroll
    for (int s = 0; s < 8; ++s) pr[s] = prm[wid * 32 + s * 4 + kq];
#pragma unroll
    for (int s = 0; s < 8; s += 2) {
      const float4 pa = pr[s], pb = pr[s + 1];
      const f2 Ca = f2{pa.z, -pa.w}, Cb = f2{pb.z, -pb.w};
      f2 Ya[4], Yb[4], Xa[4], Xb[4];
      Ya[0] = cexp_(pa.x, pa.y, ty); Yb[0] = cexp_(pb.x, pb.y, ty);
      f2 Ea = cexp_(pa.x, -pa.y, te), Eb = cexp_(pb.x, -pb.y, te);
      f2 Fa = (idx == 0) ? f2{1.f, 0.f} : Ea, Fb = (idx == 0) ? f2{1.f, 0.f} : Eb;
#pragma unroll
      for (int e = 0; e < 3; ++e) {
        f2 nEa, nEb;
        cmul6(Xa[e], Xb[e], Ya[e + 1], Yb[e + 1], nEa, nEb, Ca, Fa, Cb, Fb, Ya[e], Ya[e], Yb[e], Yb[e], Ea, Ea, Eb, Eb);
        Ea = nEa; Eb = nEb; Fa = nEa; Fb = nEb;
      }
      cmul2(Xa[3], Xb[3], Ca, Ea, Cb, Eb);
#pragma unroll
      for (int e = 0; e < 4; ++e) acc[e] = __builtin_amdgcn_mfma_f32_16x16x4f32(Xa[e].x, Ya[e].x, acc[e], 0, 0, 0);
#pragma unroll
      for (int e = 0; e < 4; ++e) acc[e] = __builtin_amdgcn_mfma_f32_16x16x4f32(Xa[e].y, Ya[e].y, acc[e], 0, 0, 0);
#pragma unroll
      for (int e = 0; e < 4; ++e) acc[e] = __builtin_amdgcn_mfma_f32_16x16x4f32(Xb[e].x, Yb[e].x, acc[e], 0, 0, 0);
#pragma unroll
      for (int e = 0; e < 4; ++e) acc[e] = __builtin_amdgcn_mfma_f32_16x16x4f32(Xb[e].y, Yb[e].y, acc[e], 0, 0, 0);
    }
    DIAG_STAMP(2);
    float* red = (float*)smem;
#pragma unroll
    for (int e = 0; e < 4; ++e)
#pragma unroll
      for (int r = 0; r < 4; ++r) red[(wid * 4 + e) * 256 + ((lane >> 4) * 4 + r) * 16 + (lane & 15)] = acc[e][r];
    __syncthreads();
    float* kc = (float*)(ws + WS_KC) + (size_t)d * KC_STRIDE;
#pragma unroll
    for (int e = 0; e < 4; ++e) {
      const float v = 2.f * ((red[(0 * 4 + e) * 256 + tid] + red[(1 * 4 + e) * 256 + tid]) +
                             (red[(2 * 4 + e) * 256 + tid] + red[(3 * 4 + e) * 256 + tid]));
      if (e == 0) kc[tid] = v;
      else if (tid >= 128) kc[128 * e + tid] = v;
      else if (e == 3 && tid == 0) kc[640] = v;
    }
    DIAG_END(ws);
  }
}

constexpr int G1_STAGE = 24 * 1024;
constexpr int G1_NBUF = 6;
constexpr int K2_LDS = G1_STAGE * G1_NBUF;

constexpr int UC_LEN = 2080;
constexpr int KR_LEN = 3840;
constexpr int CV_UCP = 0;
constexpr int CV_KR = CV_UCP + 8 * UC_LEN * 2;
constexpr int CV_URAW = CV_KR + KR_LEN * 2;
constexpr int CV_KRAW = CV_URAW + 4224;
constexpr int CV_MAX = CV_KRAW + 2592;
constexpr int CV_CH_STRIDE = 48128;
constexpr int H_STAGE = 2 * CV_CH_STRIDE;

__device__ __forceinline__ f2 gelu2(f2 g) {
  const float c0 = -2.f * 0.7978845608028654f * 1.44269504088896341f;
  const float c1 = c0 * 0.044715f;
  const f2 t = g * __builtin_elementwise_fma(g * g, f2{c1, c1}, f2{c0, c0});
  const f2 dn = f2{__builtin_amdgcn_exp2f(t.x), __builtin_amdgcn_exp2f(t.y)} + 1.f;
  return g * f2{__builtin_amdgcn_rcpf(dn.x), __builtin_amdgcn_rcpf(dn.y)};
}
__device__ __forceinline__ void glds16(const void* g, void* l) {
  __builtin_amdgcn_global_load_lds((const __attribute__((address_space(1))) u32*)g,
                                   (__attribute__((address_space(3))) u32*)l, 16, 0, 0);
}
__device__ __forceinline__ void glds16_asm(const void* g, u32 lds_dst) {
  u32 keep;
  asm volatile("s_mov_b32 %0, m0\n\ts_mov_b32 m0, %2\n\ts_nop 0\n\tglobal_load_lds_dwordx4 %1, off\n\ts_mov_b32 m0, %0"
               : "=&s"(keep) : "v"(g), "s"(lds_dst) : "memory");
}
__device__ __forceinline__ u32 bf2_to_h2(u32 w) {
  auto r = __builtin_amdgcn_cvt_pkrtz(__uint_as_float(w << 16), __uint_as_float(w & 0xffff0000u));
  return __builtin_bit_cast(u32, r);
}
#define RAW_BAR() do { asm volatile("s_waitcnt lgkmcnt(0)" ::: "memory"); __builtin_amdgcn_s_barrier(); } while (0)

__global__ __launch_bounds__(512, 2) void k2_kernel(const u16* __restrict__ x, const u16* __restrict__ dskip,
                                                    const u16* __restrict__ wg, const u16* __restrict__ wu,
                                                    char* __restrict__ ws, u16* __restrict__ out) {
  extern __shared__ __attribute__((aligned(16))) char smem[];
  const int tid = threadIdx.x, lane = tid & 63;
  const int wid = __builtin_amdgcn_readfirstlane(tid >> 6);
  DIAG_START(1)
  const int bid = blockIdx.x;
  const int xcd = bid & 7, loc = bid >> 3;
  const int ch = wid >> 2, w4 = wid & 3;
  const int d = bid * 2 + ch;
  const int t2 = tid & 255;
  const int mt = (xcd >> 1) * 4 + (loc & 3);
  const int nt = (xcd & 1) * 8 + (loc >> 2);

  u32x4 uv0; float kv0, kv1, kv2; u32 pds;
  {
    const u16* xnt = (const u16*)(ws + WS_XNT);
    const u16* s0 = xnt + (size_t)(mt * 8 + wid) * 16 * 512 + lane * 8;
    const u16 *s1, *s2;
    {
      const int row = wid * 4 + (lane >> 4), c = lane & 15;
      const int v = (row & 3) | (((row >> 3) & 1) << 2);
      int T = (c >> 1) ^ v;
      T = T > 6 ? 6 : T;
      const size_t o = (size_t)row * F_ + nt * 112 + T * 16 + (c & 1) * 8;
      s1 = wg + o;
      s2 = wu + o;
    }
    const u32 lds0 = (u32)(size_t)(__attribute__((address_space(3))) char*)smem + wid * 1024;
    auto stage = [&](int ks) {
      const u32 base = lds0 + (ks % G1_NBUF) * G1_STAGE;
      glds16_asm(s0 + ks * 512, base);
      glds16_asm(s1 + (size_t)ks * 32 * F_, base + 8192);
      glds16_asm(s2 + (size_t)ks * 32 * F_, base + 16384);
    };
#define G1_WAIT(n) asm volatile("s_waitcnt vmcnt(%0)" ::"n"(3 * (n)) : "memory")
#pragma unroll
    for (int k = 0; k < G1_NBUF; ++k) stage(k);
    const int wm = wid >> 1, wn = wid & 1;
    f32x4 acc[7][2] = {};
    bf16x8 xa[2], wa[7], xb[2], wb[7];
    int wofs[7];
    {
      const int g = lane >> 4, q = (lane & 15) >> 2, pp = lane & 3;
      const int v = q | ((g & 1) << 2);
#pragma unroll
      for (int j = 0; j < 7; ++j) {
        const int sl = wn * 7 + j;
        wofs[j] = 8192 + (sl & 1) * 8192 + (8 * g + q) * 256 + (((sl >> 1) ^ v) * 32) + pp * 8;
      }
    }
    typedef short s16x4 __attribute__((ext_vector_type(4)));
    auto rdw = [&](const char* sb, int j) -> bf16x8 {
      const s16x4 lo = __builtin_amdgcn_ds_read_tr16_b64_v4i16((__attribute__((address_space(3))) s16x4*)(sb + wofs[j]));
      const s16x4 hi = __builtin_amdgcn_ds_read_tr16_b64_v4i16((__attribute__((address_space(3))) s16x4*)(sb + wofs[j] + 1024));
      return __builtin_shufflevector(lo, hi, 0, 1, 2, 3, 4, 5, 6, 7);
    };
    G1_WAIT(G1_NBUF - 1);
    RAW_BAR();
    DIAG_STAMP(1);
    {
#pragma unroll
      for (int i = 0; i < 2; ++i) xa[i] = *(const bf16x8*)(smem + lane * 16 + (wm * 2 + i) * 1024);
#pragma unroll
      for (int j = 0; j < 7; ++j) wa[j] = rdw(smem, j);
    }
#define G1_ITER(k, XC, WC, XN, WN, NLEFT)                                                          \
  do {                                                                                             \
    G1_WAIT(NLEFT);                                                                                \
    RAW_BAR();                                                                                     \
    const char* sb_ = smem + (((k) + 1) % G1_NBUF) * G1_STAGE;                                     \
    _Pragma("unroll") for (int j = 0; j < 7; ++j) {                                                \
      acc[j][0] = __builtin_amdgcn_mfma_f32_16x16x32_bf16(WC[j], XC[0], acc[j][0], 0, 0, 0);       \
      if (j < 2) XN[j] = *(const bf16x8*)(sb_ + lane * 16 + (wm * 2 + j) * 1024);                  \
      acc[j][1] = __builtin_amdgcn_mfma_f32_16x16x32_bf16(WC[j], XC[1], acc[j][1], 0, 0, 0);       \
      WN[j] = rdw(sb_, j);                                                                         \
      __builtin_amdgcn_sched_barrier(0);                                                           \
      if (j == 3 && (k) + G1_NBUF < 16) { stage((k) + G1_NBUF); __builtin_amdgcn_sched_barrier(0); } \
    }                                                                                              \
  } while (0)
    G1_ITER(0, xa, wa, xb, wb, 4);
    G1_ITER(1, xb, wb, xa, wa, 4);
    G1_ITER(2, xa, wa, xb, wb, 4);
    G1_ITER(3, xb, wb, xa, wa, 4);
    G1_ITER(4, xa, wa, xb, wb, 4);
    G1_ITER(5, xb, wb, xa, wa, 4);
    G1_ITER(6, xa, wa, xb, wb, 4);
    G1_ITER(7, xb, wb, xa, wa, 4);
    G1_ITER(8, xa, wa, xb, wb, 4);
    G1_ITER(9, xb, wb, xa, wa, 4);
    G1_ITER(10, xa, wa, xb, wb, 4);
    G1_ITER(11, xb, wb, xa, wa, 3);
    G1_ITER(12, xa, wa, xb, wb, 2);
    G1_ITER(13, xb, wb, xa, wa, 1);
    G1_ITER(14, xa, wa, xb, wb, 0);
    {
      const u16* ug = (const u16*)(ws + WS_XNTR) + (size_t)(t2 >> 1) * 8192 + d * 16 + (t2 & 1) * 8;
      const float* kc = (const float*)(ws + WS_KC) + (size_t)d * KC_STRIDE;
      uv0 = *(const u32x4*)ug;
      kv0 = kc[t2]; kv1 = kc[t2 + 256]; kv2 = (t2 < 129) ? kc[t2 + 512] : 0.f;
      pds = dskip[d];
    }
    __builtin_amdgcn_sched_barrier(0);
#pragma unroll
    for (int j = 0; j < 7; ++j)
#pragma unroll
      for (int i = 0; i < 2; ++i) acc[j][i] = __builtin_amdgcn_mfma_f32_16x16x32_bf16(wb[j], xb[i], acc[j][i], 0, 0, 0);
    DIAG_STAMP(2);
    RAW_BAR();
    f32x4* ex = (f32x4*)(smem + H_STAGE + 28672);
    if (wn == 0) {
#pragma unroll
      for (int i = 0; i < 2; ++i) ex[(wm * 2 + i) * 64 + lane] = acc[6][i];
    }
    char* hs = smem + H_STAGE;
    auto emit = [&](int t, const f32x4& g, const f32x4& u, int i) {
      u32x2 o;
#pragma unroll
      for (int p = 0; p < 2; ++p) {
        const f2 gb = unpack2(pack2(g[2 * p], g[2 * p + 1]));
        const f2 ub = unpack2(pack2(u[2 * p], u[2 * p + 1]));
        const f2 ge = gelu2(gb);
        const f2 hv = unpack2(pack2(ge.x, ge.y)) * ub;
        o[p] = pack2(hv.x, hv.y);
      }
      *(u32x2*)(hs + (((wm * 2 + i) * 7 + t) * 32 + (lane >> 5) * 16 + (lane & 15)) * 16 + ((lane >> 4) & 1) * 8) = o;
    };
    if (wn == 0) {
#pragma unroll
      for (int t = 0; t < 3; ++t)
#pragma unroll
        for (int i = 0; i < 2; ++i) emit(t, acc[2 * t][i], acc[2 * t + 1][i], i);
      RAW_BAR();
    } else {
#pragma unroll
      for (int t = 4; t < 7; ++t)
#pragma unroll
        for (int i = 0; i < 2; ++i) emit(t, acc[2 * t - 7][i], acc[2 * t - 6][i], i);
      RAW_BAR();
#pragma unroll
      for (int i = 0; i < 2; ++i) emit(3, ex[(wm * 2 + i) * 64 + lane], acc[0][i], i);
    }
    {
      char* cbase = smem + ch * CV_CH_STRIDE;
      u32x4* uraw = (u32x4*)(cbase + CV_URAW);
      float* kraw = (float*)(cbase + CV_KRAW);
      float* kmax = (float*)(cbase + CV_MAX);
      if (t2 < 4) { uraw[t2] = u32x4{0, 0, 0, 0}; uraw[260 + t2] = u32x4{0, 0, 0, 0}; }
      u32x4 h;
#pragma unroll
      for (int j = 0; j < 4; ++j) h[j] = bf2_to_h2(uv0[j]);
      uraw[4 + t2] = h;
      kraw[t2] = kv0;
      kraw[t2 + 256] = kv1;
      if (t2 < 136) kraw[t2 + 512] = kv2;
      const float mx = wave_max(fmaxf(fmaxf(fabsf(kv0), fabsf(kv1)), fabsf(kv2)));
      if (lane == 0) kmax[w4] = mx;
    }
    RAW_BAR();
    u16* hT = (u16*)(ws + WS_H);
#pragma unroll
    for (int it = 0; it < 4; ++it) {
      const int c = tid + it * 512;
      if (c < 1792) {
        const int ml = c / 224, rem = c - ml * 224;
        st16_wt(hT + ((size_t)(mt * 8 + ml) * 56 * 64 + (size_t)nt * 7 * 32 + rem) * 8, *(const u32x4*)(hs + c * 16));
      }
    }
  }
  DIAG_STAMP(3);

  const int mt2 = (xcd >> 1) * 8 + (xcd & 1) * 4 + (loc & 3);
  const int nt2 = loc >> 2;
  const int vo = lane * 16;
  const __amdgpu_buffer_rsrc_t hr = __builtin_amdgcn_make_buffer_rsrc((void*)((const u16*)(ws + WS_H) + ((size_t)(mt2 * 4) * 56 + wid * 7) * 512), 0, 4 * 56 * 1024, 0x00020000);
  const __amdgpu_buffer_rsrc_t wr = __builtin_amdgcn_make_buffer_rsrc((void*)((const u16*)(ws + WS_WD) + ((size_t)(nt2 * 4) * 56 + wid * 7) * 512), 0, 4 * 56 * 1024, 0x00020000);
#define G2_LD(rs, i, k, aux) __builtin_bit_cast(bf16x8, __builtin_amdgcn_raw_buffer_load_b128(rs, vo, ((i) * 56 + (k)) * 1024, aux))
  bf16x8 wd[7][4];

  {
    char* cbase = smem + ch * CV_CH_STRIDE;
    u16* ucp = (u16*)(cbase + CV_UCP);
    u16* kr = (u16*)(cbase + CV_KR);
    const u32x4* uraw = (const u32x4*)(cbase + CV_URAW);
    const float* kraw = (const float*)(cbase + CV_KRAW);
    const float* kmax = (const float*)(cbase + CV_MAX);
    const float dsk = bf2f((u16)pds);
    float kscale, kinv;
    {
      const float mx = fmaxf(fmaxf(kmax[0], kmax[1]), fmaxf(kmax[2], kmax[3]));
      int e = (int)((__float_as_uint(mx) >> 23) & 0xff);
      e = min(max(e, 20), 240);
      kscale = __uint_as_float((u32)(127 + 13 + 127 - e) << 23);
      kinv = __uint_as_float((u32)(e - 13) << 23);
    }
#pragma unroll
    for (int it = 0; it < 2; ++it) {
      const int tb = it ? 256 + (t2 & 3) : t2;
      u32 w[8];
      u32x4 a = uraw[tb], b = uraw[tb + 1];
#pragma unroll
      for (int j = 0; j < 4; ++j) { w[j] = a[j]; w[4 + j] = b[j]; }
      if (it == 0 || t2 < 4) {
#pragma unroll
        for (int c = 0; c < 8; ++c) {
          u32x4 o;
#pragma unroll
          for (int j = 0; j < 4; ++j)
            o[j] = (c & 1) ? w[j + (c + 1) / 2] : __builtin_amdgcn_alignbit(w[j + c / 2 + 1], w[j + c / 2], 16);
          *(u32x4*)(ucp + c * UC_LEN + tb * 8) = o;
        }
      }
    }
    {
      float kk[8];
#pragma unroll
      for (int j = 0; j < 8; ++j) {
        const int jj = 2047 - (t2 * 8 + j);
        const int cbi = (int)f2bf((float)jj) - 17024;
        kk[j] = kraw[jj < 128 ? jj : cbi] * kscale;
      }
      f16x8 o;
#pragma unroll
      for (int j = 0; j < 8; ++j) o[j] = (_Float16)kk[j];
      *(f16x8*)(kr + t2 * 8 + 8 * (t2 >> 2)) = o;
      if (t2 < 128) { const int tb = t2 + 256; *(u32x4*)(kr + tb * 8 + 8 * (tb >> 2)) = u32x4{0, 0, 0, 0}; }
    }
    asm volatile("s_waitcnt vmcnt(0)" ::: "memory");
    RAW_BAR();
    if (tid == 0) __hip_atomic_fetch_add((gu32*)(ws + WS_CNT + mt * 256), 1u, __ATOMIC_RELAXED, __HIP_MEMORY_SCOPE_AGENT);
    DIAG_STAMP(4);
    const int r = lane & 31, h = lane >> 5;
    const u16* ap = ucp + (r & 7) * UC_LEN + 8 * h + 8 * (r >> 3);
    const int bofs0 = 2048 - 32 * (r + 1) + 8 * h + 8 * (63 - r);
    const int bofs1 = bofs0 - 1024 - 256;
    f32x16 acc0 = {}, acc1 = {};
    unsigned hcnt = 0;
    {
      const u16* apw = ap + 16 * (w4 * 16);
      const u16* bp0 = kr + bofs0 + 16 * (w4 * 16) + 8 * (w4 * 8);
      const u16* bp1 = kr + bofs1 + 16 * (w4 * 16) + 8 * (w4 * 8);
      auto lda = [&](int s) -> f16x8 { return *(const f16x8*)(apw + 16 * s); };
      auto ldb = [&](const u16* b, int s) -> f16x8 { return *(const f16x8*)(b + 16 * s + 8 * (s >> 1)); };
      f16x8 a0 = lda(0), p0 = ldb(bp0, 0), q0 = ldb(bp1, 0), c0 = lda(64);
      f16x8 a1 = lda(1), p1 = ldb(bp0, 1), q1 = ldb(bp1, 1), c1 = lda(65);
#pragma unroll
      for (int it = 0; it < 8; ++it) {
        f16x8 na0, na1, nc0, nc1, np0, np1, nq0, nq1;
        if (it < 7) {
          const int sn = 2 * it + 2;
          na0 = lda(sn); np0 = ldb(bp0, sn); nq0 = ldb(bp1, sn); nc0 = lda(sn + 64);
          na1 = lda(sn + 1); np1 = ldb(bp0, sn + 1); nq1 = ldb(bp1, sn + 1); nc1 = lda(sn + 65);
#pragma unroll
          for (int i = 0; i < 4; ++i) wd[it][i] = G2_LD(wr, i, it, 0);
        }
        if (it == 7) hcnt = __hip_atomic_load((gu32*)(ws + WS_CNT + (mt2 >> 1) * 256), __ATOMIC_RELAXED, __HIP_MEMORY_SCOPE_AGENT);
        __builtin_amdgcn_sched_barrier(0);
        acc0 = __builtin_amdgcn_mfma_f32_32x32x16_f16(a0, p0, acc0, 0, 0, 0);
        acc1 = __builtin_amdgcn_mfma_f32_32x32x16_f16(a0, q0, acc1, 0, 0, 0);
        acc1 = __builtin_amdgcn_mfma_f32_32x32x16_f16(c0, p0, acc1, 0, 0, 0);
        acc0 = __builtin_amdgcn_mfma_f32_32x32x16_f16(a1, p1, acc0, 0, 0, 0);
        acc1 = __builtin_amdgcn_mfma_f32_32x32x16_f16(a1, q1, acc1, 0, 0, 0);
        acc1 = __builtin_amdgcn_mfma_f32_32x32x16_f16(c1, p1, acc1, 0, 0, 0);
        __builtin_amdgcn_sched_barrier(0);
        if (it < 7) { a0 = na0; a1 = na1; c0 = nc0; c1 = nc1; p0 = np0; p1 = np1; q0 = nq0; q1 = nq1; }
      }
    }
    DIAG_STAMP(5);
    RAW_BAR();
    float* red = (float*)cbase;
#pragma unroll
    for (int g = 0; g < 4; ++g) {
      f32x4 v0 = {acc0[4 * g], acc0[4 * g + 1], acc0[4 * g + 2], acc0[4 * g + 3]};
      f32x4 v1 = {acc1[4 * g], acc1[4 * g + 1], acc1[4 * g + 2], acc1[4 * g + 3]};
      *(f32x4*)(red + (w4 * 32 + r) * 36 + 8 * g + 4 * h) = v0;
      *(f32x4*)(red + ((4 + w4) * 32 + r) * 36 + 8 * g + 4 * h) = v1;
    }
    if (tid == 511 && hcnt < 16u) {
      gu32* cnt = (gu32*)(ws + WS_CNT + (mt2 >> 1) * 256);
      for (unsigned spins = 0; spins < (1u << 22); ++spins) {
        if (__hip_atomic_load(cnt, __ATOMIC_RELAXED, __HIP_MEMORY_SCOPE_AGENT) >= 16u) break;
        __builtin_amdgcn_s_sleep(1);
      }
    }
    RAW_BAR();
    DIAG_STAMP2(1);
    u16* at = (u16*)(ws + WS_AT) + (size_t)d * L_;
    {
      const int c = t2;
      const int T = c >> 7, a = (c >> 2) & 31, b0 = (c & 3) * 8;
      f32x4 s0 = {}, s1 = {};
      const float* p = red + (T * 4 * 32 + a) * 36 + b0;
#pragma unroll
      for (int w = 0; w < 4; ++w) { s0 += *(const f32x4*)(p + w * 32 * 36); s1 += *(const f32x4*)(p + w * 32 * 36 + 4); }
      u32x4 o;
#pragma unroll
      for (int j = 0; j < 4; ++j) {
        const f2 y = unpack2((j < 2) ? pack2(s0[2 * j] * kinv, s0[2 * j + 1] * kinv) : pack2(s1[2 * j - 4] * kinv, s1[2 * j - 3] * kinv));
        const f2 us = unpack2(uv0[j]) * dsk;
        const f2 t = unpack2(pack2(us.x, us.y)) + y;
        u32 ov = pack2(t.x, t.y);
        if ((ov & 0xffffu) == 0xffffu) ov &= 0xffffffc0u;
        if ((ov >> 16) == 0xffffu) ov &= 0xffc0ffffu;
        o[j] = ov;
      }
      st16_wt(at + c * 8, o);
    }
  }
  DIAG_STAMP2(2);

  {
    f32x4 acc[4][4] = {};
    bf16x8 hb[4][4];
#pragma unroll
    for (int k = 0; k < 4; ++k)
#pragma unroll
      for (int i = 0; i < 4; ++i) hb[k][i] = G2_LD(hr, i, k, 16);
    const int m = tid >> 3, n0 = (tid & 7) * 8;
    const size_t go = (size_t)(mt2 * 64 + m) * D_ + nt2 * 64 + n0;
    const u32x4 xv = *(const u32x4*)(x + go);
    __builtin_amdgcn_sched_barrier(0);
#pragma unroll
    for (int k = 0; k < 7; ++k) {
#pragma unroll
      for (int j = 0; j < 4; ++j)
#pragma unroll
        for (int i = 0; i < 4; ++i) acc[j][i] = __builtin_amdgcn_mfma_f32_16x16x32_bf16(wd[k][j], hb[k & 3][i], acc[j][i], 0, 0, 0);
      if (k < 3) {
#pragma unroll
        for (int i = 0; i < 4; ++i) hb[k][i] = G2_LD(hr, i, k + 4, 16);
      }
      __builtin_amdgcn_sched_barrier(0);
    }
    DIAG_STAMP2(3);
    u32 a0, a1, a2, a3, a4, a5, a6, a7;
    const u16* at = (const u16*)(ws + WS_AT) + (size_t)(nt2 * 64 + n0) * L_ + mt2 * 64 + m;
#define A_LOADS(tail)                                                                                              \
  asm volatile("global_load_ushort %0, %8, off sc1\n\t"                                                            \
               "global_load_ushort %1, %9, off sc1\n\t"                                                            \
               "global_load_ushort %2, %10, off sc1\n\t"                                                           \
               "global_load_ushort %3, %11, off sc1\n\t"                                                           \
               "global_load_ushort %4, %12, off sc1\n\t"                                                           \
               "global_load_ushort %5, %13, off sc1\n\t"                                                           \
               "global_load_ushort %6, %14, off sc1\n\t"                                                           \
               "global_load_ushort %7, %15, off sc1" tail                                                          \
               : "=&v"(a0), "=&v"(a1), "=&v"(a2), "=&v"(a3), "=&v"(a4), "=&v"(a5), "=&v"(a6), "=&v"(a7)            \
               : "v"(at), "v"(at + L_), "v"(at + 2 * L_), "v"(at + 3 * L_), "v"(at + 4 * L_), "v"(at + 5 * L_),   \
                 "v"(at + 6 * L_), "v"(at + 7 * L_)                                                                \
               : "memory")
    A_LOADS("");
    RAW_BAR();
    float* red = (float*)smem;
    float* rp = red + (wid * 64 + (lane & 15)) * 68 + 4 * (lane >> 4);
#pragma unroll
    for (int j = 0; j < 4; ++j)
#pragma unroll
      for (int i = 0; i < 4; ++i) *(f32x4*)(rp + i * 16 * 68 + j * 16) = acc[j][i];
    asm volatile("s_waitcnt vmcnt(0)" : "+v"(a0), "+v"(a1), "+v"(a2), "+v"(a3), "+v"(a4), "+v"(a5), "+v"(a6), "+v"(a7)::"memory");
    for (unsigned spins = 0; spins < (1u << 20); ++spins) {
      const bool pend = (a0 == 0xffffu) | (a1 == 0xffffu) | (a2 == 0xffffu) | (a3 == 0xffffu) | (a4 == 0xffffu) |
                        (a5 == 0xffffu) | (a6 == 0xffffu) | (a7 == 0xffffu);
      if (!__any(pend)) break;
      __builtin_amdgcn_s_sleep(2);
      A_LOADS("\n\ts_waitcnt vmcnt(0)");
    }
    RAW_BAR();
    DIAG_STAMP2(4);
    f32x4 p0 = {}, p1 = {};
#pragma unroll
    for (int w = 0; w < 8; ++w) {
      p0 += *(const f32x4*)(red + (w * 64 + m) * 68 + n0);
      p1 += *(const f32x4*)(red + (w * 64 + m) * 68 + n0 + 4);
    }
    const u32 av[8] = {a0, a1, a2, a3, a4, a5, a6, a7};
    u32x4 o;
#pragma unroll
    for (int j = 0; j < 4; ++j) {
      const f2 xx = unpack2(xv[j]);
      const f2 aa = f2{__uint_as_float(av[2 * j] << 16), __uint_as_float(av[2 * j + 1] << 16)};
      const f2 r1 = xx + aa;
      const f2 bb = unpack2((j < 2) ? pack2(p0[2 * j], p0[2 * j + 1]) : pack2(p1[2 * j - 4], p1[2 * j - 3]));
      const f2 r2 = unpack2(pack2(r1.x, r1.y)) + bb;
      o[j] = pack2(r2.x, r2.y);
    }
    *(u32x4*)(out + go) = o;
  }
}

extern "C" void kernel_launch(void* const* d_in, const int* in_sizes, int n_in, void* d_out, int out_size,
                              void* d_ws, size_t ws_size, hipStream_t stream) {
  const u16* x = (const u16*)d_in[0];
  const u16* logA = (const u16*)d_in[1];
  const u16* Aim = (const u16*)d_in[2];
  const u16* Cre = (const u16*)d_in[3];
  const u16* Cim = (const u16*)d_in[4];
  const u16* logdt = (const u16*)d_in[5];
  const u16* dskip = (const u16*)d_in[6];
  const u16* wg = (const u16*)d_in[8];
  const u16* wu = (const u16*)d_in[9];
  const u16* wd = (const u16*)d_in[10];
  char* ws = (char*)d_ws;
  (void)hipFuncSetAttribute((const void*)k2_kernel, hipFuncAttributeMaxDynamicSharedMemorySize, K2_LDS);
  k1_kernel<<<K1_LN_BLOCKS + K1_WT_BLOCKS + K1_TC_BLOCKS + K1_KG_BLOCKS, 256, 0, stream>>>(x, logA, Aim, Cre, Cim, logdt, wg, wu, wd, ws);
  k2_kernel<<<256, 512, K2_LDS, stream>>>(x, dskip, wg, wu, ws, (u16*)d_out);
}
```

```cpp
#include <hip/hip_runtime.h>
#include <hip/hip_bf16.h>
#define WT_STORES
#define CV_PRIO 1
#define G2_PRIO 3

typedef unsigned short u16;
typedef unsigned int u32;
using bf16x8 = __attribute__((ext_vector_type(8))) short;
using f16x8 = __attribute__((ext_vector_type(8))) _Float16;
using f16x4 = __attribute__((ext_vector_type(4))) _Float16;
using f32x4 = __attribute__((ext_vector_type(4))) float;
using f32x16 = __attribute__((ext_vector_type(16))) float;
using u32x4 = __attribute__((ext_vector_type(4))) unsigned int;
using u32x2 = __attribute__((ext_vector_type(2))) unsigned int;
typedef float f2 __attribute__((ext_vector_type(2)));
typedef __attribute__((address_space(1))) unsigned gu32;

constexpr int L_ = 2048, D_ = 512, N_ = 128, F_ = 1792;
constexpr int KC_STRIDE = 648;

constexpr size_t WS_CNT = 0;
constexpr size_t WS_XNT = 4096;
constexpr size_t WS_XNTR = WS_XNT + (size_t)L_ * D_ * 2;
constexpr size_t WS_KC = WS_XNTR + (size_t)L_ * D_ * 2;
constexpr size_t WS_H = WS_KC + (size_t)D_ * KC_STRIDE * 4;
constexpr size_t WS_WG = WS_H + (size_t)L_ * F_ * 2;
constexpr size_t WS_WU = WS_WG + (size_t)D_ * F_ * 2;
constexpr size_t WS_WD = WS_WU + (size_t)D_ * F_ * 2;
constexpr size_t WS_AT = WS_WD + (size_t)D_ * F_ * 2;

#define DIAG_START(k)
#define DIAG_STAMP(i)
#define DIAG_END(wsp)
#define DIAG_STAMP2(i)

__device__ __forceinline__ float bf2f(u16 v) { return __uint_as_float(((u32)v) << 16); }
__device__ __forceinline__ u16 f2bf(float f) {
  __bf16 b = (__bf16)f;
  return __builtin_bit_cast(u16, b);
}
__device__ __forceinline__ float rbf(float f) { return bf2f(f2bf(f)); }
__device__ __forceinline__ u32 pack2(float lo, float hi) {
  typedef __bf16 b2 __attribute__((ext_vector_type(2)));
  b2 r = __builtin_convertvector(f2{lo, hi}, b2);
  return __builtin_bit_cast(u32, r);
}
__device__ __forceinline__ f2 unpack2(u32 w) { return f2{__uint_as_float(w << 16), __uint_as_float(w & 0xffff0000u)}; }

__device__ __forceinline__ void st16_wt(void* p, u32x4 v) { asm volatile("global_store_dwordx4 %0, %1, off sc1\n\ts_nop 1" ::"v"(p), "v"(v) : "memory"); }
__device__ __forceinline__ void st16(void* p, u32x4 v) { st16_wt(p, v); }
__device__ __forceinline__ void st8(void* p, u32x2 v) { asm volatile("global_store_dwordx2 %0, %1, off sc1\n\ts_nop 1" ::"v"(p), "v"(v) : "memory"); }

#define DPP_F(v, ctrl) __builtin_bit_cast(float, __builtin_amdgcn_update_dpp(0, __builtin_bit_cast(int, v), ctrl, 0xf, 0xf, true))
__device__ __forceinline__ float wave_sum(float v) {
  v += DPP_F(v, 0xB1);
  v += DPP_F(v, 0x4E);
  v += DPP_F(v, 0x141);
  v += DPP_F(v, 0x140);
  const int iv = __builtin_bit_cast(int, v);
  return (__builtin_bit_cast(float, __builtin_amdgcn_readlane(iv, 0)) + __builtin_bit_cast(float, __builtin_amdgcn_readlane(iv, 16))) +
         (__builtin_bit_cast(float, __builtin_amdgcn_readlane(iv, 32)) + __builtin_bit_cast(float, __builtin_amdgcn_readlane(iv, 48)));
}
__device__ __forceinline__ float wave_max(float v) {
  v = fmaxf(v, DPP_F(v, 0xB1));
  v = fmaxf(v, DPP_F(v, 0x4E));
  v = fmaxf(v, DPP_F(v, 0x141));
  v = fmaxf(v, DPP_F(v, 0x140));
  const int iv = __builtin_bit_cast(int, v);
  return fmaxf(fmaxf(__builtin_bit_cast(float, __builtin_amdgcn_readlane(iv, 0)), __builtin_bit_cast(float, __builtin_amdgcn_readlane(iv, 16))),
               fmaxf(__builtin_bit_cast(float, __builtin_amdgcn_readlane(iv, 32)), __builtin_bit_cast(float, __builtin_amdgcn_readlane(iv, 48))));
}

constexpr int K1_LN_BLOCKS = 128;
constexpr int K1_WT_BLOCKS = 336;
constexpr int K1_KG_BLOCKS = 512;

__device__ __forceinline__ void cmul6(f2& r0, f2& r1, f2& r2, f2& r3, f2& r4, f2& r5, f2 a0, f2 b0, f2 a1, f2 b1,
                                      f2 a2, f2 b2, f2 a3, f2 b3, f2 a4, f2 b4, f2 a5, f2 b5) {
  asm("v_pk_mul_f32 %0, %6, %7 op_sel:[1,1] op_sel_hi:[1,0] neg_lo:[0,1]\n\t"
      "v_pk_mul_f32 %1, %8, %9 op_sel:[1,1] op_sel_hi:[1,0] neg_lo:[0,1]\n\t"
      "v_pk_mul_f32 %2, %10, %11 op_sel:[1,1] op_sel_hi:[1,0] neg_lo:[0,1]\n\t"
      "v_pk_mul_f32 %3, %12, %13 op_sel:[1,1] op_sel_hi:[1,0] neg_lo:[0,1]\n\t"
      "v_pk_mul_f32 %4, %14, %15 op_sel:[1,1] op_sel_hi:[1,0] neg_lo:[0,1]\n\t"
      "v_pk_mul_f32 %5, %16, %17 op_sel:[1,1] op_sel_hi:[1,0] neg_lo:[0,1]\n\t"
      "v_pk_fma_f32 %0, %6, %7, %0 op_sel:[0,0,0] op_sel_hi:[0,1,1]\n\t"
      "v_pk_fma_f32 %1, %8, %9, %1 op_sel:[0,0,0] op_sel_hi:[0,1,1]\n\t"
      "v_pk_fma_f32 %2, %10, %11, %2 op_sel:[0,0,0] op_sel_hi:[0,1,1]\n\t"
      "v_pk_fma_f32 %3, %12, %13, %3 op_sel:[0,0,0] op_sel_hi:[0,1,1]\n\t"
      "v_pk_fma_f32 %4, %14, %15, %4 op_sel:[0,0,0] op_sel_hi:[0,1,1]\n\t"
      "v_pk_fma_f32 %5, %16, %17, %5 op_sel:[0,0,0] op_sel_hi:[0,1,1]\n\t"
      "s_nop 1"
      : "=&v"(r0), "=&v"(r1), "=&v"(r2), "=&v"(r3), "=&v"(r4), "=&v"(r5)
      : "v"(a0), "v"(b0), "v"(a1), "v"(b1), "v"(a2), "v"(b2), "v"(a3), "v"(b3), "v"(a4), "v"(b4), "v"(a5), "v"(b5));
}
__device__ __forceinline__ void cmul2(f2& r0, f2& r1, f2 a0, f2 b0, f2 a1, f2 b1) {
  asm("v_pk_mul_f32 %0, %2, %3 op_sel:[1,1] op_sel_hi:[1,0] neg_lo:[0,1]\n\t"
      "v_pk_mul_f32 %1, %4, %5 op_sel:[1,1] op_sel_hi:[1,0] neg_lo:[0,1]\n\t"
      "s_nop 0\n\t"
      "v_pk_fma_f32 %0, %2, %3, %0 op_sel:[0,0,0] op_sel_hi:[0,1,1]\n\t"
      "v_pk_fma_f32 %1, %4, %5, %1 op_sel:[0,0,0] op_sel_hi:[0,1,1]\n\t"
      "s_nop 1"
      : "=&v"(r0), "=&v"(r1)
      : "v"(a0), "v"(b0), "v"(a1), "v"(b1));
}
__device__ __forceinline__ u32x2 split_h(f2 v) {
  typedef _Float16 h2 __attribute__((ext_vector_type(2)));
  const h2 h = __builtin_bit_cast(h2, __builtin_amdgcn_cvt_pkrtz(v.x, v.y));
  const f2 r = v - f2{(float)h.x, (float)h.y};
  return u32x2{__builtin_bit_cast(u32, h), __builtin_bit_cast(u32, __builtin_amdgcn_cvt_pkrtz(r.x, r.y))};
}
__device__ __forceinline__ f2 cexp_(float zr, float zi, float t) {
  const float m = __builtin_amdgcn_exp2f((zr * t) * 1.44269504088896341f);
  float r = (zi * t) * 0.15915494309189535f;
  r -= rintf(r);
  return f2{m * __builtin_amdgcn_cosf(r), m * __builtin_amdgcn_sinf(r)};
}

__global__ __launch_bounds__(256) void k1_kernel(const u16* __restrict__ x, const u16* __restrict__ logA,
                                                 const u16* __restrict__ Aim, const u16* __restrict__ Cre,
                                                 const u16* __restrict__ Cim, const u16* __restrict__ logdt,
                                                 const u16* __restrict__ wg, const u16* __restrict__ wu,
                                                 const u16* __restrict__ wd, char* __restrict__ ws) {
  __shared__ __attribute__((aligned(16))) char smem[18448];
  const int tid = threadIdx.x, lane = tid & 63, wid = tid >> 6;
  DIAG_START(0)
  int bid = blockIdx.x - K1_KG_BLOCKS;
  if (bid >= 0 && bid < K1_LN_BLOCKS) {
    __builtin_amdgcn_s_setprio(3);
    if (bid == 0 && tid < 16) *(u32*)(ws + WS_CNT + tid * 256) = 0u;
#pragma unroll
    for (int it = 0; it < 4; ++it) st16(ws + WS_AT + ((size_t)bid * 1024 + it * 256 + tid) * 16, u32x4{0xffffffffu, 0xffffffffu, 0xffffffffu, 0xffffffffu});
    u16* tile = (u16*)smem;
    u16* xnt = (u16*)(ws + WS_XNT);
    const int r0 = bid * 16;
    u32x4 v[4];
#pragma unroll
    for (int i = 0; i < 4; ++i) v[i] = *(const u32x4*)(x + (size_t)(r0 + wid * 4 + i) * D_ + lane * 8);
#pragma unroll
    for (int i = 0; i < 4; ++i) {
      const int rl = wid * 4 + i;
      f2 f[4];
#pragma unroll
      for (int j = 0; j < 4; ++j) f[j] = unpack2(v[i][j]);
      const f2 s2 = (f[0] + f[1]) + (f[2] + f[3]);
      const float mean = wave_sum(s2.x + s2.y) * (1.f / D_);
      f2 q2 = {0.f, 0.f};
#pragma unroll
      for (int j = 0; j < 4; ++j) { const f2 t = f[j] - mean; q2 = __builtin_elementwise_fma(t, t, q2); }
      const float q = wave_sum(q2.x + q2.y);
      const float mb = rbf(mean);
      const float vb = rbf(q * (1.f / D_));
      const float sd = rbf(sqrtf(rbf(vb + 1e-5f)));
      const float rs = __builtin_amdgcn_rcpf(sd);
      u32x4 o;
#pragma unroll
      for (int j = 0; j < 4; ++j) {
        const f2 t = f[j] - mb;
        const f2 a = unpack2(pack2(t.x, t.y));
        f2 qq = a * rs;
        const f2 rem = __builtin_elementwise_fma(-qq, f2{sd, sd}, a);
        qq = __builtin_elementwise_fma(rem, f2{rs, rs}, qq);
        o[j] = pack2(qq.x, qq.y);
      }
      *(u32x4*)(tile + rl * 520 + lane * 8) = o;
    }
    DIAG_STAMP(1);
    __syncthreads();
#pragma unroll
    for (int it = 0; it < 4; ++it) {
      const int c = tid + it * 256;
      const u32x4 o = *(const u32x4*)(tile + (c & 15) * 520 + (c >> 4) * 8);
      st16(xnt + ((size_t)bid * 1024 + c) * 8, o);
    }
    u16* xtr = (u16*)(ws + WS_XNTR);
#pragma unroll
    for (int it = 0; it < 4; ++it) {
      const int c = tid + it * 256;
      const int d = c >> 1, g = c & 1;
      u32x4 o;
#pragma unroll
      for (int j = 0; j < 4; ++j)
        o[j] = (u32)tile[(g * 8 + 2 * j) * 520 + d] | ((u32)tile[(g * 8 + 2 * j + 1) * 520 + d] << 16);
      st16(xtr + ((size_t)bid * 1024 + c) * 8, o);
    }
    DIAG_END(ws);
    return;
  }
  bid -= K1_LN_BLOCKS;
  if (bid >= 0) {
    __builtin_amdgcn_s_setprio(2);
    int task = bid * 4 + wid;
    const u16* W; u16* Wt; int Kd, Nd;
    if (task < 448) { W = wg; Wt = (u16*)(ws + WS_WG); Kd = D_; Nd = F_; }
    else if (task < 896) { task -= 448; W = wu; Wt = (u16*)(ws + WS_WU); Kd = D_; Nd = F_; }
    else { task -= 896; W = wd; Wt = (u16*)(ws + WS_WD); Kd = F_; Nd = D_; }
    const int nks = Kd / 32;
    const int ks = task % nks, nb = task / nks;
    const int col = nb * 64 + lane;
    u16 v[32];
#pragma unroll
    for (int k = 0; k < 32; ++k) v[k] = W[(size_t)(ks * 32 + k) * Nd + col];
#pragma unroll
    for (int q = 0; q < 4; ++q) {
      u32x4 o;
#pragma unroll
      for (int j = 0; j < 4; ++j) o[j] = (u32)v[q * 8 + 2 * j] | ((u32)v[q * 8 + 2 * j + 1] << 16);
      st16(Wt + ((size_t)((col >> 4) * nks + ks) * 64 + q * 16 + (col & 15)) * 8, o);
    }
    DIAG_END(ws);
    return;
  }
  bid = blockIdx.x;
  {
    const int d = bid;
    float4* prm = (float4*)(smem + 16384);
    float* cmx = (float*)(smem + 16384 + 2048);
    if (tid < 128) {
      const u16 la = logA[d * N_ + tid], ai = Aim[d * N_ + tid], cr = Cre[d * N_ + tid], ci = Cim[d * N_ + tid];
      const float dt = rbf(expf(bf2f(logdt[d])));
      const float ar = -rbf(expf(bf2f(la)));
      const float c0 = bf2f(cr) * dt, c1 = bf2f(ci) * dt;
      prm[tid] = make_float4(ar * dt, bf2f(ai) * dt, c0, c1);
      const float mx = wave_max(fmaxf(fabsf(c0), fabsf(c1)));
      if (lane == 0) cmx[wid] = mx;
    }
    __syncthreads();
    DIAG_STAMP(1);
    float cs, cinv;
    {
      int e = (int)((__float_as_uint(fmaxf(cmx[0], cmx[1])) >> 23) & 0xff);
      e = min(max(e, 2), 252);
      cs = __uint_as_float((u32)(254 - e) << 23);
      cinv = __uint_as_float((u32)e << 23);
    }
    const int kq = lane >> 4, idx = lane & 15;
    f32x4 acc[4] = {};
    const float ty = (float)idx, te = (float)(idx == 0 ? 256 : 16 * idx);
    float4 pr[8];
#pragma unroll
    for (int s = 0; s < 8; ++s) pr[s] = prm[wid * 32 + (s >> 2) * 16 + kq * 4 + (s & 3)];
#pragma unroll
    for (int t = 0; t < 2; ++t) {
      u32x4 Xh[4], Xl[4], Yh[4], Yl[4];
#pragma unroll
      for (int s = 0; s < 4; s += 2) {
        const float4 pa = pr[t * 4 + s], pb = pr[t * 4 + s + 1];
        const f2 Ca = f2{pa.z, -pa.w} * cs, Cb = f2{pb.z, -pb.w} * cs;
        f2 Ya[4], Yb[4], Xa[4], Xb[4];
        Ya[0] = cexp_(pa.x, pa.y, ty); Yb[0] = cexp_(pb.x, pb.y, ty);
        f2 Ea = cexp_(pa.x, -pa.y, te), Eb = cexp_(pb.x, -pb.y, te);
        f2 Fa = (idx == 0) ? f2{1.f, 0.f} : Ea, Fb = (idx == 0) ? f2{1.f, 0.f} : Eb;
#pragma unroll
        for (int e = 0; e < 3; ++e) {
          f2 nEa, nEb;
          cmul6(Xa[e], Xb[e], Ya[e + 1], Yb[e + 1], nEa, nEb, Ca, Fa, Cb, Fb, Ya[e], Ya[e], Yb[e], Yb[e], Ea, Ea, Eb, Eb);
          Ea = nEa; Eb = nEb; Fa = nEa; Fb = nEb;
        }
        cmul2(Xa[3], Xb[3], Ca, Ea, Cb, Eb);
#pragma unroll
        for (int e = 0; e < 4; ++e) {
          const u32x2 xa2 = split_h(Xa[e]), xb2 = split_h(Xb[e]), ya2 = split_h(Ya[e]), yb2 = split_h(Yb[e]);
          Xh[e][s] = xa2[0]; Xl[e][s] = xa2[1]; Xh[e][s + 1] = xb2[0]; Xl[e][s + 1] = xb2[1];
          Yh[e][s] = ya2[0]; Yl[e][s] = ya2[1]; Yh[e][s + 1] = yb2[0]; Yl[e][s + 1] = yb2[1];
        }
      }
#pragma unroll
      for (int e = 0; e < 4; ++e) acc[e] = __builtin_amdgcn_mfma_f32_16x16x32_f16(__builtin_bit_cast(f16x8, Xl[e]), __builtin_bit_cast(f16x8, Yh[e]), acc[e], 0, 0, 0);
#pragma unroll
      for (int e = 0; e < 4; ++e) acc[e] = __builtin_amdgcn_mfma_f32_16x16x32_f16(__builtin_bit_cast(f16x8, Xh[e]), __builtin_bit_cast(f16x8, Yl[e]), acc[e], 0, 0, 0);
#pragma unroll
      for (int e = 0; e < 4; ++e) acc[e] = __builtin_amdgcn_mfma_f32_16x16x32_f16(__builtin_bit_cast(f16x8, Xh[e]), __builtin_bit_cast(f16x8, Yh[e]), acc[e], 0, 0, 0);
    }
    DIAG_STAMP(2);
    float* red = (float*)smem;
#pragma unroll
    for (int e = 0; e < 4; ++e)
#pragma unroll
      for (int r = 0; r < 4; ++r) red[(wid * 4 + e) * 256 + ((lane >> 4) * 4 + r) * 16 + (lane & 15)] = acc[e][r];
    __syncthreads();
    float* kc = (float*)(ws + WS_KC) + (size_t)d * KC_STRIDE;
#pragma unroll
    for (int e = 0; e < 4; ++e) {
      const float v = (2.f * cinv) * ((red[(0 * 4 + e) * 256 + tid] + red[(1 * 4 + e) * 256 + tid]) +
                                      (red[(2 * 4 + e) * 256 + tid] + red[(3 * 4 + e) * 256 + tid]));
      if (e == 0) kc[tid] = v;
      else if (tid >= 128) kc[128 * e + tid] = v;
      else if (e == 3 && tid == 0) kc[640] = v;
    }
    DIAG_END(ws);
  }
}

constexpr int G1_STAGE = 22 * 1024;
constexpr int G1_NBUF = 7;
constexpr int K2_LDS = G1_STAGE * G1_NBUF;

constexpr int UC_LEN = 2080;
constexpr int KR_LEN = 3840;
constexpr int CV_UCP = 0;
constexpr int CV_KR = CV_UCP + 8 * UC_LEN * 2;
constexpr int CV_URAW = CV_KR + KR_LEN * 2;
constexpr int CV_KRAW = CV_URAW + 4224;
constexpr int CV_MAX = CV_KRAW + 2592;
constexpr int CV_CH_STRIDE = 48128;
constexpr int H_STAGE = 2 * CV_CH_STRIDE;

__device__ __forceinline__ f2 gelu2(f2 g) {
  const float c0 = -2.f * 0.7978845608028654f * 1.44269504088896341f;
  const float c1 = c0 * 0.044715f;
  const f2 t = g * __builtin_elementwise_fma(g * g, f2{c1, c1}, f2{c0, c0});
  const f2 dn = f2{__builtin_amdgcn_exp2f(t.x), __builtin_amdgcn_exp2f(t.y)} + 1.f;
  return g * f2{__builtin_amdgcn_rcpf(dn.x), __builtin_amdgcn_rcpf(dn.y)};
}
__device__ __forceinline__ void glds16(const void* g, void* l) {
  __builtin_amdgcn_global_load_lds((const __attribute__((address_space(1))) u32*)g,
                                   (__attribute__((address_space(3))) u32*)l, 16, 0, 0);
}
__device__ __forceinline__ u32 bf2_to_h2(u32 w) {
  auto r = __builtin_amdgcn_cvt_pkrtz(__uint_as_float(w << 16), __uint_as_float(w & 0xffff0000u));
  return __builtin_bit_cast(u32, r);
}
#define RAW_BAR() do { asm volatile("s_waitcnt lgkmcnt(0)" ::: "memory"); __builtin_amdgcn_s_barrier(); } while (0)

__global__ __launch_bounds__(512, 2) void k2_kernel(const u16* __restrict__ x, const u16* __restrict__ dskip,
                                                    char* __restrict__ ws, u16* __restrict__ out) {
  extern __shared__ __attribute__((aligned(16))) char smem[];
  const int tid = threadIdx.x, lane = tid & 63;
  const int wid = __builtin_amdgcn_readfirstlane(tid >> 6);
  DIAG_START(1)
  const int bid = blockIdx.x;
  const int xcd = bid & 7, loc = bid >> 3;
  const int ch = wid >> 2, w4 = wid & 3;
  const int d = bid * 2 + ch;
  const int t2 = tid & 255;
  const int mt = (xcd >> 1) * 4 + (loc & 3);
  const int nt = (xcd & 1) * 8 + (loc >> 2);

  u32x4 uv0; float kv0, kv1, kv2; u32 pds;
  {
    const u16* xnt = (const u16*)(ws + WS_XNT);
    const u16* wgt = (const u16*)(ws + WS_WG);
    const u16* wut = (const u16*)(ws + WS_WU);
    auto src = [&](int f) -> const u16* {
      if (f < 8) return xnt + (size_t)(mt * 8 + f) * 16 * 512;
      const int s = f - 8;
      return ((s & 1) ? wut : wgt) + (size_t)(nt * 7 + (s >> 1)) * 16 * 512;
    };
    const u16* s0 = src(wid) + lane * 8;
    const u16* s1 = src(wid + 8) + lane * 8;
    const u16* s2 = src(wid < 6 ? wid + 16 : 21) + lane * 8;
    auto stage = [&](int ks) {
      char* base = smem + (ks % G1_NBUF) * G1_STAGE + wid * 1024 + lane * 16;
      glds16(s0 + ks * 512, base);
      glds16(s1 + ks * 512, base + 8192);
      if (wid < 6) glds16(s2 + ks * 512, base + 16384);
    };
#define G1_WAIT(n) do { if (wid < 6) asm volatile("s_waitcnt vmcnt(%0)" ::"n"(3 * (n)) : "memory"); \
                        else asm volatile("s_waitcnt vmcnt(%0)" ::"n"(2 * (n)) : "memory"); } while (0)
#pragma unroll
    for (int k = 0; k < G1_NBUF; ++k) stage(k);
    const int wm = wid >> 1, wn = wid & 1;
    f32x4 acc[7][2] = {};
    bf16x8 xa[2], wa[7], xb[2], wb[7];
    G1_WAIT(G1_NBUF - 1);
    RAW_BAR();
    DIAG_STAMP(1);
    {
      const char* base = smem + lane * 16;
#pragma unroll
      for (int i = 0; i < 2; ++i) xa[i] = *(const bf16x8*)(base + (wm * 2 + i) * 1024);
#pragma unroll
      for (int j = 0; j < 7; ++j) wa[j] = *(const bf16x8*)(base + (8 + wn * 7 + j) * 1024);
    }
#define G1_ITER(k, XC, WC, XN, WN, NLEFT)                                                          \
  do {                                                                                             \
    G1_WAIT(NLEFT);                                                                                \
    RAW_BAR();                                                                                     \
    const char* rb_ = smem + (((k) + 1) % G1_NBUF) * G1_STAGE + lane * 16;                         \
    _Pragma("unroll") for (int j = 0; j < 7; ++j) {                                                \
      acc[j][0] = __builtin_amdgcn_mfma_f32_16x16x32_bf16(WC[j], XC[0], acc[j][0], 0, 0, 0);       \
      if (j < 2) XN[j] = *(const bf16x8*)(rb_ + (wm * 2 + j) * 1024);                              \
      acc[j][1] = __builtin_amdgcn_mfma_f32_16x16x32_bf16(WC[j], XC[1], acc[j][1], 0, 0, 0);       \
      WN[j] = *(const bf16x8*)(rb_ + (8 + wn * 7 + j) * 1024);                                     \
      __builtin_amdgcn_sched_barrier(0);                                                           \
      if (j == 3 && (k) + G1_NBUF < 16) { stage((k) + G1_NBUF); __builtin_amdgcn_sched_barrier(0); } \
    }                                                                                              \
  } while (0)
    G1_ITER(0, xa, wa, xb, wb, 5);
    G1_ITER(1, xb, wb, xa, wa, 5);
    G1_ITER(2, xa, wa, xb, wb, 5);
    G1_ITER(3, xb, wb, xa, wa, 5);
    G1_ITER(4, xa, wa, xb, wb, 5);
    G1_ITER(5, xb, wb, xa, wa, 5);
    G1_ITER(6, xa, wa, xb, wb, 5);
    G1_ITER(7, xb, wb, xa, wa, 5);
    G1_ITER(8, xa, wa, xb, wb, 5);
    G1_ITER(9, xb, wb, xa, wa, 5);
    G1_ITER(10, xa, wa, xb, wb, 4);
    G1_ITER(11, xb, wb, xa, wa, 3);
    G1_ITER(12, xa, wa, xb, wb, 2);
    G1_ITER(13, xb, wb, xa, wa, 1);
    G1_ITER(14, xa, wa, xb, wb, 0);
    {
      const u16* ug = (const u16*)(ws + WS_XNTR) + (size_t)(t2 >> 1) * 8192 + d * 16 + (t2 & 1) * 8;
      const float* kc = (const float*)(ws + WS_KC) + (size_t)d * KC_STRIDE;
      uv0 = *(const u32x4*)ug;
      kv0 = kc[t2]; kv1 = kc[t2 + 256]; kv2 = (t2 < 129) ? kc[t2 + 512] : 0.f;
      pds = dskip[d];
    }
    __builtin_amdgcn_sched_barrier(0);
#pragma unroll
    for (int j = 0; j < 7; ++j)
#pragma unroll
      for (int i = 0; i < 2; ++i) acc[j][i] = __builtin_amdgcn_mfma_f32_16x16x32_bf16(wb[j], xb[i], acc[j][i], 0, 0, 0);
    DIAG_STAMP(2);
    RAW_BAR();
    f32x4* ex = (f32x4*)(smem + H_STAGE + 28672);
    if (wn == 0) {
#pragma unroll
      for (int i = 0; i < 2; ++i) ex[(wm * 2 + i) * 64 + lane] = acc[6][i];
    }
    char* hs = smem + H_STAGE;
    auto emit = [&](int t, const f32x4& g, const f32x4& u, int i) {
      u32x2 o;
#pragma unroll
      for (int p = 0; p < 2; ++p) {
        const f2 gb = unpack2(pack2(g[2 * p], g[2 * p + 1]));
        const f2 ub = unpack2(pack2(u[2 * p], u[2 * p + 1]));
        const f2 ge = gelu2(gb);
        const f2 hv = unpack2(pack2(ge.x, ge.y)) * ub;
        o[p] = pack2(hv.x, hv.y);
      }
      *(u32x2*)(hs + (((wm * 2 + i) * 7 + t) * 32 + (lane >> 5) * 16 + (lane & 15)) * 16 + ((lane >> 4) & 1) * 8) = o;
    };
    if (wn == 0) {
#pragma unroll
      for (int t = 0; t < 3; ++t)
#pragma unroll
        for (int i = 0; i < 2; ++i) emit(t, acc[2 * t][i], acc[2 * t + 1][i], i);
      RAW_BAR();
    } else {
#pragma unroll
      for (int t = 4; t < 7; ++t)
#pragma unroll
        for (int i = 0; i < 2; ++i) emit(t, acc[2 * t - 7][i], acc[2 * t - 6][i], i);
      RAW_BAR();
#pragma unroll
      for (int i = 0; i < 2; ++i) emit(3, ex[(wm * 2 + i) * 64 + lane], acc[0][i], i);
    }
    {
      char* cbase = smem + ch * CV_CH_STRIDE;
      u32x4* uraw = (u32x4*)(cbase + CV_URAW);
      float* kraw = (float*)(cbase + CV_KRAW);
      float* kmax = (float*)(cbase + CV_MAX);
      if (t2 < 4) { uraw[t2] = u32x4{0, 0, 0, 0}; uraw[260 + t2] = u32x4{0, 0, 0, 0}; }
      u32x4 h;
#pragma unroll
      for (int j = 0; j < 4; ++j) h[j] = bf2_to_h2(uv0[j]);
      uraw[4 + t2] = h;
      kraw[t2] = kv0;
      kraw[t2 + 256] = kv1;
      if (t2 < 136) kraw[t2 + 512] = kv2;
      const float mx = wave_max(fmaxf(fmaxf(fabsf(kv0), fabsf(kv1)), fabsf(kv2)));
      if (lane == 0) kmax[w4] = mx;
    }
    RAW_BAR();
    u16* hT = (u16*)(ws + WS_H);
#pragma unroll
    for (int it = 0; it < 4; ++it) {
      const int c = tid + it * 512;
      if (c < 1792) {
        const int ml = c / 224, rem = c - ml * 224;
        st16_wt(hT + ((size_t)(mt * 8 + ml) * 56 * 64 + (size_t)nt * 7 * 32 + rem) * 8, *(const u32x4*)(hs + c * 16));
      }
    }
  }
  DIAG_STAMP(3);

  const int mt2 = (xcd >> 1) * 8 + (xcd & 1) * 4 + (loc & 3);
  const int nt2 = loc >> 2;
  const int vo = lane * 16;
  const __amdgpu_buffer_rsrc_t hr = __builtin_amdgcn_make_buffer_rsrc((void*)((const u16*)(ws + WS_H) + ((size_t)(mt2 * 4) * 56 + wid * 7) * 512), 0, 4 * 56 * 1024, 0x00020000);
  const __amdgpu_buffer_rsrc_t wr = __builtin_amdgcn_make_buffer_rsrc((void*)((const u16*)(ws + WS_WD) + ((size_t)(nt2 * 4) * 56 + wid * 7) * 512), 0, 4 * 56 * 1024, 0x00020000);
#define G2_LD(rs, i, k, aux) __builtin_bit_cast(bf16x8, __builtin_amdgcn_raw_buffer_load_b128(rs, vo, ((i) * 56 + (k)) * 1024, aux))
  bf16x8 wd[7][4];

  {
    char* cbase = smem + ch * CV_CH_STRIDE;
    u16* ucp = (u16*)(cbase + CV_UCP);
    u16* kr = (u16*)(cbase + CV_KR);
    const u32x4* uraw = (const u32x4*)(cbase + CV_URAW);
    const float* kraw = (const float*)(cbase + CV_KRAW);
    const float* kmax = (const float*)(cbase + CV_MAX);
    const float dsk = bf2f((u16)pds);
    float kscale, kinv;
    {
      const float mx = fmaxf(fmaxf(kmax[0], kmax[1]), fmaxf(kmax[2], kmax[3]));
      int e = (int)((__float_as_uint(mx) >> 23) & 0xff);
      e = min(max(e, 20), 240);
      kscale = __uint_as_float((u32)(127 + 13 + 127 - e) << 23);
      kinv = __uint_as_float((u32)(e - 13) << 23);
    }
#pragma unroll
    for (int it = 0; it < 2; ++it) {
      const int tb = it ? 256 + (t2 & 3) : t2;
      u32 w[8];
      u32x4 a = uraw[tb], b = uraw[tb + 1];
#pragma unroll
      for (int j = 0; j < 4; ++j) { w[j] = a[j]; w[4 + j] = b[j]; }
      if (it == 0 || t2 < 4) {
#pragma unroll
        for (int c = 0; c < 8; ++c) {
          u32x4 o;
#pragma unroll
          for (int j = 0; j < 4; ++j)
            o[j] = (c & 1) ? w[j + (c + 1) / 2] : __builtin_amdgcn_alignbit(w[j + c / 2 + 1], w[j + c / 2], 16);
          *(u32x4*)(ucp + c * UC_LEN + tb * 8) = o;
        }
      }
    }
    {
      float kk[8];
#pragma unroll
      for (int j = 0; j < 8; ++j) {
        const int jj = 2047 - (t2 * 8 + j);
        const int cbi = (int)f2bf((float)jj) - 17024;
        kk[j] = kraw[jj < 128 ? jj : cbi] * kscale;
      }
      f16x8 o;
#pragma unroll
      for (int j = 0; j < 8; ++j) o[j] = (_Float16)kk[j];
      *(f16x8*)(kr + t2 * 8 + 8 * (t2 >> 2)) = o;
      if (t2 < 128) { const int tb = t2 + 256; *(u32x4*)(kr + tb * 8 + 8 * (tb >> 2)) = u32x4{0, 0, 0, 0}; }
    }
    asm volatile("s_waitcnt vmcnt(0)" ::: "memory");
    RAW_BAR();
    if (tid == 0) __hip_atomic_fetch_add((gu32*)(ws + WS_CNT + mt * 256), 1u, __ATOMIC_RELAXED, __HIP_MEMORY_SCOPE_AGENT);
    DIAG_STAMP(4);
    const int r = lane & 31, h = lane >> 5;
    const u16* ap = ucp + (r & 7) * UC_LEN + 8 * h + 8 * (r >> 3);
    const int bofs0 = 2048 - 32 * (r + 1) + 8 * h + 8 * (63 - r);
    const int bofs1 = bofs0 - 1024 - 256;
    f32x16 acc0 = {}, acc1 = {};
    unsigned hcnt = 0;
    {
      const u16* apw = ap + 16 * (w4 * 16);
      const u16* bp0 = kr + bofs0 + 16 * (w4 * 16) + 8 * (w4 * 8);
      const u16* bp1 = kr + bofs1 + 16 * (w4 * 16) + 8 * (w4 * 8);
      auto lda = [&](int s) -> f16x8 { return *(const f16x8*)(apw + 16 * s); };
      auto ldb = [&](const u16* b, int s) -> f16x8 { return *(const f16x8*)(b + 16 * s + 8 * (s >> 1)); };
      f16x8 a0 = lda(0), p0 = ldb(bp0, 0), q0 = ldb(bp1, 0), c0 = lda(64);
      f16x8 a1 = lda(1), p1 = ldb(bp0, 1), q1 = ldb(bp1, 1), c1 = lda(65);
#pragma unroll
      for (int it = 0; it < 8; ++it) {
        f16x8 na0, na1, nc0, nc1, np0, np1, nq0, nq1;
        if (it < 7) {
          const int sn = 2 * it + 2;
          na0 = lda(sn); np0 = ldb(bp0, sn); nq0 = ldb(bp1, sn); nc0 = lda(sn + 64);
          na1 = lda(sn + 1); np1 = ldb(bp0, sn + 1); nq1 = ldb(bp1, sn + 1); nc1 = lda(sn + 65);
#pragma unroll
          for (int i = 0; i < 4; ++i) wd[it][i] = G2_LD(wr, i, it, 0);
        }
        if (it == 7) hcnt = __hip_atomic_load((gu32*)(ws + WS_CNT + (mt2 >> 1) * 256), __ATOMIC_RELAXED, __HIP_MEMORY_SCOPE_AGENT);
        __builtin_amdgcn_sched_barrier(0);
        acc0 = __builtin_amdgcn_mfma_f32_32x32x16_f16(a0, p0, acc0, 0, 0, 0);
        acc1 = __builtin_amdgcn_mfma_f32_32x32x16_f16(a0, q0, acc1, 0, 0, 0);
        acc1 = __builtin_amdgcn_mfma_f32_32x32x16_f16(c0, p0, acc1, 0, 0, 0);
        acc0 = __builtin_amdgcn_mfma_f32_32x32x16_f16(a1, p1, acc0, 0, 0, 0);
        acc1 = __builtin_amdgcn_mfma_f32_32x32x16_f16(a1, q1, acc1, 0, 0, 0);
        acc1 = __builtin_amdgcn_mfma_f32_32x32x16_f16(c1, p1, acc1, 0, 0, 0);
        __builtin_amdgcn_sched_barrier(0);
        if (it < 7) { a0 = na0; a1 = na1; c0 = nc0; c1 = nc1; p0 = np0; p1 = np1; q0 = nq0; q1 = nq1; }
      }
    }
    DIAG_STAMP(5);
    RAW_BAR();
    float* red = (float*)cbase;
#pragma unroll
    for (int g = 0; g < 4; ++g) {
      f32x4 v0 = {acc0[4 * g], acc0[4 * g + 1], acc0[4 * g + 2], acc0[4 * g + 3]};
      f32x4 v1 = {acc1[4 * g], acc1[4 * g + 1], acc1[4 * g + 2], acc1[4 * g + 3]};
      *(f32x4*)(red + (w4 * 32 + r) * 36 + 8 * g + 4 * h) = v0;
      *(f32x4*)(red + ((4 + w4) * 32 + r) * 36 + 8 * g + 4 * h) = v1;
    }
    if (tid == 511 && hcnt < 16u) {
      gu32* cnt = (gu32*)(ws + WS_CNT + (mt2 >> 1) * 256);
      for (unsigned spins = 0; spins < (1u << 22); ++spins) {
        if (__hip_atomic_load(cnt, __ATOMIC_RELAXED, __HIP_MEMORY_SCOPE_AGENT) >= 16u) break;
        __builtin_amdgcn_s_sleep(1);
      }
    }
    RAW_BAR();
    DIAG_STAMP2(1);
    u16* at = (u16*)(ws + WS_AT) + (size_t)d * L_;
    {
      const int c = t2;
      const int T = c >> 7, a = (c >> 2) & 31, b0 = (c & 3) * 8;
      f32x4 s0 = {}, s1 = {};
      const float* p = red + (T * 4 * 32 + a) * 36 + b0;
#pragma unroll
      for (int w = 0; w < 4; ++w) { s0 += *(const f32x4*)(p + w * 32 * 36); s1 += *(const f32x4*)(p + w * 32 * 36 + 4); }
      u32x4 o;
#pragma unroll
      for (int j = 0; j < 4; ++j) {
        const f2 y = unpack2((j < 2) ? pack2(s0[2 * j] * kinv, s0[2 * j + 1] * kinv) : pack2(s1[2 * j - 4] * kinv, s1[2 * j - 3] * kinv));
        const f2 us = unpack2(uv0[j]) * dsk;
        const f2 t = unpack2(pack2(us.x, us.y)) + y;
        u32 ov = pack2(t.x, t.y);
        if ((ov & 0xffffu) == 0xffffu) ov &= 0xffffffc0u;
        if ((ov >> 16) == 0xffffu) ov &= 0xffc0ffffu;
        o[j] = ov;
      }
      st16_wt(at + c * 8, o);
    }
  }
  DIAG_STAMP2(2);

  {
    f32x4 acc[4][4] = {};
    bf16x8 hb[4][4];
#pragma unroll
    for (int k = 0; k < 4; ++k)
#pragma unroll
      for (int i = 0; i < 4; ++i) hb[k][i] = G2_LD(hr, i, k, 16);
    const int m = tid >> 3, n0 = (tid & 7) * 8;
    const size_t go = (size_t)(mt2 * 64 + m) * D_ + nt2 * 64 + n0;
    const u32x4 xv = *(const u32x4*)(x + go);
    __builtin_amdgcn_sched_barrier(0);
#pragma unroll
    for (int k = 0; k < 7; ++k) {
#pragma unroll
      for (int j = 0; j < 4; ++j)
#pragma unroll
        for (int i = 0; i < 4; ++i) acc[j][i] = __builtin_amdgcn_mfma_f32_16x16x32_bf16(wd[k][j], hb[k & 3][i], acc[j][i], 0, 0, 0);
      if (k < 3) {
#pragma unroll
        for (int i = 0; i < 4; ++i) hb[k][i] = G2_LD(hr, i, k + 4, 16);
      }
      __builtin_amdgcn_sched_barrier(0);
    }
    DIAG_STAMP2(3);
    u32 a0, a1, a2, a3, a4, a5, a6, a7;
    const u16* at = (const u16*)(ws + WS_AT) + (size_t)(nt2 * 64 + n0) * L_ + mt2 * 64 + m;
#define A_LOADS(tail)                                                                                              \
  asm volatile("global_load_ushort %0, %8, off sc1\n\t"                                                            \
               "global_load_ushort %1, %9, off sc1\n\t"                                                            \
               "global_load_ushort %2, %10, off sc1\n\t"                                                           \
               "global_load_ushort %3, %11, off sc1\n\t"                                                           \
               "global_load_ushort %4, %12, off sc1\n\t"                                                           \
               "global_load_ushort %5, %13, off sc1\n\t"                                                           \
               "global_load_ushort %6, %14, off sc1\n\t"                                                           \
               "global_load_ushort %7, %15, off sc1" tail                                                          \
               : "=&v"(a0), "=&v"(a1), "=&v"(a2), "=&v"(a3), "=&v"(a4), "=&v"(a5), "=&v"(a6), "=&v"(a7)            \
               : "v"(at), "v"(at + L_), "v"(at + 2 * L_), "v"(at + 3 * L_), "v"(at + 4 * L_), "v"(at + 5 * L_),   \
                 "v"(at + 6 * L_), "v"(at + 7 * L_)                                                                \
               : "memory")
    A_LOADS("");
    RAW_BAR();
    float* red = (float*)smem;
    float* rp = red + (wid * 64 + (lane & 15)) * 68 + 4 * (lane >> 4);
#pragma unroll
    for (int j = 0; j < 4; ++j)
#pragma unroll
      for (int i = 0; i < 4; ++i) *(f32x4*)(rp + i * 16 * 68 + j * 16) = acc[j][i];
    asm volatile("s_waitcnt vmcnt(0)" : "+v"(a0), "+v"(a1), "+v"(a2), "+v"(a3), "+v"(a4), "+v"(a5), "+v"(a6), "+v"(a7)::"memory");
    for (unsigned spins = 0; spins < (1u << 20); ++spins) {
      const bool pend = (a0 == 0xffffu) | (a1 == 0xffffu) | (a2 == 0xffffu) | (a3 == 0xffffu) | (a4 == 0xffffu) |
                        (a5 == 0xffffu) | (a6 == 0xffffu) | (a7 == 0xffffu);
      if (!__any(pend)) break;
      __builtin_amdgcn_s_sleep(2);
      A_LOADS("\n\ts_waitcnt vmcnt(0)");
    }
    RAW_BAR();
    DIAG_STAMP2(4);
    f32x4 p0 = {}, p1 = {};
#pragma unroll
    for (int w = 0; w < 8; ++w) {
      p0 += *(const f32x4*)(red + (w * 64 + m) * 68 + n0);
      p1 += *(const f32x4*)(red + (w * 64 + m) * 68 + n0 + 4);
    }
    const u32 av[8] = {a0, a1, a2, a3, a4, a5, a6, a7};
    u32x4 o;
#pragma unroll
    for (int j = 0; j < 4; ++j) {
      const f2 xx = unpack2(xv[j]);
      const f2 aa = f2{__uint_as_float(av[2 * j] << 16), __uint_as_float(av[2 * j + 1] << 16)};
      const f2 r1 = xx + aa;
      const f2 bb = unpack2((j < 2) ? pack2(p0[2 * j], p0[2 * j + 1]) : pack2(p1[2 * j - 4], p1[2 * j - 3]));
      const f2 r2 = unpack2(pack2(r1.x, r1.y)) + bb;
      o[j] = pack2(r2.x, r2.y);
    }
    *(u32x4*)(out + go) = o;
  }
}

extern "C" void kernel_launch(void* const* d_in, const int* in_sizes, int n_in, void* d_out, int out_size,
                              void* d_ws, size_t ws_size, hipStream_t stream) {
  const u16* x = (const u16*)d_in[0];
  const u16* logA = (const u16*)d_in[1];
  const u16* Aim = (const u16*)d_in[2];
  const u16* Cre = (const u16*)d_in[3];
  const u16* Cim = (const u16*)d_in[4];
  const u16* logdt = (const u16*)d_in[5];
  const u16* dskip = (const u16*)d_in[6];
  const u16* wg = (const u16*)d_in[8];
  const u16* wu = (const u16*)d_in[9];
  const u16* wd = (const u16*)d_in[10];
  char* ws = (char*)d_ws;
  (void)hipFuncSetAttribute((const void*)k2_kernel, hipFuncAttributeMaxDynamicSharedMemorySize, K2_LDS);
  k1_kernel<<<K1_LN_BLOCKS + K1_WT_BLOCKS + K1_KG_BLOCKS, 256, 0, stream>>>(x, logA, Aim, Cre, Cim, logdt, wg, wu, wd, ws);
  k2_kernel<<<256, 512, K2_LDS, stream>>>(x, dskip, ws, (u16*)d_out);
}
```

```cpp
#include <hip/hip_runtime.h>
#include <hip/hip_bf16.h>
#define WT_STORES
#define CV_PRIO 1
#define G2_PRIO 3

typedef unsigned short u16;
typedef unsigned int u32;
using bf16x8 = __attribute__((ext_vector_type(8))) short;
using f16x8 = __attribute__((ext_vector_type(8))) _Float16;
using f16x4 = __attribute__((ext_vector_type(4))) _Float16;
using f32x4 = __attribute__((ext_vector_type(4))) float;
using f32x16 = __attribute__((ext_vector_type(16))) float;
using u32x4 = __attribute__((ext_vector_type(4))) unsigned int;
using u32x2 = __attribute__((ext_vector_type(2))) unsigned int;
typedef float f2 __attribute__((ext_vector_type(2)));
typedef __attribute__((address_space(1))) unsigned gu32;

constexpr int L_ = 2048, D_ = 512, N_ = 128, F_ = 1792;
constexpr int KC_STRIDE = 648;

constexpr size_t WS_CNT = 0;
constexpr size_t WS_XNT = 4096;
constexpr size_t WS_XNTR = WS_XNT + (size_t)L_ * D_ * 2;
constexpr size_t WS_KC = WS_XNTR + (size_t)L_ * D_ * 2;
constexpr size_t WS_H = WS_KC + (size_t)D_ * KC_STRIDE * 4;
constexpr size_t WS_WG = WS_H + (size_t)L_ * F_ * 2;
constexpr size_t WS_WU = WS_WG + (size_t)D_ * F_ * 2;
constexpr size_t WS_WD = WS_WU + (size_t)D_ * F_ * 2;
constexpr size_t WS_AT = WS_WD + (size_t)D_ * F_ * 2;

#define DIAG_START(k)
#define DIAG_STAMP(i)
#define DIAG_END(wsp)
#define DIAG_STAMP2(i)

__device__ __forceinline__ float bf2f(u16 v) { return __uint_as_float(((u32)v) << 16); }
__device__ __forceinline__ u16 f2bf(float f) {
  __bf16 b = (__bf16)f;
  return __builtin_bit_cast(u16, b);
}
__device__ __forceinline__ float rbf(float f) { return bf2f(f2bf(f)); }
__device__ __forceinline__ u32 pack2(float lo, float hi) {
  typedef __bf16 b2 __attribute__((ext_vector_type(2)));
  b2 r = __builtin_convertvector(f2{lo, hi}, b2);
  return __builtin_bit_cast(u32, r);
}
__device__ __forceinline__ f2 unpack2(u32 w) { return f2{__uint_as_float(w << 16), __uint_as_float(w & 0xffff0000u)}; }

__device__ __forceinline__ void st16_wt(void* p, u32x4 v) { asm volatile("global_store_dwordx4 %0, %1, off sc1\n\ts_nop 1" ::"v"(p), "v"(v) : "memory"); }
__device__ __forceinline__ void st16(void* p, u32x4 v) { st16_wt(p, v); }
__device__ __forceinline__ void st8(void* p, u32x2 v) { asm volatile("global_store_dwordx2 %0, %1, off sc1\n\ts_nop 1" ::"v"(p), "v"(v) : "memory"); }

#define DPP_F(v, ctrl) __builtin_bit_cast(float, __builtin_amdgcn_update_dpp(0, __builtin_bit_cast(int, v), ctrl, 0xf, 0xf, true))
__device__ __forceinline__ float wave_sum(float v) {
  v += DPP_F(v, 0xB1);
  v += DPP_F(v, 0x4E);
  v += DPP_F(v, 0x141);
  v += DPP_F(v, 0x140);
  const int iv = __builtin_bit_cast(int, v);
  return (__builtin_bit_cast(float, __builtin_amdgcn_readlane(iv, 0)) + __builtin_bit_cast(float, __builtin_amdgcn_readlane(iv, 16))) +
         (__builtin_bit_cast(float, __builtin_amdgcn_readlane(iv, 32)) + __builtin_bit_cast(float, __builtin_amdgcn_readlane(iv, 48)));
}
__device__ __forceinline__ float wave_max(float v) {
  v = fmaxf(v, DPP_F(v, 0xB1));
  v = fmaxf(v, DPP_F(v, 0x4E));
  v = fmaxf(v, DPP_F(v, 0x141));
  v = fmaxf(v, DPP_F(v, 0x140));
  const int iv = __builtin_bit_cast(int, v);
  return fmaxf(fmaxf(__builtin_bit_cast(float, __builtin_amdgcn_readlane(iv, 0)), __builtin_bit_cast(float, __builtin_amdgcn_readlane(iv, 16))),
               fmaxf(__builtin_bit_cast(float, __builtin_amdgcn_readlane(iv, 32)), __builtin_bit_cast(float, __builtin_amdgcn_readlane(iv, 48))));
}

constexpr int K1_LN_BLOCKS = 128;
constexpr int K1_WT_BLOCKS = 336;
constexpr int K1_KG_BLOCKS = 512;

__device__ __forceinline__ void cmul6(f2& r0, f2& r1, f2& r2, f2& r3, f2& r4, f2& r5, f2 a0, f2 b0, f2 a1, f2 b1,
                                      f2 a2, f2 b2, f2 a3, f2 b3, f2 a4, f2 b4, f2 a5, f2 b5) {
  asm("v_pk_mul_f32 %0, %6, %7 op_sel:[1,1] op_sel_hi:[1,0] neg_lo:[0,1]\n\t"
      "v_pk_mul_f32 %1, %8, %9 op_sel:[1,1] op_sel_hi:[1,0] neg_lo:[0,1]\n\t"
      "v_pk_mul_f32 %2, %10, %11 op_sel:[1,1] op_sel_hi:[1,0] neg_lo:[0,1]\n\t"
      "v_pk_mul_f32 %3, %12, %13 op_sel:[1,1] op_sel_hi:[1,0] neg_lo:[0,1]\n\t"
      "v_pk_mul_f32 %4, %14, %15 op_sel:[1,1] op_sel_hi:[1,0] neg_lo:[0,1]\n\t"
      "v_pk_mul_f32 %5, %16, %17 op_sel:[1,1] op_sel_hi:[1,0] neg_lo:[0,1]\n\t"
      "v_pk_fma_f32 %0, %6, %7, %0 op_sel:[0,0,0] op_sel_hi:[0,1,1]\n\t"
      "v_pk_fma_f32 %1, %8, %9, %1 op_sel:[0,0,0] op_sel_hi:[0,1,1]\n\t"
      "v_pk_fma_f32 %2, %10, %11, %2 op_sel:[0,0,0] op_sel_hi:[0,1,1]\n\t"
      "v_pk_fma_f32 %3, %12, %13, %3 op_sel:[0,0,0] op_sel_hi:[0,1,1]\n\t"
      "v_pk_fma_f32 %4, %14, %15, %4 op_sel:[0,0,0] op_sel_hi:[0,1,1]\n\t"
      "v_pk_fma_f32 %5, %16, %17, %5 op_sel:[0,0,0] op_sel_hi:[0,1,1]\n\t"
      "s_nop 1"
      : "=&v"(r0), "=&v"(r1), "=&v"(r2), "=&v"(r3), "=&v"(r4), "=&v"(r5)
      : "v"(a0), "v"(b0), "v"(a1), "v"(b1), "v"(a2), "v"(b2), "v"(a3), "v"(b3), "v"(a4), "v"(b4), "v"(a5), "v"(b5));
}
__device__ __forceinline__ void cmul2(f2& r0, f2& r1, f2 a0, f2 b0, f2 a1, f2 b1) {
  asm("v_pk_mul_f32 %0, %2, %3 op_sel:[1,1] op_sel_hi:[1,0] neg_lo:[0,1]\n\t"
      "v_pk_mul_f32 %1, %4, %5 op_sel:[1,1] op_sel_hi:[1,0] neg_lo:[0,1]\n\t"
      "s_nop 0\n\t"
      "v_pk_fma_f32 %0, %2, %3, %0 op_sel:[0,0,0] op_sel_hi:[0,1,1]\n\t"
      "v_pk_fma_f32 %1, %4, %5, %1 op_sel:[0,0,0] op_sel_hi:[0,1,1]\n\t"
      "s_nop 1"
      : "=&v"(r0), "=&v"(r1)
      : "v"(a0), "v"(b0), "v"(a1), "v"(b1));
}
__device__ __forceinline__ u32x2 split_h(f2 v) {
  typedef _Float16 h2 __attribute__((ext_vector_type(2)));
  const h2 h = __builtin_bit_cast(h2, __builtin_amdgcn_cvt_pkrtz(v.x, v.y));
  const f2 r = v - f2{(float)h.x, (float)h.y};
  return u32x2{__builtin_bit_cast(u32, h), __builtin_bit_cast(u32, __builtin_amdgcn_cvt_pkrtz(r.x, r.y))};
}
__device__ __forceinline__ f2 cexp_(float zr, float zi, float t) {
  const float m = __builtin_amdgcn_exp2f((zr * t) * 1.44269504088896341f);
  float r = (zi * t) * 0.15915494309189535f;
  r -= rintf(r);
  return f2{m * __builtin_amdgcn_cosf(r), m * __builtin_amdgcn_sinf(r)};
}

__global__ __launch_bounds__(256) void k1_kernel(const u16* __restrict__ x, const u16* __restrict__ logA,
                                                 const u16* __restrict__ Aim, const u16* __restrict__ Cre,
                                                 const u16* __restrict__ Cim, const u16* __restrict__ logdt,
                                                 const u16* __restrict__ wg, const u16* __restrict__ wu,
                                                 const u16* __restrict__ wd, char* __restrict__ ws) {
  __shared__ __attribute__((aligned(16))) char smem[18448];
  const int tid = threadIdx.x, lane = tid & 63, wid = tid >> 6;
  DIAG_START(0)
  int bid = blockIdx.x - K1_KG_BLOCKS;
  if (bid >= 0 && bid < K1_LN_BLOCKS) {
    __builtin_amdgcn_s_setprio(3);
    if (bid == 0 && tid < 16) *(u32*)(ws + WS_CNT + tid * 256) = 0u;
#pragma unroll
    for (int it = 0; it < 4; ++it) st16(ws + WS_AT + ((size_t)bid * 1024 + it * 256 + tid) * 16, u32x4{0xffffffffu, 0xffffffffu, 0xffffffffu, 0xffffffffu});
    u16* tile = (u16*)smem;
    u16* xnt = (u16*)(ws + WS_XNT);
    const int r0 = bid * 16;
    u32x4 v[4];
#pragma unroll
    for (int i = 0; i < 4; ++i) v[i] = *(const u32x4*)(x + (size_t)(r0 + wid * 4 + i) * D_ + lane * 8);
#pragma unroll
    for (int i = 0; i < 4; ++i) {
      const int rl = wid * 4 + i;
      f2 f[4];
#pragma unroll
      for (int j = 0; j < 4; ++j) f[j] = unpack2(v[i][j]);
      const f2 s2 = (f[0] + f[1]) + (f[2] + f[3]);
      const float mean = wave_sum(s2.x + s2.y) * (1.f / D_);
      f2 q2 = {0.f, 0.f};
#pragma unroll
      for (int j = 0; j < 4; ++j) { const f2 t = f[j] - mean; q2 = __builtin_elementwise_fma(t, t, q2); }
      const float q = wave_sum(q2.x + q2.y);
      const float mb = rbf(mean);
      const float vb = rbf(q * (1.f / D_));
      const float sd = rbf(sqrtf(rbf(vb + 1e-5f)));
      const float rs = __builtin_amdgcn_rcpf(sd);
      u32x4 o;
#pragma unroll
      for (int j = 0; j < 4; ++j) {
        const f2 t = f[j] - mb;
        const f2 a = unpack2(pack2(t.x, t.y));
        f2 qq = a * rs;
        const f2 rem = __builtin_elementwise_fma(-qq, f2{sd, sd}, a);
        qq = __builtin_elementwise_fma(rem, f2{rs, rs}, qq);
        o[j] = pack2(qq.x, qq.y);
      }
      *(u32x4*)(tile + rl * 520 + lane * 8) = o;
    }
    DIAG_STAMP(1);
    __syncthreads();
#pragma unroll
    for (int it = 0; it < 4; ++it) {
      const int c = tid + it * 256;
      const u32x4 o = *(const u32x4*)(tile + (c & 15) * 520 + (c >> 4) * 8);
      st16(xnt + ((size_t)bid * 1024 + c) * 8, o);
    }
    u16* xtr = (u16*)(ws + WS_XNTR);
#pragma unroll
    for (int it = 0; it < 4; ++it) {
      const int c = tid + it * 256;
      const int d = c >> 1, g = c & 1;
      u32x4 o;
#pragma unroll
      for (int j = 0; j < 4; ++j)
        o[j] = (u32)tile[(g * 8 + 2 * j) * 520 + d] | ((u32)tile[(g * 8 + 2 * j + 1) * 520 + d] << 16);
      st16(xtr + ((size_t)bid * 1024 + c) * 8, o);
    }
    DIAG_END(ws);
    return;
  }
  bid -= K1_LN_BLOCKS;
  if (bid >= 0) {
    __builtin_amdgcn_s_setprio(2);
    int task = bid * 4 + wid;
    const u16* W; u16* Wt; int Kd, Nd;
    if (task < 448) { W = wg; Wt = (u16*)(ws + WS_WG); Kd = D_; Nd = F_; }
    else if (task < 896) { task -= 448; W = wu; Wt = (u16*)(ws + WS_WU); Kd = D_; Nd = F_; }
    else { task -= 896; W = wd; Wt = (u16*)(ws + WS_WD); Kd = F_; Nd = D_; }
    const int nks = Kd / 32;
    const int ks = task % nks, nb = task / nks;
    const int col = nb * 64 + lane;
    u16 v[32];
#pragma unroll
    for (int k = 0; k < 32; ++k) v[k] = W[(size_t)(ks * 32 + k) * Nd + col];
#pragma unroll
    for (int q = 0; q < 4; ++q) {
      u32x4 o;
#pragma unroll
      for (int j = 0; j < 4; ++j) o[j] = (u32)v[q * 8 + 2 * j] | ((u32)v[q * 8 + 2 * j + 1] << 16);
      st16(Wt + ((size_t)((col >> 4) * nks + ks) * 64 + q * 16 + (col & 15)) * 8, o);
    }
    DIAG_END(ws);
    return;
  }
  bid = blockIdx.x;
  {
    const int d = bid;
    float4* prm = (float4*)(smem + 16384);
    float* cmx = (float*)(smem + 16384 + 2048);
    if (tid < 128) {
      const u16 la = logA[d * N_ + tid], ai = Aim[d * N_ + tid], cr = Cre[d * N_ + tid], ci = Cim[d * N_ + tid];
      const float dt = rbf(expf(bf2f(logdt[d])));
      const float ar = -rbf(expf(bf2f(la)));
      const float c0 = bf2f(cr) * dt, c1 = bf2f(ci) * dt;
      prm[tid] = make_float4(ar * dt, bf2f(ai) * dt, c0, c1);
      const float mx = wave_max(fmaxf(fabsf(c0), fabsf(c1)));
      if (lane == 0) cmx[wid] = mx;
    }
    __syncthreads();
    DIAG_STAMP(1);
    float cs, cinv;
    {
      int e = (int)((__float_as_uint(fmaxf(cmx[0], cmx[1])) >> 23) & 0xff);
      e = min(max(e, 2), 252);
      cs = __uint_as_float((u32)(254 - e) << 23);
      cinv = __uint_as_float((u32)e << 23);
    }
    const int kq = lane >> 4, idx = lane & 15;
    f32x4 acc[4] = {};
    const float ty = (float)idx, te = (float)(idx == 0 ? 256 : 16 * idx);
    float4 pr[8];
#pragma unroll
    for (int s = 0; s < 8; ++s) pr[s] = prm[wid * 32 + (s >> 2) * 16 + kq * 4 + (s & 3)];
#pragma unroll
    for (int t = 0; t < 2; ++t) {
      u32x4 Xh[4], Xl[4], Yh[4], Yl[4];
#pragma unroll
      for (int s = 0; s < 4; s += 2) {
        const float4 pa = pr[t * 4 + s], pb = pr[t * 4 + s + 1];
        const f2 Ca = f2{pa.z, -pa.w} * cs, Cb = f2{pb.z, -pb.w} * cs;
        f2 Ya[4], Yb[4], Xa[4], Xb[4];
        Ya[0] = cexp_(pa.x, pa.y, ty); Yb[0] = cexp_(pb.x, pb.y, ty);
        f2 Ea = cexp_(pa.x, -pa.y, te), Eb = cexp_(pb.x, -pb.y, te);
        f2 Fa = (idx == 0) ? f2{1.f, 0.f} : Ea, Fb = (idx == 0) ? f2{1.f, 0.f} : Eb;
#pragma unroll
        for (int e = 0; e < 3; ++e) {
          f2 nEa, nEb;
          cmul6(Xa[e], Xb[e], Ya[e + 1], Yb[e + 1], nEa, nEb, Ca, Fa, Cb, Fb, Ya[e], Ya[e], Yb[e], Yb[e], Ea, Ea, Eb, Eb);
          Ea = nEa; Eb = nEb; Fa = nEa; Fb = nEb;
        }
        cmul2(Xa[3], Xb[3], Ca, Ea, Cb, Eb);
#pragma unroll
        for (int e = 0; e < 4; ++e) {
          const u32x2 xa2 = split_h(Xa[e]), xb2 = split_h(Xb[e]), ya2 = split_h(Ya[e]), yb2 = split_h(Yb[e]);
          Xh[e][s] = xa2[0]; Xl[e][s] = xa2[1]; Xh[e][s + 1] = xb2[0]; Xl[e][s + 1] = xb2[1];
          Yh[e][s] = ya2[0]; Yl[e][s] = ya2[1]; Yh[e][s + 1] = yb2[0]; Yl[e][s + 1] = yb2[1];
        }
      }
#pragma unroll
      for (int e = 0; e < 4; ++e) acc[e] = __builtin_amdgcn_mfma_f32_16x16x32_f16(__builtin_bit_cast(f16x8, Xl[e]), __builtin_bit_cast(f16x8, Yh[e]), acc[e], 0, 0, 0);
#pragma unroll
      for (int e = 0; e < 4; ++e) acc[e] = __builtin_amdgcn_mfma_f32_16x16x32_f16(__builtin_bit_cast(f16x8, Xh[e]), __builtin_bit_cast(f16x8, Yl[e]), acc[e], 0, 0, 0);
#pragma unroll
      for (int e = 0; e < 4; ++e) acc[e] = __builtin_amdgcn_mfma_f32_16x16x32_f16(__builtin_bit_cast(f16x8, Xh[e]), __builtin_bit_cast(f16x8, Yh[e]), acc[e], 0, 0, 0);
    }
    DIAG_STAMP(2);
    float* red = (float*)smem;
#pragma unroll
    for (int e = 0; e < 4; ++e)
#pragma unroll
      for (int r = 0; r < 4; ++r) red[(wid * 4 + e) * 256 + ((lane >> 4) * 4 + r) * 16 + (lane & 15)] = acc[e][r];
    __syncthreads();
    float* kc = (float*)(ws + WS_KC) + (size_t)d * KC_STRIDE;
#pragma unroll
    for (int e = 0; e < 4; ++e) {
      const float v = (2.f * cinv) * ((red[(0 * 4 + e) * 256 + tid] + red[(1 * 4 + e) * 256 + tid]) +
                                      (red[(2 * 4 + e) * 256 + tid] + red[(3 * 4 + e) * 256 + tid]));
      if (e == 0) kc[tid] = v;
      else if (tid >= 128) kc[128 * e + tid] = v;
      else if (e == 3 && tid == 0) kc[640] = v;
    }
    DIAG_END(ws);
  }
}

constexpr int G1_STAGE = 22 * 1024;
constexpr int G1_NBUF = 7;
constexpr int K2_LDS = G1_STAGE * G1_NBUF;

constexpr int UC_LEN = 2080;
constexpr int KR_LEN = 3840;
constexpr int CV_UCP = 0;
constexpr int CV_KR = CV_UCP + 8 * UC_LEN * 2;
constexpr int CV_URAW = CV_KR + KR_LEN * 2;
constexpr int CV_KRAW = CV_URAW + 4224;
constexpr int CV_MAX = CV_KRAW + 2592;
constexpr int CV_CH_STRIDE = 48128;
constexpr int H_STAGE = 2 * CV_CH_STRIDE;

__device__ __forceinline__ f2 gelu2(f2 g) {
  const float c0 = -2.f * 0.7978845608028654f * 1.44269504088896341f;
  const float c1 = c0 * 0.044715f;
  const f2 t = g * __builtin_elementwise_fma(g * g, f2{c1, c1}, f2{c0, c0});
  const f2 dn = f2{__builtin_amdgcn_exp2f(t.x), __builtin_amdgcn_exp2f(t.y)} + 1.f;
  return g * f2{__builtin_amdgcn_rcpf(dn.x), __builtin_amdgcn_rcpf(dn.y)};
}
__device__ __forceinline__ void glds16(const void* g, void* l) {
  __builtin_amdgcn_global_load_lds((const __attribute__((address_space(1))) u32*)g,
                                   (__attribute__((address_space(3))) u32*)l, 16, 0, 0);
}
__device__ __forceinline__ u32 bf2_to_h2(u32 w) {
  auto r = __builtin_amdgcn_cvt_pkrtz(__uint_as_float(w << 16), __uint_as_float(w & 0xffff0000u));
  return __builtin_bit_cast(u32, r);
}
#define RAW_BAR() do { asm volatile("s_waitcnt lgkmcnt(0)" ::: "memory"); __builtin_amdgcn_s_barrier(); } while (0)

__global__ __launch_bounds__(512, 2) void k2_kernel(const u16* __restrict__ x, const u16* __restrict__ dskip,
                                                    char* __restrict__ ws, u16* __restrict__ out) {
  extern __shared__ __attribute__((aligned(16))) char smem[];
  const int tid = threadIdx.x, lane = tid & 63;
  const int wid = __builtin_amdgcn_readfirstlane(tid >> 6);
  DIAG_START(1)
  const int bid = blockIdx.x;
  const int xcd = bid & 7, loc = bid >> 3;
  const int ch = wid >> 2, w4 = wid & 3;
  const int d = bid * 2 + ch;
  const int t2 = tid & 255;
  const int mt = (xcd >> 1) * 4 + (loc & 3);
  const int nt = (xcd & 1) * 8 + (loc >> 2);

  u32x4 uv0; float kv0, kv1, kv2; u32 pds;
  {
    const u16* xnt = (const u16*)(ws + WS_XNT);
    const u16* wgt = (const u16*)(ws + WS_WG);
    const u16* wut = (const u16*)(ws + WS_WU);
    auto src = [&](int f) -> const u16* {
      if (f < 8) return xnt + (size_t)(mt * 8 + f) * 16 * 512;
      const int s = f - 8;
      return ((s & 1) ? wut : wgt) + (size_t)(nt * 7 + (s >> 1)) * 16 * 512;
    };
    const u16* s0 = src(wid) + lane * 8;
    const u16* s1 = src(wid + 8) + lane * 8;
    const u16* s2 = src(wid < 6 ? wid + 16 : 21) + lane * 8;
    auto stage = [&](int ks) {
      char* base = smem + (ks % G1_NBUF) * G1_STAGE + wid * 1024 + lane * 16;
      glds16(s0 + ks * 512, base);
      glds16(s1 + ks * 512, base + 8192);
      if (wid < 6) glds16(s2 + ks * 512, base + 16384);
    };
#define G1_WAIT(n) do { if (wid < 6) asm volatile("s_waitcnt vmcnt(%0)" ::"n"(3 * (n)) : "memory"); \
                        else asm volatile("s_waitcnt vmcnt(%0)" ::"n"(2 * (n)) : "memory"); } while (0)
#pragma unroll
    for (int k = 0; k < G1_NBUF; ++k) stage(k);
    const int wm = wid >> 1, wn = wid & 1;
    f32x4 acc[7][2] = {};
    bf16x8 xa[2], wa[7], xb[2], wb[7];
    G1_WAIT(G1_NBUF - 1);
    RAW_BAR();
    DIAG_STAMP(1);
    {
      const char* base = smem + lane * 16;
#pragma unroll
      for (int i = 0; i < 2; ++i) xa[i] = *(const bf16x8*)(base + (wm * 2 + i) * 1024);
#pragma unroll
      for (int j = 0; j < 7; ++j) wa[j] = *(const bf16x8*)(base + (8 + wn * 7 + j) * 1024);
    }
#define G1_ITER(k, XC, WC, XN, WN, NLEFT)                                                          \
  do {                                                                                             \
    G1_WAIT(NLEFT);                                                                                \
    RAW_BAR();                                                                                     \
    const char* rb_ = smem + (((k) + 1) % G1_NBUF) * G1_STAGE + lane * 16;                         \
    _Pragma("unroll") for (int j = 0; j < 7; ++j) {                                                \
      acc[j][0] = __builtin_amdgcn_mfma_f32_16x16x32_bf16(WC[j], XC[0], acc[j][0], 0, 0, 0);       \
      if (j < 2) XN[j] = *(const bf16x8*)(rb_ + (wm * 2 + j) * 1024);                              \
      acc[j][1] = __builtin_amdgcn_mfma_f32_16x16x32_bf16(WC[j], XC[1], acc[j][1], 0, 0, 0);       \
      WN[j] = *(const bf16x8*)(rb_ + (8 + wn * 7 + j) * 1024);                                     \
      __builtin_amdgcn_sched_barrier(0);                                                           \
      if (j == 3 && (k) + G1_NBUF < 16) { stage((k) + G1_NBUF); __builtin_amdgcn_sched_barrier(0); } \
    }                                                                                              \
  } while (0)
    G1_ITER(0, xa, wa, xb, wb, 5);
    G1_ITER(1, xb, wb, xa, wa, 5);
    G1_ITER(2, xa, wa, xb, wb, 5);
    G1_ITER(3, xb, wb, xa, wa, 5);
    G1_ITER(4, xa, wa, xb, wb, 5);
    G1_ITER(5, xb, wb, xa, wa, 5);
    G1_ITER(6, xa, wa, xb, wb, 5);
    G1_ITER(7, xb, wb, xa, wa, 5);
    G1_ITER(8, xa, wa, xb, wb, 5);
    G1_ITER(9, xb, wb, xa, wa, 5);
    G1_ITER(10, xa, wa, xb, wb, 4);
    G1_ITER(11, xb, wb, xa, wa, 3);
    G1_ITER(12, xa, wa, xb, wb, 2);
    G1_ITER(13, xb, wb, xa, wa, 1);
    G1_ITER(14, xa, wa, xb, wb, 0);
    {
      const u16* ug = (const u16*)(ws + WS_XNTR) + (size_t)(t2 >> 1) * 8192 + d * 16 + (t2 & 1) * 8;
      const float* kc = (const float*)(ws + WS_KC) + (size_t)d * KC_STRIDE;
      uv0 = *(const u32x4*)ug;
      kv0 = kc[t2]; kv1 = kc[t2 + 256]; kv2 = (t2 < 129) ? kc[t2 + 512] : 0.f;
      pds = dskip[d];
    }
    __builtin_amdgcn_sched_barrier(0);
#pragma unroll
    for (int j = 0; j < 7; ++j)
#pragma unroll
      for (int i = 0; i < 2; ++i) acc[j][i] = __builtin_amdgcn_mfma_f32_16x16x32_bf16(wb[j], xb[i], acc[j][i], 0, 0, 0);
    DIAG_STAMP(2);
    RAW_BAR();
    f32x4* ex = (f32x4*)(smem + H_STAGE + 28672);
    if (wn == 0) {
#pragma unroll
      for (int i = 0; i < 2; ++i) ex[(wm * 2 + i) * 64 + lane] = acc[6][i];
    }
    char* hs = smem + H_STAGE;
    auto emit = [&](int t, const f32x4& g, const f32x4& u, int i) {
      u32x2 o;
#pragma unroll
      for (int p = 0; p < 2; ++p) {
        const f2 gb = unpack2(pack2(g[2 * p], g[2 * p + 1]));
        const f2 ub = unpack2(pack2(u[2 * p], u[2 * p + 1]));
        const f2 ge = gelu2(gb);
        const f2 hv = unpack2(pack2(ge.x, ge.y)) * ub;
        o[p] = pack2(hv.x, hv.y);
      }
      *(u32x2*)(hs + (((wm * 2 + i) * 7 + t) * 32 + (lane >> 5) * 16 + (lane & 15)) * 16 + ((lane >> 4) & 1) * 8) = o;
    };
    if (wn == 0) {
#pragma unroll
      for (int t = 0; t < 3; ++t)
#pragma unroll
        for (int i = 0; i < 2; ++i) emit(t, acc[2 * t][i], acc[2 * t + 1][i], i);
      RAW_BAR();
    } else {
#pragma unroll
      for (int t = 4; t < 7; ++t)
#pragma unroll
        for (int i = 0; i < 2; ++i) emit(t, acc[2 * t - 7][i], acc[2 * t - 6][i], i);
      RAW_BAR();
#pragma unroll
      for (int i = 0; i < 2; ++i) emit(3, ex[(wm * 2 + i) * 64 + lane], acc[0][i], i);
    }
    {
      char* cbase = smem + ch * CV_CH_STRIDE;
      u32x4* uraw = (u32x4*)(cbase + CV_URAW);
      float* kraw = (float*)(cbase + CV_KRAW);
      float* kmax = (float*)(cbase + CV_MAX);
      if (t2 < 4) { uraw[t2] = u32x4{0, 0, 0, 0}; uraw[260 + t2] = u32x4{0, 0, 0, 0}; }
      u32x4 h;
#pragma unroll
      for (int j = 0; j < 4; ++j) h[j] = bf2_to_h2(uv0[j]);
      uraw[4 + t2] = h;
      kraw[t2] = kv0;
      kraw[t2 + 256] = kv1;
      if (t2 < 136) kraw[t2 + 512] = kv2;
      const float mx = wave_max(fmaxf(fmaxf(fabsf(kv0), fabsf(kv1)), fabsf(kv2)));
      if (lane == 0) kmax[w4] = mx;
    }
    RAW_BAR();
    u16* hT = (u16*)(ws + WS_H);
#pragma unroll
    for (int it = 0; it < 4; ++it) {
      const int c = tid + it * 512;
      if (c < 1792) {
        const int ml = c / 224, rem = c - ml * 224;
        st16_wt(hT + ((size_t)(mt * 8 + ml) * 56 * 64 + (size_t)nt * 7 * 32 + rem) * 8, *(const u32x4*)(hs + c * 16));
      }
    }
  }
  DIAG_STAMP(3);

  const int mt2 = xcd * 8 + (loc & 7);
  const int nt2 = loc >> 3;
  const int vo = lane * 16;
  const __amdgpu_buffer_rsrc_t hr = __builtin_amdgcn_make_buffer_rsrc((void*)((const u16*)(ws + WS_H) + ((size_t)(mt2 * 2) * 56 + wid * 7) * 512), 0, 2 * 56 * 1024, 0x00020000);
  const __amdgpu_buffer_rsrc_t wr = __builtin_amdgcn_make_buffer_rsrc((void*)((const u16*)(ws + WS_WD) + ((size_t)(nt2 * 8) * 56 + wid * 7) * 512), 0, 8 * 56 * 1024, 0x00020000);
#define G2_LD(rs, i, k, aux) __builtin_bit_cast(bf16x8, __builtin_amdgcn_raw_buffer_load_b128(rs, vo, ((i) * 56 + (k)) * 1024, aux))
  bf16x8 wd[3][8];

  {
    char* cbase = smem + ch * CV_CH_STRIDE;
    u16* ucp = (u16*)(cbase + CV_UCP);
    u16* kr = (u16*)(cbase + CV_KR);
    const u32x4* uraw = (const u32x4*)(cbase + CV_URAW);
    const float* kraw = (const float*)(cbase + CV_KRAW);
    const float* kmax = (const float*)(cbase + CV_MAX);
    const float dsk = bf2f((u16)pds);
    float kscale, kinv;
    {
      const float mx = fmaxf(fmaxf(kmax[0], kmax[1]), fmaxf(kmax[2], kmax[3]));
      int e = (int)((__float_as_uint(mx) >> 23) & 0xff);
      e = min(max(e, 20), 240);
      kscale = __uint_as_float((u32)(127 + 13 + 127 - e) << 23);
      kinv = __uint_as_float((u32)(e - 13) << 23);
    }
#pragma unroll
    for (int it = 0; it < 2; ++it) {
      const int tb = it ? 256 + (t2 & 3) : t2;
      u32 w[8];
      u32x4 a = uraw[tb], b = uraw[tb + 1];
#pragma unroll
      for (int j = 0; j < 4; ++j) { w[j] = a[j]; w[4 + j] = b[j]; }
      if (it == 0 || t2 < 4) {
#pragma unroll
        for (int c = 0; c < 8; ++c) {
          u32x4 o;
#pragma unroll
          for (int j = 0; j < 4; ++j)
            o[j] = (c & 1) ? w[j + (c + 1) / 2] : __builtin_amdgcn_alignbit(w[j + c / 2 + 1], w[j + c / 2], 16);
          *(u32x4*)(ucp + c * UC_LEN + tb * 8) = o;
        }
      }
    }
    {
      float kk[8];
#pragma unroll
      for (int j = 0; j < 8; ++j) {
        const int jj = 2047 - (t2 * 8 + j);
        const int cbi = (int)f2bf((float)jj) - 17024;
        kk[j] = kraw[jj < 128 ? jj : cbi] * kscale;
      }
      f16x8 o;
#pragma unroll
      for (int j = 0; j < 8; ++j) o[j] = (_Float16)kk[j];
      *(f16x8*)(kr + t2 * 8 + 8 * (t2 >> 2)) = o;
      if (t2 < 128) { const int tb = t2 + 256; *(u32x4*)(kr + tb * 8 + 8 * (tb >> 2)) = u32x4{0, 0, 0, 0}; }
    }
    asm volatile("s_waitcnt vmcnt(0)" ::: "memory");
    RAW_BAR();
    if (tid == 0) __hip_atomic_fetch_add((gu32*)(ws + WS_CNT + mt * 256), 1u, __ATOMIC_RELAXED, __HIP_MEMORY_SCOPE_AGENT);
    DIAG_STAMP(4);
    const int r = lane & 31, h = lane >> 5;
    const u16* ap = ucp + (r & 7) * UC_LEN + 8 * h + 8 * (r >> 3);
    const int bofs0 = 2048 - 32 * (r + 1) + 8 * h + 8 * (63 - r);
    const int bofs1 = bofs0 - 1024 - 256;
    f32x16 acc0 = {}, acc1 = {};
    unsigned hcnt = 0;
    {
      const u16* apw = ap + 16 * (w4 * 16);
      const u16* bp0 = kr + bofs0 + 16 * (w4 * 16) + 8 * (w4 * 8);
      const u16* bp1 = kr + bofs1 + 16 * (w4 * 16) + 8 * (w4 * 8);
      auto lda = [&](int s) -> f16x8 { return *(const f16x8*)(apw + 16 * s); };
      auto ldb = [&](const u16* b, int s) -> f16x8 { return *(const f16x8*)(b + 16 * s + 8 * (s >> 1)); };
      f16x8 a0 = lda(0), p0 = ldb(bp0, 0), q0 = ldb(bp1, 0), c0 = lda(64);
      f16x8 a1 = lda(1), p1 = ldb(bp0, 1), q1 = ldb(bp1, 1), c1 = lda(65);
#pragma unroll
      for (int it = 0; it < 8; ++it) {
        f16x8 na0, na1, nc0, nc1, np0, np1, nq0, nq1;
        if (it < 7) {
          const int sn = 2 * it + 2;
          na0 = lda(sn); np0 = ldb(bp0, sn); nq0 = ldb(bp1, sn); nc0 = lda(sn + 64);
          na1 = lda(sn + 1); np1 = ldb(bp0, sn + 1); nq1 = ldb(bp1, sn + 1); nc1 = lda(sn + 65);
          if (it < 6) {
#pragma unroll
            for (int i = 0; i < 4; ++i) wd[it >> 1][(it & 1) * 4 + i] = G2_LD(wr, (it & 1) * 4 + i, it >> 1, 0);
          }
        }
        if (it == 7) hcnt = __hip_atomic_load((gu32*)(ws + WS_CNT + (mt2 >> 2) * 256), __ATOMIC_RELAXED, __HIP_MEMORY_SCOPE_AGENT);
        __builtin_amdgcn_sched_barrier(0);
        acc0 = __builtin_amdgcn_mfma_f32_32x32x16_f16(a0, p0, acc0, 0, 0, 0);
        acc1 = __builtin_amdgcn_mfma_f32_32x32x16_f16(a0, q0, acc1, 0, 0, 0);
        acc1 = __builtin_amdgcn_mfma_f32_32x32x16_f16(c0, p0, acc1, 0, 0, 0);
        acc0 = __builtin_amdgcn_mfma_f32_32x32x16_f16(a1, p1, acc0, 0, 0, 0);
        acc1 = __builtin_amdgcn_mfma_f32_32x32x16_f16(a1, q1, acc1, 0, 0, 0);
        acc1 = __builtin_amdgcn_mfma_f32_32x32x16_f16(c1, p1, acc1, 0, 0, 0);
        __builtin_amdgcn_sched_barrier(0);
        if (it < 7) { a0 = na0; a1 = na1; c0 = nc0; c1 = nc1; p0 = np0; p1 = np1; q0 = nq0; q1 = nq1; }
      }
    }
    DIAG_STAMP(5);
    RAW_BAR();
    float* red = (float*)cbase;
#pragma unroll
    for (int g = 0; g < 4; ++g) {
      f32x4 v0 = {acc0[4 * g], acc0[4 * g + 1], acc0[4 * g + 2], acc0[4 * g + 3]};
      f32x4 v1 = {acc1[4 * g], acc1[4 * g + 1], acc1[4 * g + 2], acc1[4 * g + 3]};
      *(f32x4*)(red + (w4 * 32 + r) * 36 + 8 * g + 4 * h) = v0;
      *(f32x4*)(red + ((4 + w4) * 32 + r) * 36 + 8 * g + 4 * h) = v1;
    }
    if (tid == 511 && hcnt < 16u) {
      gu32* cnt = (gu32*)(ws + WS_CNT + (mt2 >> 2) * 256);
      for (unsigned spins = 0; spins < (1u << 22); ++spins) {
        if (__hip_atomic_load(cnt, __ATOMIC_RELAXED, __HIP_MEMORY_SCOPE_AGENT) >= 16u) break;
        __builtin_amdgcn_s_sleep(1);
      }
    }
    RAW_BAR();
    DIAG_STAMP2(1);
    u16* at = (u16*)(ws + WS_AT) + (size_t)d * L_;
    {
      const int c = t2;
      const int T = c >> 7, a = (c >> 2) & 31, b0 = (c & 3) * 8;
      f32x4 s0 = {}, s1 = {};
      const float* p = red + (T * 4 * 32 + a) * 36 + b0;
#pragma unroll
      for (int w = 0; w < 4; ++w) { s0 += *(const f32x4*)(p + w * 32 * 36); s1 += *(const f32x4*)(p + w * 32 * 36 + 4); }
      u32x4 o;
#pragma unroll
      for (int j = 0; j < 4; ++j) {
        const f2 y = unpack2((j < 2) ? pack2(s0[2 * j] * kinv, s0[2 * j + 1] * kinv) : pack2(s1[2 * j - 4] * kinv, s1[2 * j - 3] * kinv));
        const f2 us = unpack2(uv0[j]) * dsk;
        const f2 t = unpack2(pack2(us.x, us.y)) + y;
        u32 ov = pack2(t.x, t.y);
        if ((ov & 0xffffu) == 0xffffu) ov &= 0xffffffc0u;
        if ((ov >> 16) == 0xffffu) ov &= 0xffc0ffffu;
        o[j] = ov;
      }
      st16_wt(at + c * 8, o);
    }
  }
  DIAG_STAMP2(2);

  {
    f32x4 acc[8][2] = {};
    bf16x8 hb[7][2];
#pragma unroll
    for (int k = 0; k < 7; ++k)
#pragma unroll
      for (int i = 0; i < 2; ++i) hb[k][i] = G2_LD(hr, i, k, 16);
    const int m = tid >> 4, n0 = (tid & 15) * 8;
    const size_t go = (size_t)(mt2 * 32 + m) * D_ + nt2 * 128 + n0;
    const u32x4 xv = *(const u32x4*)(x + go);
    __builtin_amdgcn_sched_barrier(0);
#pragma unroll
    for (int k = 0; k < 7; ++k) {
#pragma unroll
      for (int j = 0; j < 8; ++j)
#pragma unroll
        for (int i = 0; i < 2; ++i) acc[j][i] = __builtin_amdgcn_mfma_f32_16x16x32_bf16(wd[k % 3][j], hb[k][i], acc[j][i], 0, 0, 0);
      if (k < 4) {
#pragma unroll
        for (int j = 0; j < 8; ++j) wd[k % 3][j] = G2_LD(wr, j, k + 3, 0);
      }
      __builtin_amdgcn_sched_barrier(0);
    }
    DIAG_STAMP2(3);
    u32 a0, a1, a2, a3, a4, a5, a6, a7;
    const u16* at = (const u16*)(ws + WS_AT) + (size_t)(nt2 * 128 + n0) * L_ + mt2 * 32 + m;
#define A_LOADS(tail)                                                                                              \
  asm volatile("global_load_ushort %0, %8, off sc1\n\t"                                                            \
               "global_load_ushort %1, %9, off sc1\n\t"                                                            \
               "global_load_ushort %2, %10, off sc1\n\t"                                                           \
               "global_load_ushort %3, %11, off sc1\n\t"                                                           \
               "global_load_ushort %4, %12, off sc1\n\t"                                                           \
               "global_load_ushort %5, %13, off sc1\n\t"                                                           \
               "global_load_ushort %6, %14, off sc1\n\t"                                                           \
               "global_load_ushort %7, %15, off sc1" tail                                                          \
               : "=&v"(a0), "=&v"(a1), "=&v"(a2), "=&v"(a3), "=&v"(a4), "=&v"(a5), "=&v"(a6), "=&v"(a7)            \
               : "v"(at), "v"(at + L_), "v"(at + 2 * L_), "v"(at + 3 * L_), "v"(at + 4 * L_), "v"(at + 5 * L_),   \
                 "v"(at + 6 * L_), "v"(at + 7 * L_)                                                                \
               : "memory")
    A_LOADS("");
    RAW_BAR();
    float* red = (float*)smem;
    float* rp = red + (wid * 32 + (lane & 15)) * 132 + 4 * (lane >> 4);
#pragma unroll
    for (int j = 0; j < 8; ++j)
#pragma unroll
      for (int i = 0; i < 2; ++i) *(f32x4*)(rp + i * 16 * 132 + j * 16) = acc[j][i];
    asm volatile("s_waitcnt vmcnt(0)" : "+v"(a0), "+v"(a1), "+v"(a2), "+v"(a3), "+v"(a4), "+v"(a5), "+v"(a6), "+v"(a7)::"memory");
    for (unsigned spins = 0; spins < (1u << 20); ++spins) {
      const bool pend = (a0 == 0xffffu) | (a1 == 0xffffu) | (a2 == 0xffffu) | (a3 == 0xffffu) | (a4 == 0xffffu) |
                        (a5 == 0xffffu) | (a6 == 0xffffu) | (a7 == 0xffffu);
      if (!__any(pend)) break;
      __builtin_amdgcn_s_sleep(2);
      A_LOADS("\n\ts_waitcnt vmcnt(0)");
    }
    RAW_BAR();
    DIAG_STAMP2(4);
    f32x4 p0 = {}, p1 = {};
#pragma unroll
    for (int w = 0; w < 8; ++w) {
      p0 += *(const f32x4*)(red + (w * 32 + m) * 132 + n0);
      p1 += *(const f32x4*)(red + (w * 32 + m) * 132 + n0 + 4);
    }
    const u32 av[8] = {a0, a1, a2, a3, a4, a5, a6, a7};
    u32x4 o;
#pragma unroll
    for (int j = 0; j < 4; ++j) {
      const f2 xx = unpack2(xv[j]);
      const f2 aa = f2{__uint_as_float(av[2 * j] << 16), __uint_as_float(av[2 * j + 1] << 16)};
      const f2 r1 = xx + aa;
      const f2 bb = unpack2((j < 2) ? pack2(p0[2 * j], p0[2 * j + 1]) : pack2(p1[2 * j - 4], p1[2 * j - 3]));
      const f2 r2 = unpack2(pack2(r1.x, r1.y)) + bb;
      o[j] = pack2(r2.x, r2.y);
    }
    *(u32x4*)(out + go) = o;
  }
}

extern "C" void kernel_launch(void* const* d_in, const int* in_sizes, int n_in, void* d_out, int out_size,
                              void* d_ws, size_t ws_size, hipStream_t stream) {
  const u16* x = (const u16*)d_in[0];
  const u16* logA = (const u16*)d_in[1];
  const u16* Aim = (const u16*)d_in[2];
  const u16* Cre = (const u16*)d_in[3];
  const u16* Cim = (const u16*)d_in[4];
  const u16* logdt = (const u16*)d_in[5];
  const u16* dskip = (const u16*)d_in[6];
  const u16* wg = (const u16*)d_in[8];
  const u16* wu = (const u16*)d_in[9];
  const u16* wd = (const u16*)d_in[10];
  char* ws = (char*)d_ws;
  (void)hipFuncSetAttribute((const void*)k2_kernel, hipFuncAttributeMaxDynamicSharedMemorySize, K2_LDS);
  k1_kernel<<<K1_LN_BLOCKS + K1_WT_BLOCKS + K1_KG_BLOCKS, 256, 0, stream>>>(x, logA, Aim, Cre, Cim, logdt, wg, wu, wd, ws);
  k2_kernel<<<256, 512, K2_LDS, stream>>>(x, dskip, ws, (u16*)d_out);
}
```

```cpp
#include <hip/hip_runtime.h>
#include <hip/hip_bf16.h>
#define WT_STORES
#define CV_PRIO 1
#define G2_PRIO 3

typedef unsigned short u16;
typedef unsigned int u32;
using bf16x8 = __attribute__((ext_vector_type(8))) short;
using f16x8 = __attribute__((ext_vector_type(8))) _Float16;
using f16x4 = __attribute__((ext_vector_type(4))) _Float16;
using f32x4 = __attribute__((ext_vector_type(4))) float;
using f32x16 = __attribute__((ext_vector_type(16))) float;
using u32x4 = __attribute__((ext_vector_type(4))) unsigned int;
using u32x2 = __attribute__((ext_vector_type(2))) unsigned int;
typedef float f2 __attribute__((ext_vector_type(2)));
typedef __attribute__((address_space(1))) unsigned gu32;

constexpr int L_ = 2048, D_ = 512, N_ = 128, F_ = 1792;
constexpr int KC_STRIDE = 648;

constexpr size_t WS_CNT = 0;
constexpr size_t WS_XNT = 4096;
constexpr size_t WS_XNTR = WS_XNT + (size_t)L_ * D_ * 2;
constexpr size_t WS_KC = WS_XNTR + (size_t)L_ * D_ * 2;
constexpr size_t WS_H = WS_KC + (size_t)D_ * KC_STRIDE * 4;
constexpr size_t WS_WG = WS_H + (size_t)L_ * F_ * 2;
constexpr size_t WS_WU = WS_WG + (size_t)D_ * F_ * 2;
constexpr size_t WS_WD = WS_WU + (size_t)D_ * F_ * 2;
constexpr size_t WS_AT = WS_WD + (size_t)D_ * F_ * 2;

#define DIAG_START(k)
#define DIAG_STAMP(i)
#define DIAG_END(wsp)
#define DIAG_STAMP2(i)

__device__ __forceinline__ float bf2f(u16 v) { return __uint_as_float(((u32)v) << 16); }
__device__ __forceinline__ u16 f2bf(float f) {
  __bf16 b = (__bf16)f;
  return __builtin_bit_cast(u16, b);
}
__device__ __forceinline__ float rbf(float f) { return bf2f(f2bf(f)); }
__device__ __forceinline__ u32 pack2(float lo, float hi) {
  typedef __bf16 b2 __attribute__((ext_vector_type(2)));
  b2 r = __builtin_convertvector(f2{lo, hi}, b2);
  return __builtin_bit_cast(u32, r);
}
__device__ __forceinline__ f2 unpack2(u32 w) { return f2{__uint_as_float(w << 16), __uint_as_float(w & 0xffff0000u)}; }

__device__ __forceinline__ void st16_wt(void* p, u32x4 v) { asm volatile("global_store_dwordx4 %0, %1, off sc1\n\ts_nop 1" ::"v"(p), "v"(v) : "memory"); }
__device__ __forceinline__ void st16(void* p, u32x4 v) { st16_wt(p, v); }
__device__ __forceinline__ void st8(void* p, u32x2 v) { asm volatile("global_store_dwordx2 %0, %1, off sc1\n\ts_nop 1" ::"v"(p), "v"(v) : "memory"); }

#define DPP_F(v, ctrl) __builtin_bit_cast(float, __builtin_amdgcn_update_dpp(0, __builtin_bit_cast(int, v), ctrl, 0xf, 0xf, true))
__device__ __forceinline__ float wave_sum(float v) {
  v += DPP_F(v, 0xB1);
  v += DPP_F(v, 0x4E);
  v += DPP_F(v, 0x141);
  v += DPP_F(v, 0x140);
  const int iv = __builtin_bit_cast(int, v);
  return (__builtin_bit_cast(float, __builtin_amdgcn_readlane(iv, 0)) + __builtin_bit_cast(float, __builtin_amdgcn_readlane(iv, 16))) +
         (__builtin_bit_cast(float, __builtin_amdgcn_readlane(iv, 32)) + __builtin_bit_cast(float, __builtin_amdgcn_readlane(iv, 48)));
}
__device__ __forceinline__ float wave_max(float v) {
  v = fmaxf(v, DPP_F(v, 0xB1));
  v = fmaxf(v, DPP_F(v, 0x4E));
  v = fmaxf(v, DPP_F(v, 0x141));
  v = fmaxf(v, DPP_F(v, 0x140));
  const int iv = __builtin_bit_cast(int, v);
  return fmaxf(fmaxf(__builtin_bit_cast(float, __builtin_amdgcn_readlane(iv, 0)), __builtin_bit_cast(float, __builtin_amdgcn_readlane(iv, 16))),
               fmaxf(__builtin_bit_cast(float, __builtin_amdgcn_readlane(iv, 32)), __builtin_bit_cast(float, __builtin_amdgcn_readlane(iv, 48))));
}

constexpr int K1_LN_BLOCKS = 128;
constexpr int K1_WT_BLOCKS = 336;
constexpr int K1_KG_BLOCKS = 512;

__device__ __forceinline__ void cmul6(f2& r0, f2& r1, f2& r2, f2& r3, f2& r4, f2& r5, f2 a0, f2 b0, f2 a1, f2 b1,
                                      f2 a2, f2 b2, f2 a3, f2 b3, f2 a4, f2 b4, f2 a5, f2 b5) {
  asm("v_pk_mul_f32 %0, %6, %7 op_sel:[1,1] op_sel_hi:[1,0] neg_lo:[0,1]\n\t"
      "v_pk_mul_f32 %1, %8, %9 op_sel:[1,1] op_sel_hi:[1,0] neg_lo:[0,1]\n\t"
      "v_pk_mul_f32 %2, %10, %11 op_sel:[1,1] op_sel_hi:[1,0] neg_lo:[0,1]\n\t"
      "v_pk_mul_f32 %3, %12, %13 op_sel:[1,1] op_sel_hi:[1,0] neg_lo:[0,1]\n\t"
      "v_pk_mul_f32 %4, %14, %15 op_sel:[1,1] op_sel_hi:[1,0] neg_lo:[0,1]\n\t"
      "v_pk_mul_f32 %5, %16, %17 op_sel:[1,1] op_sel_hi:[1,0] neg_lo:[0,1]\n\t"
      "v_pk_fma_f32 %0, %6, %7, %0 op_sel:[0,0,0] op_sel_hi:[0,1,1]\n\t"
      "v_pk_fma_f32 %1, %8, %9, %1 op_sel:[0,0,0] op_sel_hi:[0,1,1]\n\t"
      "v_pk_fma_f32 %2, %10, %11, %2 op_sel:[0,0,0] op_sel_hi:[0,1,1]\n\t"
      "v_pk_fma_f32 %3, %12, %13, %3 op_sel:[0,0,0] op_sel_hi:[0,1,1]\n\t"
      "v_pk_fma_f32 %4, %14, %15, %4 op_sel:[0,0,0] op_sel_hi:[0,1,1]\n\t"
      "v_pk_fma_f32 %5, %16, %17, %5 op_sel:[0,0,0] op_sel_hi:[0,1,1]\n\t"
      "s_nop 1"
      : "=&v"(r0), "=&v"(r1), "=&v"(r2), "=&v"(r3), "=&v"(r4), "=&v"(r5)
      : "v"(a0), "v"(b0), "v"(a1), "v"(b1), "v"(a2), "v"(b2), "v"(a3), "v"(b3), "v"(a4), "v"(b4), "v"(a5), "v"(b5));
}
__device__ __forceinline__ void cmul2(f2& r0, f2& r1, f2 a0, f2 b0, f2 a1, f2 b1) {
  asm("v_pk_mul_f32 %0, %2, %3 op_sel:[1,1] op_sel_hi:[1,0] neg_lo:[0,1]\n\t"
      "v_pk_mul_f32 %1, %4, %5 op_sel:[1,1] op_sel_hi:[1,0] neg_lo:[0,1]\n\t"
      "s_nop 0\n\t"
      "v_pk_fma_f32 %0, %2, %3, %0 op_sel:[0,0,0] op_sel_hi:[0,1,1]\n\t"
      "v_pk_fma_f32 %1, %4, %5, %1 op_sel:[0,0,0] op_sel_hi:[0,1,1]\n\t"
      "s_nop 1"
      : "=&v"(r0), "=&v"(r1)
      : "v"(a0), "v"(b0), "v"(a1), "v"(b1));
}
__device__ __forceinline__ u32x2 split_h(f2 v) {
  typedef _Float16 h2 __attribute__((ext_vector_type(2)));
  const h2 h = __builtin_bit_cast(h2, __builtin_amdgcn_cvt_pkrtz(v.x, v.y));
  const f2 r = v - f2{(float)h.x, (float)h.y};
  return u32x2{__builtin_bit_cast(u32, h), __builtin_bit_cast(u32, __builtin_amdgcn_cvt_pkrtz(r.x, r.y))};
}
__device__ __forceinline__ f2 cexp_(float zr, float zi, float t) {
  const float m = __builtin_amdgcn_exp2f((zr * t) * 1.44269504088896341f);
  float r = (zi * t) * 0.15915494309189535f;
  r -= rintf(r);
  return f2{m * __builtin_amdgcn_cosf(r), m * __builtin_amdgcn_sinf(r)};
}

__global__ __launch_bounds__(256) void k1_kernel(const u16* __restrict__ x, const u16* __restrict__ logA,
                                                 const u16* __restrict__ Aim, const u16* __restrict__ Cre,
                                                 const u16* __restrict__ Cim, const u16* __restrict__ logdt,
                                                 const u16* __restrict__ wg, const u16* __restrict__ wu,
                                                 const u16* __restrict__ wd, char* __restrict__ ws) {
  __shared__ __attribute__((aligned(16))) char smem[18448];
  const int tid = threadIdx.x, lane = tid & 63, wid = tid >> 6;
  DIAG_START(0)
  int bid = blockIdx.x - K1_KG_BLOCKS;
  if (bid >= 0 && bid < K1_LN_BLOCKS) {
    __builtin_amdgcn_s_setprio(3);
    if (bid == 0 && tid < 16) *(u32*)(ws + WS_CNT + tid * 256) = 0u;
#pragma unroll
    for (int it = 0; it < 4; ++it) st16(ws + WS_AT + ((size_t)bid * 1024 + it * 256 + tid) * 16, u32x4{0xffffffffu, 0xffffffffu, 0xffffffffu, 0xffffffffu});
    u16* tile = (u16*)smem;
    u16* xnt = (u16*)(ws + WS_XNT);
    const int r0 = bid * 16;
    u32x4 v[4];
#pragma unroll
    for (int i = 0; i < 4; ++i) v[i] = *(const u32x4*)(x + (size_t)(r0 + wid * 4 + i) * D_ + lane * 8);
#pragma unroll
    for (int i = 0; i < 4; ++i) {
      const int rl = wid * 4 + i;
      f2 f[4];
#pragma unroll
      for (int j = 0; j < 4; ++j) f[j] = unpack2(v[i][j]);
      const f2 s2 = (f[0] + f[1]) + (f[2] + f[3]);
      const float mean = wave_sum(s2.x + s2.y) * (1.f / D_);
      f2 q2 = {0.f, 0.f};
#pragma unroll
      for (int j = 0; j < 4; ++j) { const f2 t = f[j] - mean; q2 = __builtin_elementwise_fma(t, t, q2); }
      const float q = wave_sum(q2.x + q2.y);
      const float mb = rbf(mean);
      const float vb = rbf(q * (1.f / D_));
      const float sd = rbf(sqrtf(rbf(vb + 1e-5f)));
      const float rs = __builtin_amdgcn_rcpf(sd);
      u32x4 o;
#pragma unroll
      for (int j = 0; j < 4; ++j) {
        const f2 t = f[j] - mb;
        const f2 a = unpack2(pack2(t.x, t.y));
        f2 qq = a * rs;
        const f2 rem = __builtin_elementwise_fma(-qq, f2{sd, sd}, a);
        qq = __builtin_elementwise_fma(rem, f2{rs, rs}, qq);
        o[j] = pack2(qq.x, qq.y);
      }
      *(u32x4*)(tile + rl * 520 + lane * 8) = o;
    }
    DIAG_STAMP(1);
    __syncthreads();
#pragma unroll
    for (int it = 0; it < 4; ++it) {
      const int c = tid + it * 256;
      const u32x4 o = *(const u32x4*)(tile + (c & 15) * 520 + (c >> 4) * 8);
      st16(xnt + ((size_t)bid * 1024 + c) * 8, o);
    }
    u16* xtr = (u16*)(ws + WS_XNTR);
#pragma unroll
    for (int it = 0; it < 4; ++it) {
      const int c = tid + it * 256;
      const int d = c >> 1, g = c & 1;
      u32x4 o;
#pragma unroll
      for (int j = 0; j < 4; ++j)
        o[j] = (u32)tile[(g * 8 + 2 * j) * 520 + d] | ((u32)tile[(g * 8 + 2 * j + 1) * 520 + d] << 16);
      st16(xtr + ((size_t)bid * 1024 + c) * 8, o);
    }
    DIAG_END(ws);
    return;
  }
  bid -= K1_LN_BLOCKS;
  if (bid >= 0) {
    __builtin_amdgcn_s_setprio(2);
    int task = bid * 4 + wid;
    const u16* W; u16* Wt; int Kd, Nd;
    if (task < 448) { W = wg; Wt = (u16*)(ws + WS_WG); Kd = D_; Nd = F_; }
    else if (task < 896) { task -= 448; W = wu; Wt = (u16*)(ws + WS_WU); Kd = D_; Nd = F_; }
    else { task -= 896; W = wd; Wt = (u16*)(ws + WS_WD); Kd = F_; Nd = D_; }
    const int nks = Kd / 32;
    const int ks = task % nks, nb = task / nks;
    const int col = nb * 64 + lane;
    u16 v[32];
#pragma unroll
    for (int k = 0; k < 32; ++k) v[k] = W[(size_t)(ks * 32 + k) * Nd + col];
#pragma unroll
    for (int q = 0; q < 4; ++q) {
      u32x4 o;
#pragma unroll
      for (int j = 0; j < 4; ++j) o[j] = (u32)v[q * 8 + 2 * j] | ((u32)v[q * 8 + 2 * j + 1] << 16);
      st16(Wt + ((size_t)((col >> 4) * nks + ks) * 64 + q * 16 + (col & 15)) * 8, o);
    }
    DIAG_END(ws);
    return;
  }
  bid = blockIdx.x;
  {
    const int d = bid;
    float4* prm = (float4*)(smem + 16384);
    float* cmx = (float*)(smem + 16384 + 2048);
    if (tid < 128) {
      const u16 la = logA[d * N_ + tid], ai = Aim[d * N_ + tid], cr = Cre[d * N_ + tid], ci = Cim[d * N_ + tid];
      const float dt = rbf(expf(bf2f(logdt[d])));
      const float ar = -rbf(expf(bf2f(la)));
      const float c0 = bf2f(cr) * dt, c1 = bf2f(ci) * dt;
      prm[tid] = make_float4(ar * dt, bf2f(ai) * dt, c0, c1);
      const float mx = wave_max(fmaxf(fabsf(c0), fabsf(c1)));
      if (lane == 0) cmx[wid] = mx;
    }
    __syncthreads();
    DIAG_STAMP(1);
    float cs, cinv;
    {
      int e = (int)((__float_as_uint(fmaxf(cmx[0], cmx[1])) >> 23) & 0xff);
      e = min(max(e, 2), 252);
      cs = __uint_as_float((u32)(254 - e) << 23);
      cinv = __uint_as_float((u32)e << 23);
    }
    const int kq = lane >> 4, idx = lane & 15;
    f32x4 acc[4] = {};
    const float ty = (float)idx, te = (float)(idx == 0 ? 256 : 16 * idx);
    float4 pr[8];
#pragma unroll
    for (int s = 0; s < 8; ++s) pr[s] = prm[wid * 32 + (s >> 2) * 16 + kq * 4 + (s & 3)];
#pragma unroll
    for (int t = 0; t < 2; ++t) {
      u32x4 Xh[4], Xl[4], Yh[4], Yl[4];
#pragma unroll
      for (int s = 0; s < 4; s += 2) {
        const float4 pa = pr[t * 4 + s], pb = pr[t * 4 + s + 1];
        const f2 Ca = f2{pa.z, -pa.w} * cs, Cb = f2{pb.z, -pb.w} * cs;
        f2 Ya[4], Yb[4], Xa[4], Xb[4];
        Ya[0] = cexp_(pa.x, pa.y, ty); Yb[0] = cexp_(pb.x, pb.y, ty);
        f2 Ea = cexp_(pa.x, -pa.y, te), Eb = cexp_(pb.x, -pb.y, te);
        f2 Fa = (idx == 0) ? f2{1.f, 0.f} : Ea, Fb = (idx == 0) ? f2{1.f, 0.f} : Eb;
#pragma unroll
        for (int e = 0; e < 3; ++e) {
          f2 nEa, nEb;
          cmul6(Xa[e], Xb[e], Ya[e + 1], Yb[e + 1], nEa, nEb, Ca, Fa, Cb, Fb, Ya[e], Ya[e], Yb[e], Yb[e], Ea, Ea, Eb, Eb);
          Ea = nEa; Eb = nEb; Fa = nEa; Fb = nEb;
        }
        cmul2(Xa[3], Xb[3], Ca, Ea, Cb, Eb);
#pragma unroll
        for (int e = 0; e < 4; ++e) {
          const u32x2 xa2 = split_h(Xa[e]), xb2 = split_h(Xb[e]), ya2 = split_h(Ya[e]), yb2 = split_h(Yb[e]);
          Xh[e][s] = xa2[0]; Xl[e][s] = xa2[1]; Xh[e][s + 1] = xb2[0]; Xl[e][s + 1] = xb2[1];
          Yh[e][s] = ya2[0]; Yl[e][s] = ya2[1]; Yh[e][s + 1] = yb2[0]; Yl[e][s + 1] = yb2[1];
        }
      }
#pragma unroll
      for (int e = 0; e < 4; ++e) acc[e] = __builtin_amdgcn_mfma_f32_16x16x32_f16(__builtin_bit_cast(f16x8, Xl[e]), __builtin_bit_cast(f16x8, Yh[e]), acc[e], 0, 0, 0);
#pragma unroll
      for (int e = 0; e < 4; ++e) acc[e] = __builtin_amdgcn_mfma_f32_16x16x32_f16(__builtin_bit_cast(f16x8, Xh[e]), __builtin_bit_cast(f16x8, Yl[e]), acc[e], 0, 0, 0);
#pragma unroll
      for (int e = 0; e < 4; ++e) acc[e] = __builtin_amdgcn_mfma_f32_16x16x32_f16(__builtin_bit_cast(f16x8, Xh[e]), __builtin_bit_cast(f16x8, Yh[e]), acc[e], 0, 0, 0);
    }
    DIAG_STAMP(2);
    float* red = (float*)smem;
#pragma unroll
    for (int e = 0; e < 4; ++e)
#pragma unroll
      for (int r = 0; r < 4; ++r) red[(wid * 4 + e) * 256 + ((lane >> 4) * 4 + r) * 16 + (lane & 15)] = acc[e][r];
    __syncthreads();
    float* kc = (float*)(ws + WS_KC) + (size_t)d * KC_STRIDE;
#pragma unroll
    for (int e = 0; e < 4; ++e) {
      const float v = (2.f * cinv) * ((red[(0 * 4 + e) * 256 + tid] + red[(1 * 4 + e) * 256 + tid]) +
                                      (red[(2 * 4 + e) * 256 + tid] + red[(3 * 4 + e) * 256 + tid]));
      if (e == 0) kc[tid] = v;
      else if (tid >= 128) kc[128 * e + tid] = v;
      else if (e == 3 && tid == 0) kc[640] = v;
    }
    DIAG_END(ws);
  }
}

constexpr int G1_STAGE = 22 * 1024;
constexpr int G1_NBUF = 7;
constexpr int K2_LDS = G1_STAGE * G1_NBUF;

constexpr int UC_LEN = 2080;
constexpr int KR_LEN = 3840;
constexpr int CV_UCP = 0;
constexpr int CV_KR = CV_UCP + 8 * UC_LEN * 2;
constexpr int CV_URAW = CV_KR + KR_LEN * 2;
constexpr int CV_KRAW = CV_URAW + 4224;
constexpr int CV_MAX = CV_KRAW + 2592;
constexpr int CV_CH_STRIDE = 48128;
constexpr int H_STAGE = 2 * CV_CH_STRIDE;

__device__ __forceinline__ f2 gelu2(f2 g) {
  const float c0 = -2.f * 0.7978845608028654f * 1.44269504088896341f;
  const float c1 = c0 * 0.044715f;
  const f2 t = g * __builtin_elementwise_fma(g * g, f2{c1, c1}, f2{c0, c0});
  const f2 dn = f2{__builtin_amdgcn_exp2f(t.x), __builtin_amdgcn_exp2f(t.y)} + 1.f;
  return g * f2{__builtin_amdgcn_rcpf(dn.x), __builtin_amdgcn_rcpf(dn.y)};
}
__device__ __forceinline__ void glds16(const void* g, void* l) {
  __builtin_amdgcn_global_load_lds((const __attribute__((address_space(1))) u32*)g,
                                   (__attribute__((address_space(3))) u32*)l, 16, 0, 0);
}
__device__ __forceinline__ u32 bf2_to_h2(u32 w) {
  auto r = __builtin_amdgcn_cvt_pkrtz(__uint_as_float(w << 16), __uint_as_float(w & 0xffff0000u));
  return __builtin_bit_cast(u32, r);
}
#define RAW_BAR() do { asm volatile("s_waitcnt lgkmcnt(0)" ::: "memory"); __builtin_amdgcn_s_barrier(); } while (0)

__global__ __launch_bounds__(512, 2) void k2_kernel(const u16* __restrict__ x, const u16* __restrict__ dskip,
                                                    char* __restrict__ ws, u16* __restrict__ out) {
  extern __shared__ __attribute__((aligned(16))) char smem[];
  const int tid = threadIdx.x, lane = tid & 63;
  const int wid = __builtin_amdgcn_readfirstlane(tid >> 6);
  DIAG_START(1)
  const int bid = blockIdx.x;
  const int xcd = bid & 7, loc = bid >> 3;
  const int ch = wid >> 2, w4 = wid & 3;
  const int d = bid * 2 + ch;
  const int t2 = tid & 255;
  const int mt = (xcd >> 1) * 4 + (loc & 3);
  const int nt = (xcd & 1) * 8 + (loc >> 2);

  const int mt2 = (xcd >> 1) * 8 + (xcd & 1) * 4 + (loc & 3);
  const int nt2 = loc >> 2;
  const int vo = lane * 16;
  const __amdgpu_buffer_rsrc_t hr = __builtin_amdgcn_make_buffer_rsrc((void*)((const u16*)(ws + WS_H) + ((size_t)(mt2 * 4) * 56 + wid * 7) * 512), 0, 4 * 56 * 1024, 0x00020000);
  const __amdgpu_buffer_rsrc_t wr = __builtin_amdgcn_make_buffer_rsrc((void*)((const u16*)(ws + WS_WD) + ((size_t)(nt2 * 4) * 56 + wid * 7) * 512), 0, 4 * 56 * 1024, 0x00020000);
#define G2_LD(rs, i, k, aux) __builtin_bit_cast(bf16x8, __builtin_amdgcn_raw_buffer_load_b128(rs, vo, ((i) * 56 + (k)) * 1024, aux))
  bf16x8 wd[7][4];
  f32x16 acc0 = {}, acc1 = {};
  float kinv;

  u32x4 uv0; float kv0, kv1, kv2; u32 pds;
  {
    const u16* xnt = (const u16*)(ws + WS_XNT);
    const u16* wgt = (const u16*)(ws + WS_WG);
    const u16* wut = (const u16*)(ws + WS_WU);
    auto src = [&](int f) -> const u16* {
      if (f < 8) return xnt + (size_t)(mt * 8 + f) * 16 * 512;
      const int s = f - 8;
      return ((s & 1) ? wut : wgt) + (size_t)(nt * 7 + (s >> 1)) * 16 * 512;
    };
    const u16* s0 = src(wid) + lane * 8;
    const u16* s1 = src(wid + 8) + lane * 8;
    const u16* s2 = src(wid < 6 ? wid + 16 : 21) + lane * 8;
    auto stage = [&](int ks) {
      char* base = smem + (ks % G1_NBUF) * G1_STAGE + wid * 1024 + lane * 16;
      glds16(s0 + ks * 512, base);
      glds16(s1 + ks * 512, base + 8192);
      if (wid < 6) glds16(s2 + ks * 512, base + 16384);
    };
#define G1_WAIT(n) do { if (wid < 6) asm volatile("s_waitcnt vmcnt(%0)" ::"n"(3 * (n)) : "memory"); \
                        else asm volatile("s_waitcnt vmcnt(%0)" ::"n"(2 * (n)) : "memory"); } while (0)
#pragma unroll
    for (int k = 0; k < G1_NBUF; ++k) stage(k);
    const int wm = wid >> 1, wn = wid & 1;
    f32x4 acc[7][2] = {};
    bf16x8 xa[2], wa[7], xb[2], wb[7];
    G1_WAIT(G1_NBUF - 1);
    RAW_BAR();
    DIAG_STAMP(1);
    {
      const char* base = smem + lane * 16;
#pragma unroll
      for (int i = 0; i < 2; ++i) xa[i] = *(const bf16x8*)(base + (wm * 2 + i) * 1024);
#pragma unroll
      for (int j = 0; j < 7; ++j) wa[j] = *(const bf16x8*)(base + (8 + wn * 7 + j) * 1024);
    }
#define G1_ITER(k, XC, WC, XN, WN, NLEFT)                                                          \
  do {                                                                                             \
    G1_WAIT(NLEFT);                                                                                \
    RAW_BAR();                                                                                     \
    const char* rb_ = smem + (((k) + 1) % G1_NBUF) * G1_STAGE + lane * 16;                         \
    _Pragma("unroll") for (int j = 0; j < 7; ++j) {                                                \
      acc[j][0] = __builtin_amdgcn_mfma_f32_16x16x32_bf16(WC[j], XC[0], acc[j][0], 0, 0, 0);       \
      if (j < 2) XN[j] = *(const bf16x8*)(rb_ + (wm * 2 + j) * 1024);                              \
      acc[j][1] = __builtin_amdgcn_mfma_f32_16x16x32_bf16(WC[j], XC[1], acc[j][1], 0, 0, 0);       \
      WN[j] = *(const bf16x8*)(rb_ + (8 + wn * 7 + j) * 1024);                                     \
      __builtin_amdgcn_sched_barrier(0);                                                           \
      if (j == 3 && (k) + G1_NBUF < 16) { stage((k) + G1_NBUF); __builtin_amdgcn_sched_barrier(0); } \
    }                                                                                              \
  } while (0)
    G1_ITER(0, xa, wa, xb, wb, 5);
    G1_ITER(1, xb, wb, xa, wa, 5);
    G1_ITER(2, xa, wa, xb, wb, 5);
    G1_ITER(3, xb, wb, xa, wa, 5);
    G1_ITER(4, xa, wa, xb, wb, 5);
    G1_ITER(5, xb, wb, xa, wa, 5);
    G1_ITER(6, xa, wa, xb, wb, 5);
    G1_ITER(7, xb, wb, xa, wa, 5);
    G1_ITER(8, xa, wa, xb, wb, 5);
    G1_ITER(9, xb, wb, xa, wa, 5);
    G1_ITER(10, xa, wa, xb, wb, 4);
    G1_ITER(11, xb, wb, xa, wa, 3);
    G1_ITER(12, xa, wa, xb, wb, 2);
    G1_ITER(13, xb, wb, xa, wa, 1);
    G1_ITER(14, xa, wa, xb, wb, 0);
    {
      const u16* ug = (const u16*)(ws + WS_XNTR) + (size_t)(t2 >> 1) * 8192 + d * 16 + (t2 & 1) * 8;
      const float* kc = (const float*)(ws + WS_KC) + (size_t)d * KC_STRIDE;
      uv0 = *(const u32x4*)ug;
      kv0 = kc[t2]; kv1 = kc[t2 + 256]; kv2 = (t2 < 129) ? kc[t2 + 512] : 0.f;
      pds = dskip[d];
    }
    __builtin_amdgcn_sched_barrier(0);
#pragma unroll
    for (int j = 0; j < 7; ++j)
#pragma unroll
      for (int i = 0; i < 2; ++i) acc[j][i] = __builtin_amdgcn_mfma_f32_16x16x32_bf16(wb[j], xb[i], acc[j][i], 0, 0, 0);
    DIAG_STAMP(2);
    RAW_BAR();
    f32x4* ex = (f32x4*)(smem + H_STAGE + 28672);
    if (wn == 0) {
#pragma unroll
      for (int i = 0; i < 2; ++i) ex[(wm * 2 + i) * 64 + lane] = acc[6][i];
    }
    char* cbase = smem + ch * CV_CH_STRIDE;
    u16* ucp = (u16*)(cbase + CV_UCP);
    u16* kr = (u16*)(cbase + CV_KR);
    u32x4* uraw = (u32x4*)(cbase + CV_URAW);
    float* kraw = (float*)(cbase + CV_KRAW);
    float* kmax = (float*)(cbase + CV_MAX);
    {
      if (t2 < 4) { uraw[t2] = u32x4{0, 0, 0, 0}; uraw[260 + t2] = u32x4{0, 0, 0, 0}; }
      u32x4 h;
#pragma unroll
      for (int j = 0; j < 4; ++j) h[j] = bf2_to_h2(uv0[j]);
      uraw[4 + t2] = h;
      kraw[t2] = kv0;
      kraw[t2 + 256] = kv1;
      if (t2 < 136) kraw[t2 + 512] = kv2;
      const float mx = wave_max(fmaxf(fmaxf(fabsf(kv0), fabsf(kv1)), fabsf(kv2)));
      if (lane == 0) kmax[w4] = mx;
    }
    RAW_BAR();
    f32x4 g3[2];
#pragma unroll
    for (int i = 0; i < 2; ++i) g3[i] = ex[(wm * 2 + i) * 64 + lane];
    float kscale;
    {
      const float mx = fmaxf(fmaxf(kmax[0], kmax[1]), fmaxf(kmax[2], kmax[3]));
      int e = (int)((__float_as_uint(mx) >> 23) & 0xff);
      e = min(max(e, 20), 240);
      kscale = __uint_as_float((u32)(127 + 13 + 127 - e) << 23);
      kinv = __uint_as_float((u32)(e - 13) << 23);
    }
#pragma unroll
    for (int it = 0; it < 2; ++it) {
      const int tb = it ? 256 + (t2 & 3) : t2;
      u32 w[8];
      u32x4 a = uraw[tb], b = uraw[tb + 1];
#pragma unroll
      for (int j = 0; j < 4; ++j) { w[j] = a[j]; w[4 + j] = b[j]; }
      if (it == 0 || t2 < 4) {
#pragma unroll
        for (int c = 0; c < 8; ++c) {
          u32x4 o;
#pragma unroll
          for (int j = 0; j < 4; ++j)
            o[j] = (c & 1) ? w[j + (c + 1) / 2] : __builtin_amdgcn_alignbit(w[j + c / 2 + 1], w[j + c / 2], 16);
          *(u32x4*)(ucp + c * UC_LEN + tb * 8) = o;
        }
      }
    }
    {
      float kk[8];
#pragma unroll
      for (int j = 0; j < 8; ++j) {
        const int jj = 2047 - (t2 * 8 + j);
        const int cbi = (int)f2bf((float)jj) - 17024;
        kk[j] = kraw[jj < 128 ? jj : cbi] * kscale;
      }
      f16x8 o;
#pragma unroll
      for (int j = 0; j < 8; ++j) o[j] = (_Float16)kk[j];
      *(f16x8*)(kr + t2 * 8 + 8 * (t2 >> 2)) = o;
      if (t2 < 128) { const int tb = t2 + 256; *(u32x4*)(kr + tb * 8 + 8 * (tb >> 2)) = u32x4{0, 0, 0, 0}; }
    }
    RAW_BAR();
    DIAG_STAMP(3);
    char* hs = smem + H_STAGE;
    auto emit = [&](int t, const f32x4& g, const f32x4& u, int i) {
      u32x2 o;
#pragma unroll
      for (int p = 0; p < 2; ++p) {
        const f2 gb = unpack2(pack2(g[2 * p], g[2 * p + 1]));
        const f2 ub = unpack2(pack2(u[2 * p], u[2 * p + 1]));
        const f2 ge = gelu2(gb);
        const f2 hv = unpack2(pack2(ge.x, ge.y)) * ub;
        o[p] = pack2(hv.x, hv.y);
      }
      *(u32x2*)(hs + (((wm * 2 + i) * 7 + t) * 32 + (lane >> 5) * 16 + (lane & 15)) * 16 + ((lane >> 4) & 1) * 8) = o;
    };
    const int r = lane & 31, h = lane >> 5;
    const u16* ap = ucp + (r & 7) * UC_LEN + 8 * h + 8 * (r >> 3);
    const int bofs0 = 2048 - 32 * (r + 1) + 8 * h + 8 * (63 - r);
    const int bofs1 = bofs0 - 1024 - 256;
    const u16* apw = ap + 16 * (w4 * 16);
    const u16* bp0 = kr + bofs0 + 16 * (w4 * 16) + 8 * (w4 * 8);
    const u16* bp1 = kr + bofs1 + 16 * (w4 * 16) + 8 * (w4 * 8);
    auto lda = [&](int s) -> f16x8 { return *(const f16x8*)(apw + 16 * s); };
    auto ldb = [&](const u16* b, int s) -> f16x8 { return *(const f16x8*)(b + 16 * s + 8 * (s >> 1)); };
    f32x4 ug[8], uu[8];
#pragma unroll
    for (int u = 0; u < 8; ++u) {
      const int i = u & 1, tt = u >> 1;
      if (tt == 0) { ug[u] = wn ? g3[i] : acc[0][i]; uu[u] = wn ? acc[0][i] : acc[1][i]; }
      else if (tt < 3) { ug[u] = wn ? acc[2 * tt - 1][i] : acc[2 * tt][i]; uu[u] = wn ? acc[2 * tt][i] : acc[2 * tt + 1][i]; }
      else { ug[u] = acc[5][i]; uu[u] = acc[6][i]; }
    }
    f16x8 a0 = lda(0), p0 = ldb(bp0, 0), q0 = ldb(bp1, 0), c0 = lda(64);
    f16x8 a1 = lda(1), p1 = ldb(bp0, 1), q1 = ldb(bp1, 1), c1 = lda(65);
#pragma unroll
    for (int it = 0; it < 8; ++it) {
      f16x8 na0, na1, nc0, nc1, np0, np1, nq0, nq1;
      if (it < 7) {
        const int sn = 2 * it + 2;
        na0 = lda(sn); np0 = ldb(bp0, sn); nq0 = ldb(bp1, sn); nc0 = lda(sn + 64);
        na1 = lda(sn + 1); np1 = ldb(bp0, sn + 1); nq1 = ldb(bp1, sn + 1); nc1 = lda(sn + 65);
      }
      if (it >= 4 && it < 7) {
#pragma unroll
        for (int i = 0; i < 4; ++i) wd[it - 4][i] = G2_LD(wr, i, it - 4, 0);
      }
      __builtin_amdgcn_sched_barrier(0);
      acc0 = __builtin_amdgcn_mfma_f32_32x32x16_f16(a0, p0, acc0, 0, 0, 0);
      acc1 = __builtin_amdgcn_mfma_f32_32x32x16_f16(a0, q0, acc1, 0, 0, 0);
      acc1 = __builtin_amdgcn_mfma_f32_32x32x16_f16(c0, p0, acc1, 0, 0, 0);
      acc0 = __builtin_amdgcn_mfma_f32_32x32x16_f16(a1, p1, acc0, 0, 0, 0);
      acc1 = __builtin_amdgcn_mfma_f32_32x32x16_f16(a1, q1, acc1, 0, 0, 0);
      acc1 = __builtin_amdgcn_mfma_f32_32x32x16_f16(c1, p1, acc1, 0, 0, 0);
      if (it < 6 || wn) emit(wn * 3 + (it >> 1), ug[it], uu[it], it & 1);
#pragma unroll
      for (int g = 0; g < 6; ++g) {
        __builtin_amdgcn_sched_group_barrier(0x8, 1, 0);
        __builtin_amdgcn_sched_group_barrier(0x2, 8, 0);
      }
      __builtin_amdgcn_sched_barrier(0);
      if (it < 7) { a0 = na0; a1 = na1; c0 = nc0; c1 = nc1; p0 = np0; p1 = np1; q0 = nq0; q1 = nq1; }
    }
    DIAG_STAMP(4);
    RAW_BAR();
    u16* hT = (u16*)(ws + WS_H);
#pragma unroll
    for (int it = 0; it < 4; ++it) {
      const int c = tid + it * 512;
      if (c < 1792) {
        const int ml = c / 224, rem = c - ml * 224;
        st16_wt(hT + ((size_t)(mt * 8 + ml) * 56 * 64 + (size_t)nt * 7 * 32 + rem) * 8, *(const u32x4*)(hs + c * 16));
      }
    }
    float* red = (float*)cbase;
#pragma unroll
    for (int g = 0; g < 4; ++g) {
      f32x4 v0 = {acc0[4 * g], acc0[4 * g + 1], acc0[4 * g + 2], acc0[4 * g + 3]};
      f32x4 v1 = {acc1[4 * g], acc1[4 * g + 1], acc1[4 * g + 2], acc1[4 * g + 3]};
      *(f32x4*)(red + (w4 * 32 + r) * 36 + 8 * g + 4 * h) = v0;
      *(f32x4*)(red + ((4 + w4) * 32 + r) * 36 + 8 * g + 4 * h) = v1;
    }
    asm volatile("s_waitcnt vmcnt(0)" ::: "memory");
    RAW_BAR();
    if (tid == 0) __hip_atomic_fetch_add((gu32*)(ws + WS_CNT + mt * 256), 1u, __ATOMIC_RELAXED, __HIP_MEMORY_SCOPE_AGENT);
    DIAG_STAMP(5);
#pragma unroll
    for (int k = 3; k < 7; ++k)
#pragma unroll
      for (int i = 0; i < 4; ++i) wd[k][i] = G2_LD(wr, i, k, 0);
    __builtin_amdgcn_sched_barrier(0);
    u16* at = (u16*)(ws + WS_AT) + (size_t)d * L_;
    {
      const float dsk = bf2f((u16)pds);
      const int c = t2;
      const int T = c >> 7, a = (c >> 2) & 31, b0 = (c & 3) * 8;
      f32x4 s0 = {}, s1 = {};
      const float* p = red + (T * 4 * 32 + a) * 36 + b0;
#pragma unroll
      for (int w = 0; w < 4; ++w) { s0 += *(const f32x4*)(p + w * 32 * 36); s1 += *(const f32x4*)(p + w * 32 * 36 + 4); }
      u32x4 o;
#pragma unroll
      for (int j = 0; j < 4; ++j) {
        const f2 y = unpack2((j < 2) ? pack2(s0[2 * j] * kinv, s0[2 * j + 1] * kinv) : pack2(s1[2 * j - 4] * kinv, s1[2 * j - 3] * kinv));
        const f2 us = unpack2(uv0[j]) * dsk;
        const f2 t = unpack2(pack2(us.x, us.y)) + y;
        u32 ov = pack2(t.x, t.y);
        if ((ov & 0xffffu) == 0xffffu) ov &= 0xffffffc0u;
        if ((ov >> 16) == 0xffffu) ov &= 0xffc0ffffu;
        o[j] = ov;
      }
      st16_wt(at + c * 8, o);
    }
    DIAG_STAMP2(1);
    if (tid == 511) {
      gu32* cnt = (gu32*)(ws + WS_CNT + (mt2 >> 1) * 256);
      for (unsigned spins = 0; spins < (1u << 22); ++spins) {
        if (__hip_atomic_load(cnt, __ATOMIC_RELAXED, __HIP_MEMORY_SCOPE_AGENT) >= 16u) break;
        __builtin_amdgcn_s_sleep(1);
      }
    }
    RAW_BAR();
  }
  DIAG_STAMP2(2);

  {
    f32x4 acc[4][4] = {};
    bf16x8 hb[4][4];
#pragma unroll
    for (int k = 0; k < 4; ++k)
#pragma unroll
      for (int i = 0; i < 4; ++i) hb[k][i] = G2_LD(hr, i, k, 16);
    const int m = tid >> 3, n0 = (tid & 7) * 8;
    const size_t go = (size_t)(mt2 * 64 + m) * D_ + nt2 * 64 + n0;
    const u32x4 xv = *(const u32x4*)(x + go);
    __builtin_amdgcn_sched_barrier(0);
#pragma unroll
    for (int k = 0; k < 7; ++k) {
#pragma unroll
      for (int j = 0; j < 4; ++j)
#pragma unroll
        for (int i = 0; i < 4; ++i) acc[j][i] = __builtin_amdgcn_mfma_f32_16x16x32_bf16(wd[k][j], hb[k & 3][i], acc[j][i], 0, 0, 0);
      if (k < 3) {
#pragma unroll
        for (int i = 0; i < 4; ++i) hb[k][i] = G2_LD(hr, i, k + 4, 16);
      }
      __builtin_amdgcn_sched_barrier(0);
    }
    DIAG_STAMP2(3);
    u32 a0, a1, a2, a3, a4, a5, a6, a7;
    const u16* at = (const u16*)(ws + WS_AT) + (size_t)(nt2 * 64 + n0) * L_ + mt2 * 64 + m;
#define A_LOADS(tail)                                                                                              \
  asm volatile("global_load_ushort %0, %8, off sc1\n\t"                                                            \
               "global_load_ushort %1, %9, off sc1\n\t"                                                            \
               "global_load_ushort %2, %10, off sc1\n\t"                                                           \
               "global_load_ushort %3, %11, off sc1\n\t"                                                           \
               "global_load_ushort %4, %12, off sc1\n\t"                                                           \
               "global_load_ushort %5, %13, off sc1\n\t"                                                           \
               "global_load_ushort %6, %14, off sc1\n\t"                                                           \
               "global_load_ushort %7, %15, off sc1" tail                                                          \
               : "=&v"(a0), "=&v"(a1), "=&v"(a2), "=&v"(a3), "=&v"(a4), "=&v"(a5), "=&v"(a6), "=&v"(a7)            \
               : "v"(at), "v"(at + L_), "v"(at + 2 * L_), "v"(at + 3 * L_), "v"(at + 4 * L_), "v"(at + 5 * L_),   \
                 "v"(at + 6 * L_), "v"(at + 7 * L_)                                                                \
               : "memory")
    A_LOADS("");
    RAW_BAR();
    float* red = (float*)smem;
    float* rp = red + (wid * 64 + (lane & 15)) * 68 + 4 * (lane >> 4);
#pragma unroll
    for (int j = 0; j < 4; ++j)
#pragma unroll
      for (int i = 0; i < 4; ++i) *(f32x4*)(rp + i * 16 * 68 + j * 16) = acc[j][i];
    asm volatile("s_waitcnt vmcnt(0)" : "+v"(a0), "+v"(a1), "+v"(a2), "+v"(a3), "+v"(a4), "+v"(a5), "+v"(a6), "+v"(a7)::"memory");
    for (unsigned spins = 0; spins < (1u << 20); ++spins) {
      const bool pend = (a0 == 0xffffu) | (a1 == 0xffffu) | (a2 == 0xffffu) | (a3 == 0xffffu) | (a4 == 0xffffu) |
                        (a5 == 0xffffu) | (a6 == 0xffffu) | (a7 == 0xffffu);
      if (!__any(pend)) break;
      __builtin_amdgcn_s_sleep(2);
      A_LOADS("\n\ts_waitcnt vmcnt(0)");
    }
    RAW_BAR();
    DIAG_STAMP2(4);
    f32x4 p0 = {}, p1 = {};
#pragma unroll
    for (int w = 0; w < 8; ++w) {
      p0 += *(const f32x4*)(red + (w * 64 + m) * 68 + n0);
      p1 += *(const f32x4*)(red + (w * 64 + m) * 68 + n0 + 4);
    }
    const u32 av[8] = {a0, a1, a2, a3, a4, a5, a6, a7};
    u32x4 o;
#pragma unroll
    for (int j = 0; j < 4; ++j) {
      const f2 xx = unpack2(xv[j]);
      const f2 aa = f2{__uint_as_float(av[2 * j] << 16), __uint_as_float(av[2 * j + 1] << 16)};
      const f2 r1 = xx + aa;
      const f2 bb = unpack2((j < 2) ? pack2(p0[2 * j], p0[2 * j + 1]) : pack2(p1[2 * j - 4], p1[2 * j - 3]));
      const f2 r2 = unpack2(pack2(r1.x, r1.y)) + bb;
      o[j] = pack2(r2.x, r2.y);
    }
    *(u32x4*)(out + go) = o;
  }
}

extern "C" void kernel_launch(void* const* d_in, const int* in_sizes, int n_in, void* d_out, int out_size,
                              void* d_ws, size_t ws_size, hipStream_t stream) {
  const u16* x = (const u16*)d_in[0];
  const u16* logA = (const u16*)d_in[1];
  const u16* Aim = (const u16*)d_in[2];
  const u16* Cre = (const u16*)d_in[3];
  const u16* Cim = (const u16*)d_in[4];
  const u16* logdt = (const u16*)d_in[5];
  const u16* dskip = (const u16*)d_in[6];
  const u16* wg = (const u16*)d_in[8];
  const u16* wu = (const u16*)d_in[9];
  const u16* wd = (const u16*)d_in[10];
  char* ws = (char*)d_ws;
  (void)hipFuncSetAttribute((const void*)k2_kernel, hipFuncAttributeMaxDynamicSharedMemorySize, K2_LDS);
  k1_kernel<<<K1_LN_BLOCKS + K1_WT_BLOCKS + K1_KG_BLOCKS, 256, 0, stream>>>(x, logA, Aim, Cre, Cim, logdt, wg, wu, wd, ws);
  k2_kernel<<<256, 512, K2_LDS, stream>>>(x, dskip, ws, (u16*)d_out);
}
```

```cpp
#include <hip/hip_runtime.h>
#include <hip/hip_bf16.h>


typedef unsigned short u16;
typedef unsigned int u32;
using bf16x8 = __attribute__((ext_vector_type(8))) short;
using f16x8 = __attribute__((ext_vector_type(8))) _Float16;
using f16x4 = __attribute__((ext_vector_type(4))) _Float16;
using f32x4 = __attribute__((ext_vector_type(4))) float;
using f32x16 = __attribute__((ext_vector_type(16))) float;
using u32x4 = __attribute__((ext_vector_type(4))) unsigned int;
using u32x2 = __attribute__((ext_vector_type(2))) unsigned int;
typedef float f2 __attribute__((ext_vector_type(2)));
typedef __attribute__((address_space(1))) unsigned gu32;

constexpr int L_ = 2048, D_ = 512, N_ = 128, F_ = 1792;
constexpr int KC_STRIDE = 648;

constexpr size_t WS_CNT = 0;
constexpr size_t WS_XNT = 4096;
constexpr size_t WS_XNTR = WS_XNT + (size_t)L_ * D_ * 2;
constexpr size_t WS_KC = WS_XNTR + (size_t)L_ * D_ * 2;
constexpr size_t WS_H = WS_KC + (size_t)D_ * KC_STRIDE * 4;
constexpr size_t WS_WG = WS_H + (size_t)L_ * F_ * 2;
constexpr size_t WS_WU = WS_WG + (size_t)D_ * F_ * 2;
constexpr size_t WS_WD = WS_WU + (size_t)D_ * F_ * 2;
constexpr size_t WS_AT = WS_WD + (size_t)D_ * F_ * 2;


__device__ __forceinline__ float bf2f(u16 v) { return __uint_as_float(((u32)v) << 16); }
__device__ __forceinline__ u16 f2bf(float f) {
  __bf16 b = (__bf16)f;
  return __builtin_bit_cast(u16, b);
}
__device__ __forceinline__ float rbf(float f) { return bf2f(f2bf(f)); }
__device__ __forceinline__ u32 pack2(float lo, float hi) {
  typedef __bf16 b2 __attribute__((ext_vector_type(2)));
  b2 r = __builtin_convertvector(f2{lo, hi}, b2);
  return __builtin_bit_cast(u32, r);
}
__device__ __forceinline__ f2 unpack2(u32 w) { return f2{__uint_as_float(w << 16), __uint_as_float(w & 0xffff0000u)}; }

__device__ __forceinline__ void st16_wt(void* p, u32x4 v) { asm volatile("global_store_dwordx4 %0, %1, off sc1\n\ts_nop 1" ::"v"(p), "v"(v) : "memory"); }
__device__ __forceinline__ void st16(void* p, u32x4 v) { st16_wt(p, v); }

#define DPP_F(v, ctrl) __builtin_bit_cast(float, __builtin_amdgcn_update_dpp(0, __builtin_bit_cast(int, v), ctrl, 0xf, 0xf, true))
__device__ __forceinline__ float wave_sum(float v) {
  v += DPP_F(v, 0xB1);
  v += DPP_F(v, 0x4E);
  v += DPP_F(v, 0x141);
  v += DPP_F(v, 0x140);
  const int iv = __builtin_bit_cast(int, v);
  return (__builtin_bit_cast(float, __builtin_amdgcn_readlane(iv, 0)) + __builtin_bit_cast(float, __builtin_amdgcn_readlane(iv, 16))) +
         (__builtin_bit_cast(float, __builtin_amdgcn_readlane(iv, 32)) + __builtin_bit_cast(float, __builtin_amdgcn_readlane(iv, 48)));
}
__device__ __forceinline__ float wave_max(float v) {
  v = fmaxf(v, DPP_F(v, 0xB1));
  v = fmaxf(v, DPP_F(v, 0x4E));
  v = fmaxf(v, DPP_F(v, 0x141));
  v = fmaxf(v, DPP_F(v, 0x140));
  const int iv = __builtin_bit_cast(int, v);
  return fmaxf(fmaxf(__builtin_bit_cast(float, __builtin_amdgcn_readlane(iv, 0)), __builtin_bit_cast(float, __builtin_amdgcn_readlane(iv, 16))),
               fmaxf(__builtin_bit_cast(float, __builtin_amdgcn_readlane(iv, 32)), __builtin_bit_cast(float, __builtin_amdgcn_readlane(iv, 48))));
}

constexpr int K1_LN_BLOCKS = 128;
constexpr int K1_WT_BLOCKS = 336;
constexpr int K1_KG_BLOCKS = 512;

__device__ __forceinline__ void cmul6(f2& r0, f2& r1, f2& r2, f2& r3, f2& r4, f2& r5, f2 a0, f2 b0, f2 a1, f2 b1,
                                      f2 a2, f2 b2, f2 a3, f2 b3, f2 a4, f2 b4, f2 a5, f2 b5) {
  asm("v_pk_mul_f32 %0, %6, %7 op_sel:[1,1] op_sel_hi:[1,0] neg_lo:[0,1]\n\t"
      "v_pk_mul_f32 %1, %8, %9 op_sel:[1,1] op_sel_hi:[1,0] neg_lo:[0,1]\n\t"
      "v_pk_mul_f32 %2, %10, %11 op_sel:[1,1] op_sel_hi:[1,0] neg_lo:[0,1]\n\t"
      "v_pk_mul_f32 %3, %12, %13 op_sel:[1,1] op_sel_hi:[1,0] neg_lo:[0,1]\n\t"
      "v_pk_mul_f32 %4, %14, %15 op_sel:[1,1] op_sel_hi:[1,0] neg_lo:[0,1]\n\t"
      "v_pk_mul_f32 %5, %16, %17 op_sel:[1,1] op_sel_hi:[1,0] neg_lo:[0,1]\n\t"
      "v_pk_fma_f32 %0, %6, %7, %0 op_sel:[0,0,0] op_sel_hi:[0,1,1]\n\t"
      "v_pk_fma_f32 %1, %8, %9, %1 op_sel:[0,0,0] op_sel_hi:[0,1,1]\n\t"
      "v_pk_fma_f32 %2, %10, %11, %2 op_sel:[0,0,0] op_sel_hi:[0,1,1]\n\t"
      "v_pk_fma_f32 %3, %12, %13, %3 op_sel:[0,0,0] op_sel_hi:[0,1,1]\n\t"
      "v_pk_fma_f32 %4, %14, %15, %4 op_sel:[0,0,0] op_sel_hi:[0,1,1]\n\t"
      "v_pk_fma_f32 %5, %16, %17, %5 op_sel:[0,0,0] op_sel_hi:[0,1,1]\n\t"
      "s_nop 1"
      : "=&v"(r0), "=&v"(r1), "=&v"(r2), "=&v"(r3), "=&v"(r4), "=&v"(r5)
      : "v"(a0), "v"(b0), "v"(a1), "v"(b1), "v"(a2), "v"(b2), "v"(a3), "v"(b3), "v"(a4), "v"(b4), "v"(a5), "v"(b5));
}
__device__ __forceinline__ void cmul2(f2& r0, f2& r1, f2 a0, f2 b0, f2 a1, f2 b1) {
  asm("v_pk_mul_f32 %0, %2, %3 op_sel:[1,1] op_sel_hi:[1,0] neg_lo:[0,1]\n\t"
      "v_pk_mul_f32 %1, %4, %5 op_sel:[1,1] op_sel_hi:[1,0] neg_lo:[0,1]\n\t"
      "s_nop 0\n\t"
      "v_pk_fma_f32 %0, %2, %3, %0 op_sel:[0,0,0] op_sel_hi:[0,1,1]\n\t"
      "v_pk_fma_f32 %1, %4, %5, %1 op_sel:[0,0,0] op_sel_hi:[0,1,1]\n\t"
      "s_nop 1"
      : "=&v"(r0), "=&v"(r1)
      : "v"(a0), "v"(b0), "v"(a1), "v"(b1));
}
__device__ __forceinline__ u32x2 split_h(f2 v) {
  typedef _Float16 h2 __attribute__((ext_vector_type(2)));
  const h2 h = __builtin_bit_cast(h2, __builtin_amdgcn_cvt_pkrtz(v.x, v.y));
  const f2 r = v - f2{(float)h.x, (float)h.y};
  return u32x2{__builtin_bit_cast(u32, h), __builtin_bit_cast(u32, __builtin_amdgcn_cvt_pkrtz(r.x, r.y))};
}
__device__ __forceinline__ f2 cexp_(float zr, float zi, float t) {
  const float m = __builtin_amdgcn_exp2f((zr * t) * 1.44269504088896341f);
  float r = (zi * t) * 0.15915494309189535f;
  r -= rintf(r);
  return f2{m * __builtin_amdgcn_cosf(r), m * __builtin_amdgcn_sinf(r)};
}

__global__ __launch_bounds__(256) void k1_kernel(const u16* __restrict__ x, const u16* __restrict__ logA,
                                                 const u16* __restrict__ Aim, const u16* __restrict__ Cre,
                                                 const u16* __restrict__ Cim, const u16* __restrict__ logdt,
                                                 const u16* __restrict__ wg, const u16* __restrict__ wu,
                                                 const u16* __restrict__ wd, char* __restrict__ ws) {
  __shared__ __attribute__((aligned(16))) char smem[18448];
  const int tid = threadIdx.x, lane = tid & 63, wid = tid >> 6;
  int bid = blockIdx.x - K1_KG_BLOCKS;
  if (bid >= 0 && bid < K1_LN_BLOCKS) {
    __builtin_amdgcn_s_setprio(3);
    if (bid == 0 && tid < 16) *(u32*)(ws + WS_CNT + tid * 256) = 0u;
#pragma unroll
    for (int it = 0; it < 4; ++it) st16(ws + WS_AT + ((size_t)bid * 1024 + it * 256 + tid) * 16, u32x4{0xffffffffu, 0xffffffffu, 0xffffffffu, 0xffffffffu});
    u16* tile = (u16*)smem;
    u16* xnt = (u16*)(ws + WS_XNT);
    const int r0 = bid * 16;
    u32x4 v[4];
#pragma unroll
    for (int i = 0; i < 4; ++i) v[i] = *(const u32x4*)(x + (size_t)(r0 + wid * 4 + i) * D_ + lane * 8);
#pragma unroll
    for (int i = 0; i < 4; ++i) {
      const int rl = wid * 4 + i;
      f2 f[4];
#pragma unroll
      for (int j = 0; j < 4; ++j) f[j] = unpack2(v[i][j]);
      const f2 s2 = (f[0] + f[1]) + (f[2] + f[3]);
      const float mean = wave_sum(s2.x + s2.y) * (1.f / D_);
      f2 q2 = {0.f, 0.f};
#pragma unroll
      for (int j = 0; j < 4; ++j) { const f2 t = f[j] - mean; q2 = __builtin_elementwise_fma(t, t, q2); }
      const float q = wave_sum(q2.x + q2.y);
      const float mb = rbf(mean);
      const float vb = rbf(q * (1.f / D_));
      const float sd = rbf(sqrtf(rbf(vb + 1e-5f)));
      const float rs = __builtin_amdgcn_rcpf(sd);
      u32x4 o;
#pragma unroll
      for (int j = 0; j < 4; ++j) {
        const f2 t = f[j] - mb;
        const f2 a = unpack2(pack2(t.x, t.y));
        f2 qq = a * rs;
        const f2 rem = __builtin_elementwise_fma(-qq, f2{sd, sd}, a);
        qq = __builtin_elementwise_fma(rem, f2{rs, rs}, qq);
        o[j] = pack2(qq.x, qq.y);
      }
      *(u32x4*)(tile + rl * 520 + lane * 8) = o;
    }
    __syncthreads();
#pragma unroll
    for (int it = 0; it < 4; ++it) {
      const int c = tid + it * 256;
      const u32x4 o = *(const u32x4*)(tile + (c & 15) * 520 + (c >> 4) * 8);
      st16(xnt + ((size_t)bid * 1024 + c) * 8, o);
    }
    u16* xtr = (u16*)(ws + WS_XNTR);
#pragma unroll
    for (int it = 0; it < 4; ++it) {
      const int c = tid + it * 256;
      const int d = c >> 1, g = c & 1;
      u32x4 o;
#pragma unroll
      for (int j = 0; j < 4; ++j)
        o[j] = (u32)tile[(g * 8 + 2 * j) * 520 + d] | ((u32)tile[(g * 8 + 2 * j + 1) * 520 + d] << 16);
      st16(xtr + ((size_t)bid * 1024 + c) * 8, o);
    }
    return;
  }
  bid -= K1_LN_BLOCKS;
  if (bid >= 0) {
    __builtin_amdgcn_s_setprio(2);
    int task = bid * 4 + wid;
    const u16* W; u16* Wt; int Kd, Nd;
    if (task < 448) { W = wg; Wt = (u16*)(ws + WS_WG); Kd = D_; Nd = F_; }
    else if (task < 896) { task -= 448; W = wu; Wt = (u16*)(ws + WS_WU); Kd = D_; Nd = F_; }
    else { task -= 896; W = wd; Wt = (u16*)(ws + WS_WD); Kd = F_; Nd = D_; }
    const int nks = Kd / 32;
    const int ks = task % nks, nb = task / nks;
    const int col = nb * 64 + lane;
    u16 v[32];
#pragma unroll
    for (int k = 0; k < 32; ++k) v[k] = W[(size_t)(ks * 32 + k) * Nd + col];
#pragma unroll
    for (int q = 0; q < 4; ++q) {
      u32x4 o;
#pragma unroll
      for (int j = 0; j < 4; ++j) o[j] = (u32)v[q * 8 + 2 * j] | ((u32)v[q * 8 + 2 * j + 1] << 16);
      st16(Wt + ((size_t)((col >> 4) * nks + ks) * 64 + q * 16 + (col & 15)) * 8, o);
    }
    return;
  }
  bid = blockIdx.x;
  {
    const int d = bid;
    float4* prm = (float4*)(smem + 16384);
    float* cmx = (float*)(smem + 16384 + 2048);
    if (tid < 128) {
      const u16 la = logA[d * N_ + tid], ai = Aim[d * N_ + tid], cr = Cre[d * N_ + tid], ci = Cim[d * N_ + tid];
      const float dt = rbf(expf(bf2f(logdt[d])));
      const float ar = -rbf(expf(bf2f(la)));
      const float c0 = bf2f(cr) * dt, c1 = bf2f(ci) * dt;
      prm[tid] = make_float4(ar * dt, bf2f(ai) * dt, c0, c1);
      const float mx = wave_max(fmaxf(fabsf(c0), fabsf(c1)));
      if (lane == 0) cmx[wid] = mx;
    }
    __syncthreads();
    float cs, cinv;
    {
      int e = (int)((__float_as_uint(fmaxf(cmx[0], cmx[1])) >> 23) & 0xff);
      e = min(max(e, 2), 252);
      cs = __uint_as_float((u32)(254 - e) << 23);
      cinv = __uint_as_float((u32)e << 23);
    }
    const int kq = lane >> 4, idx = lane & 15;
    f32x4 acc[4] = {};
    const float ty = (float)idx, te = (float)(idx == 0 ? 256 : 16 * idx);
    float4 pr[8];
#pragma unroll
    for (int s = 0; s < 8; ++s) pr[s] = prm[wid * 32 + (s >> 2) * 16 + kq * 4 + (s & 3)];
#pragma unroll
    for (int t = 0; t < 2; ++t) {
      u32x4 Xh[4], Xl[4], Yh[4], Yl[4];
#pragma unroll
      for (int s = 0; s < 4; s += 2) {
        const float4 pa = pr[t * 4 + s], pb = pr[t * 4 + s + 1];
        const f2 Ca = f2{pa.z, -pa.w} * cs, Cb = f2{pb.z, -pb.w} * cs;
        f2 Ya[4], Yb[4], Xa[4], Xb[4];
        Ya[0] = cexp_(pa.x, pa.y, ty); Yb[0] = cexp_(pb.x, pb.y, ty);
        f2 Ea = cexp_(pa.x, -pa.y, te), Eb = cexp_(pb.x, -pb.y, te);
        f2 Fa = (idx == 0) ? f2{1.f, 0.f} : Ea, Fb = (idx == 0) ? f2{1.f, 0.f} : Eb;
#pragma unroll
        for (int e = 0; e < 3; ++e) {
          f2 nEa, nEb;
          cmul6(Xa[e], Xb[e], Ya[e + 1], Yb[e + 1], nEa, nEb, Ca, Fa, Cb, Fb, Ya[e], Ya[e], Yb[e], Yb[e], Ea, Ea, Eb, Eb);
          Ea = nEa; Eb = nEb; Fa = nEa; Fb = nEb;
        }
        cmul2(Xa[3], Xb[3], Ca, Ea, Cb, Eb);
#pragma unroll
        for (int e = 0; e < 4; ++e) {
          const u32x2 xa2 = split_h(Xa[e]), xb2 = split_h(Xb[e]), ya2 = split_h(Ya[e]), yb2 = split_h(Yb[e]);
          Xh[e][s] = xa2[0]; Xl[e][s] = xa2[1]; Xh[e][s + 1] = xb2[0]; Xl[e][s + 1] = xb2[1];
          Yh[e][s] = ya2[0]; Yl[e][s] = ya2[1]; Yh[e][s + 1] = yb2[0]; Yl[e][s + 1] = yb2[1];
        }
      }
#pragma unroll
      for (int e = 0; e < 4; ++e) acc[e] = __builtin_amdgcn_mfma_f32_16x16x32_f16(__builtin_bit_cast(f16x8, Xl[e]), __builtin_bit_cast(f16x8, Yh[e]), acc[e], 0, 0, 0);
#pragma unroll
      for (int e = 0; e < 4; ++e) acc[e] = __builtin_amdgcn_mfma_f32_16x16x32_f16(__builtin_bit_cast(f16x8, Xh[e]), __builtin_bit_cast(f16x8, Yl[e]), acc[e], 0, 0, 0);
#pragma unroll
      for (int e = 0; e < 4; ++e) acc[e] = __builtin_amdgcn_mfma_f32_16x16x32_f16(__builtin_bit_cast(f16x8, Xh[e]), __builtin_bit_cast(f16x8, Yh[e]), acc[e], 0, 0, 0);
    }
    float* red = (float*)smem;
#pragma unroll
    for (int e = 0; e < 4; ++e)
#pragma unroll
      for (int r = 0; r < 4; ++r) red[(wid * 4 + e) * 256 + ((lane >> 4) * 4 + r) * 16 + (lane & 15)] = acc[e][r];
    __syncthreads();
    float* kc = (float*)(ws + WS_KC) + (size_t)d * KC_STRIDE;
#pragma unroll
    for (int e = 0; e < 4; ++e) {
      const float v = (2.f * cinv) * ((red[(0 * 4 + e) * 256 + tid] + red[(1 * 4 + e) * 256 + tid]) +
                                      (red[(2 * 4 + e) * 256 + tid] + red[(3 * 4 + e) * 256 + tid]));
      if (e == 0) kc[tid] = v;
      else if (tid >= 128) kc[128 * e + tid] = v;
      else if (e == 3 && tid == 0) kc[640] = v;
    }
  }
}

constexpr int G1_STAGE = 22 * 1024;
constexpr int G1_NBUF = 7;
constexpr int K2_LDS = G1_STAGE * G1_NBUF;

constexpr int UC_LEN = 2080;
constexpr int KR_LEN = 3840;
constexpr int CV_UCP = 0;
constexpr int CV_KR = CV_UCP + 8 * UC_LEN * 2;
constexpr int CV_URAW = CV_KR + KR_LEN * 2;
constexpr int CV_KRAW = CV_URAW + 4224;
constexpr int CV_MAX = CV_KRAW + 2592;
constexpr int CV_CH_STRIDE = 48128;
constexpr int H_STAGE = 2 * CV_CH_STRIDE;

__device__ __forceinline__ f2 gelu2(f2 g) {
  const float c0 = -2.f * 0.7978845608028654f * 1.44269504088896341f;
  const float c1 = c0 * 0.044715f;
  const f2 t = g * __builtin_elementwise_fma(g * g, f2{c1, c1}, f2{c0, c0});
  const f2 dn = f2{__builtin_amdgcn_exp2f(t.x), __builtin_amdgcn_exp2f(t.y)} + 1.f;
  return g * f2{__builtin_amdgcn_rcpf(dn.x), __builtin_amdgcn_rcpf(dn.y)};
}
__device__ __forceinline__ void glds16(const void* g, void* l) {
  __builtin_amdgcn_global_load_lds((const __attribute__((address_space(1))) u32*)g,
                                   (__attribute__((address_space(3))) u32*)l, 16, 0, 0);
}
__device__ __forceinline__ u32 bf2_to_h2(u32 w) {
  auto r = __builtin_amdgcn_cvt_pkrtz(__uint_as_float(w << 16), __uint_as_float(w & 0xffff0000u));
  return __builtin_bit_cast(u32, r);
}
#define RAW_BAR() do { asm volatile("s_waitcnt lgkmcnt(0)" ::: "memory"); __builtin_amdgcn_s_barrier(); } while (0)

__global__ __launch_bounds__(512, 2) void k2_kernel(const u16* __restrict__ x, const u16* __restrict__ dskip,
                                                    char* __restrict__ ws, u16* __restrict__ out) {
  extern __shared__ __attribute__((aligned(16))) char smem[];
  const int tid = threadIdx.x, lane = tid & 63;
  const int wid = __builtin_amdgcn_readfirstlane(tid >> 6);
  const int bid = blockIdx.x;
  const int xcd = bid & 7, loc = bid >> 3;
  const int ch = wid >> 2, w4 = wid & 3;
  const int d = bid * 2 + ch;
  const int t2 = tid & 255;
  const int mt = (xcd >> 1) * 4 + (loc & 3);
  const int nt = (xcd & 1) * 8 + (loc >> 2);

  u32x4 uv0; float kv0, kv1, kv2; u32 pds;
  {
    const u16* xnt = (const u16*)(ws + WS_XNT);
    const u16* wgt = (const u16*)(ws + WS_WG);
    const u16* wut = (const u16*)(ws + WS_WU);
    auto src = [&](int f) -> const u16* {
      if (f < 8) return xnt + (size_t)(mt * 8 + f) * 16 * 512;
      const int s = f - 8;
      return ((s & 1) ? wut : wgt) + (size_t)(nt * 7 + (s >> 1)) * 16 * 512;
    };
    const u16* s0 = src(wid) + lane * 8;
    const u16* s1 = src(wid + 8) + lane * 8;
    const u16* s2 = src(wid < 6 ? wid + 16 : 21) + lane * 8;
    auto stage = [&](int ks) {
      char* base = smem + (ks % G1_NBUF) * G1_STAGE + wid * 1024 + lane * 16;
      glds16(s0 + ks * 512, base);
      glds16(s1 + ks * 512, base + 8192);
      if (wid < 6) glds16(s2 + ks * 512, base + 16384);
    };
#define G1_WAIT(n) do { if (wid < 6) asm volatile("s_waitcnt vmcnt(%0)" ::"n"(3 * (n)) : "memory"); \
                        else asm volatile("s_waitcnt vmcnt(%0)" ::"n"(2 * (n)) : "memory"); } while (0)
#pragma unroll
    for (int k = 0; k < G1_NBUF; ++k) stage(k);
    const int wm = wid >> 1, wn = wid & 1;
    f32x4 acc[7][2] = {};
    bf16x8 xa[2], wa[7], xb[2], wb[7];
    G1_WAIT(G1_NBUF - 1);
    RAW_BAR();
    {
      const char* base = smem + lane * 16;
#pragma unroll
      for (int i = 0; i < 2; ++i) xa[i] = *(const bf16x8*)(base + (wm * 2 + i) * 1024);
#pragma unroll
      for (int j = 0; j < 7; ++j) wa[j] = *(const bf16x8*)(base + (8 + wn * 7 + j) * 1024);
    }
#define G1_ITER(k, XC, WC, XN, WN, NLEFT)                                                          \
  do {                                                                                             \
    G1_WAIT(NLEFT);                                                                                \
    RAW_BAR();                                                                                     \
    const char* rb_ = smem + (((k) + 1) % G1_NBUF) * G1_STAGE + lane * 16;                         \
    _Pragma("unroll") for (int j = 0; j < 7; ++j) {                                                \
      acc[j][0] = __builtin_amdgcn_mfma_f32_16x16x32_bf16(WC[j], XC[0], acc[j][0], 0, 0, 0);       \
      if (j < 2) XN[j] = *(const bf16x8*)(rb_ + (wm * 2 + j) * 1024);                              \
      acc[j][1] = __builtin_amdgcn_mfma_f32_16x16x32_bf16(WC[j], XC[1], acc[j][1], 0, 0, 0);       \
      WN[j] = *(const bf16x8*)(rb_ + (8 + wn * 7 + j) * 1024);                                     \
      __builtin_amdgcn_sched_barrier(0);                                                           \
      if (j == 3 && (k) + G1_NBUF < 16) { stage((k) + G1_NBUF); __builtin_amdgcn_sched_barrier(0); } \
    }                                                                                              \
  } while (0)
    G1_ITER(0, xa, wa, xb, wb, 5);
    G1_ITER(1, xb, wb, xa, wa, 5);
    G1_ITER(2, xa, wa, xb, wb, 5);
    G1_ITER(3, xb, wb, xa, wa, 5);
    G1_ITER(4, xa, wa, xb, wb, 5);
    G1_ITER(5, xb, wb, xa, wa, 5);
    G1_ITER(6, xa, wa, xb, wb, 5);
    G1_ITER(7, xb, wb, xa, wa, 5);
    G1_ITER(8, xa, wa, xb, wb, 5);
    G1_ITER(9, xb, wb, xa, wa, 5);
    G1_ITER(10, xa, wa, xb, wb, 4);
    G1_ITER(11, xb, wb, xa, wa, 3);
    G1_ITER(12, xa, wa, xb, wb, 2);
    G1_ITER(13, xb, wb, xa, wa, 1);
    G1_ITER(14, xa, wa, xb, wb, 0);
    {
      const u16* ug = (const u16*)(ws + WS_XNTR) + (size_t)(t2 >> 1) * 8192 + d * 16 + (t2 & 1) * 8;
      const float* kc = (const float*)(ws + WS_KC) + (size_t)d * KC_STRIDE;
      uv0 = *(const u32x4*)ug;
      kv0 = kc[t2]; kv1 = kc[t2 + 256]; kv2 = (t2 < 129) ? kc[t2 + 512] : 0.f;
      pds = dskip[d];
    }
    __builtin_amdgcn_sched_barrier(0);
#pragma unroll
    for (int j = 0; j < 7; ++j)
#pragma unroll
      for (int i = 0; i < 2; ++i) acc[j][i] = __builtin_amdgcn_mfma_f32_16x16x32_bf16(wb[j], xb[i], acc[j][i], 0, 0, 0);
    RAW_BAR();
    f32x4* ex = (f32x4*)(smem + H_STAGE + 28672);
    if (wn == 0) {
#pragma unroll
      for (int i = 0; i < 2; ++i) ex[(wm * 2 + i) * 64 + lane] = acc[6][i];
    }
    char* hs = smem + H_STAGE;
    auto emit = [&](int t, const f32x4& g, const f32x4& u, int i) {
      u32x2 o;
#pragma unroll
      for (int p = 0; p < 2; ++p) {
        const f2 gb = unpack2(pack2(g[2 * p], g[2 * p + 1]));
        const f2 ub = unpack2(pack2(u[2 * p], u[2 * p + 1]));
        const f2 ge = gelu2(gb);
        const f2 hv = unpack2(pack2(ge.x, ge.y)) * ub;
        o[p] = pack2(hv.x, hv.y);
      }
      *(u32x2*)(hs + (((wm * 2 + i) * 7 + t) * 32 + (lane >> 5) * 16 + (lane & 15)) * 16 + ((lane >> 4) & 1) * 8) = o;
    };
    if (wn == 0) {
#pragma unroll
      for (int t = 0; t < 3; ++t)
#pragma unroll
        for (int i = 0; i < 2; ++i) emit(t, acc[2 * t][i], acc[2 * t + 1][i], i);
      RAW_BAR();
    } else {
#pragma unroll
      for (int t = 4; t < 7; ++t)
#pragma unroll
        for (int i = 0; i < 2; ++i) emit(t, acc[2 * t - 7][i], acc[2 * t - 6][i], i);
      RAW_BAR();
#pragma unroll
      for (int i = 0; i < 2; ++i) emit(3, ex[(wm * 2 + i) * 64 + lane], acc[0][i], i);
    }
    {
      char* cbase = smem + ch * CV_CH_STRIDE;
      u32x4* uraw = (u32x4*)(cbase + CV_URAW);
      float* kraw = (float*)(cbase + CV_KRAW);
      float* kmax = (float*)(cbase + CV_MAX);
      if (t2 < 4) { uraw[t2] = u32x4{0, 0, 0, 0}; uraw[260 + t2] = u32x4{0, 0, 0, 0}; }
      u32x4 h;
#pragma unroll
      for (int j = 0; j < 4; ++j) h[j] = bf2_to_h2(uv0[j]);
      uraw[4 + t2] = h;
      kraw[t2] = kv0;
      kraw[t2 + 256] = kv1;
      if (t2 < 136) kraw[t2 + 512] = kv2;
      const float mx = wave_max(fmaxf(fmaxf(fabsf(kv0), fabsf(kv1)), fabsf(kv2)));
      if (lane == 0) kmax[w4] = mx;
    }
    RAW_BAR();
    u16* hT = (u16*)(ws + WS_H);
#pragma unroll
    for (int it = 0; it < 4; ++it) {
      const int c = tid + it * 512;
      if (c < 1792) {
        const int ml = c / 224, rem = c - ml * 224;
        st16_wt(hT + ((size_t)(mt * 8 + ml) * 56 * 64 + (size_t)nt * 7 * 32 + rem) * 8, *(const u32x4*)(hs + c * 16));
      }
    }
  }

  const int mt2 = (xcd >> 1) * 8 + (xcd & 1) * 4 + (loc & 3);
  const int nt2 = loc >> 2;
  const int vo = lane * 16;
  const __amdgpu_buffer_rsrc_t hr = __builtin_amdgcn_make_buffer_rsrc((void*)((const u16*)(ws + WS_H) + ((size_t)(mt2 * 4) * 56 + wid * 7) * 512), 0, 4 * 56 * 1024, 0x00020000);
  const __amdgpu_buffer_rsrc_t wr = __builtin_amdgcn_make_buffer_rsrc((void*)((const u16*)(ws + WS_WD) + ((size_t)(nt2 * 4) * 56 + wid * 7) * 512), 0, 4 * 56 * 1024, 0x00020000);
#define G2_LD(rs, i, k, aux) __builtin_bit_cast(bf16x8, __builtin_amdgcn_raw_buffer_load_b128(rs, vo, ((i) * 56 + (k)) * 1024, aux))
  bf16x8 wd[7][4];

  {
    char* cbase = smem + ch * CV_CH_STRIDE;
    u16* ucp = (u16*)(cbase + CV_UCP);
    u16* kr = (u16*)(cbase + CV_KR);
    const u32x4* uraw = (const u32x4*)(cbase + CV_URAW);
    const float* kraw = (const float*)(cbase + CV_KRAW);
    const float* kmax = (const float*)(cbase + CV_MAX);
    const float dsk = bf2f((u16)pds);
    float kscale, kinv;
    {
      const float mx = fmaxf(fmaxf(kmax[0], kmax[1]), fmaxf(kmax[2], kmax[3]));
      int e = (int)((__float_as_uint(mx) >> 23) & 0xff);
      e = min(max(e, 20), 240);
      kscale = __uint_as_float((u32)(127 + 13 + 127 - e) << 23);
      kinv = __uint_as_float((u32)(e - 13) << 23);
    }
#pragma unroll
    for (int it = 0; it < 2; ++it) {
      const int tb = it ? 256 + (t2 & 3) : t2;
      u32 w[8];
      u32x4 a = uraw[tb], b = uraw[tb + 1];
#pragma unroll
      for (int j = 0; j < 4; ++j) { w[j] = a[j]; w[4 + j] = b[j]; }
      if (it == 0 || t2 < 4) {
#pragma unroll
        for (int c = 0; c < 8; ++c) {
          u32x4 o;
#pragma unroll
          for (int j = 0; j < 4; ++j)
            o[j] = (c & 1) ? w[j + (c + 1) / 2] : __builtin_amdgcn_alignbit(w[j + c / 2 + 1], w[j + c / 2], 16);
          *(u32x4*)(ucp + c * UC_LEN + tb * 8) = o;
        }
      }
    }
    {
      float kk[8];
#pragma unroll
      for (int j = 0; j < 8; ++j) {
        const int jj = 2047 - (t2 * 8 + j);
        const int cbi = (int)f2bf((float)jj) - 17024;
        kk[j] = kraw[jj < 128 ? jj : cbi] * kscale;
      }
      f16x8 o;
#pragma unroll
      for (int j = 0; j < 8; ++j) o[j] = (_Float16)kk[j];
      *(f16x8*)(kr + t2 * 8 + 8 * (t2 >> 2)) = o;
      if (t2 < 128) { const int tb = t2 + 256; *(u32x4*)(kr + tb * 8 + 8 * (tb >> 2)) = u32x4{0, 0, 0, 0}; }
    }
    asm volatile("s_waitcnt vmcnt(0)" ::: "memory");
    RAW_BAR();
    if (tid == 0) __hip_atomic_fetch_add((gu32*)(ws + WS_CNT + mt * 256), 1u, __ATOMIC_RELAXED, __HIP_MEMORY_SCOPE_AGENT);
    const int r = lane & 31, h = lane >> 5;
    const u16* ap = ucp + (r & 7) * UC_LEN + 8 * h + 8 * (r >> 3);
    const int bofs0 = 2048 - 32 * (r + 1) + 8 * h + 8 * (63 - r);
    const int bofs1 = bofs0 - 1024 - 256;
    f32x16 acc0 = {}, acc1 = {};
    unsigned hcnt = 0;
    {
      const u16* apw = ap + 16 * (w4 * 16);
      const u16* bp0 = kr + bofs0 + 16 * (w4 * 16) + 8 * (w4 * 8);
      const u16* bp1 = kr + bofs1 + 16 * (w4 * 16) + 8 * (w4 * 8);
      auto lda = [&](int s) -> f16x8 { return *(const f16x8*)(apw + 16 * s); };
      auto ldb = [&](const u16* b, int s) -> f16x8 { return *(const f16x8*)(b + 16 * s + 8 * (s >> 1)); };
      f16x8 a0 = lda(0), p0 = ldb(bp0, 0), q0 = ldb(bp1, 0), c0 = lda(64);
      f16x8 a1 = lda(1), p1 = ldb(bp0, 1), q1 = ldb(bp1, 1), c1 = lda(65);
#pragma unroll
      for (int it = 0; it < 8; ++it) {
        f16x8 na0, na1, nc0, nc1, np0, np1, nq0, nq1;
        if (it < 7) {
          const int sn = 2 * it + 2;
          na0 = lda(sn); np0 = ldb(bp0, sn); nq0 = ldb(bp1, sn); nc0 = lda(sn + 64);
          na1 = lda(sn + 1); np1 = ldb(bp0, sn + 1); nq1 = ldb(bp1, sn + 1); nc1 = lda(sn + 65);
#pragma unroll
          for (int i = 0; i < 4; ++i) wd[it][i] = G2_LD(wr, i, it, 0);
        }
        if (it == 7) hcnt = __hip_atomic_load((gu32*)(ws + WS_CNT + (mt2 >> 1) * 256), __ATOMIC_RELAXED, __HIP_MEMORY_SCOPE_AGENT);
        __builtin_amdgcn_sched_barrier(0);
        acc0 = __builtin_amdgcn_mfma_f32_32x32x16_f16(a0, p0, acc0, 0, 0, 0);
        acc1 = __builtin_amdgcn_mfma_f32_32x32x16_f16(a0, q0, acc1, 0, 0, 0);
        acc1 = __builtin_amdgcn_mfma_f32_32x32x16_f16(c0, p0, acc1, 0, 0, 0);
        acc0 = __builtin_amdgcn_mfma_f32_32x32x16_f16(a1, p1, acc0, 0, 0, 0);
        acc1 = __builtin_amdgcn_mfma_f32_32x32x16_f16(a1, q1, acc1, 0, 0, 0);
        acc1 = __builtin_amdgcn_mfma_f32_32x32x16_f16(c1, p1, acc1, 0, 0, 0);
        __builtin_amdgcn_sched_barrier(0);
        if (it < 7) { a0 = na0; a1 = na1; c0 = nc0; c1 = nc1; p0 = np0; p1 = np1; q0 = nq0; q1 = nq1; }
      }
    }
    RAW_BAR();
    float* red = (float*)cbase;
#pragma unroll
    for (int g = 0; g < 4; ++g) {
      f32x4 v0 = {acc0[4 * g], acc0[4 * g + 1], acc0[4 * g + 2], acc0[4 * g + 3]};
      f32x4 v1 = {acc1[4 * g], acc1[4 * g + 1], acc1[4 * g + 2], acc1[4 * g + 3]};
      *(f32x4*)(red + (w4 * 32 + r) * 36 + 8 * g + 4 * h) = v0;
      *(f32x4*)(red + ((4 + w4) * 32 + r) * 36 + 8 * g + 4 * h) = v1;
    }
    if (tid == 511 && hcnt < 16u) {
      gu32* cnt = (gu32*)(ws + WS_CNT + (mt2 >> 1) * 256);
      for (unsigned spins = 0; spins < (1u << 22); ++spins) {
        if (__hip_atomic_load(cnt, __ATOMIC_RELAXED, __HIP_MEMORY_SCOPE_AGENT) >= 16u) break;
        __builtin_amdgcn_s_sleep(1);
      }
    }
    RAW_BAR();
    u16* at = (u16*)(ws + WS_AT) + (size_t)d * L_;
    {
      const int c = t2;
      const int T = c >> 7, a = (c >> 2) & 31, b0 = (c & 3) * 8;
      f32x4 s0 = {}, s1 = {};
      const float* p = red + (T * 4 * 32 + a) * 36 + b0;
#pragma unroll
      for (int w = 0; w < 4; ++w) { s0 += *(const f32x4*)(p + w * 32 * 36); s1 += *(const f32x4*)(p + w * 32 * 36 + 4); }
      u32x4 o;
#pragma unroll
      for (int j = 0; j < 4; ++j) {
        const f2 y = unpack2((j < 2) ? pack2(s0[2 * j] * kinv, s0[2 * j + 1] * kinv) : pack2(s1[2 * j - 4] * kinv, s1[2 * j - 3] * kinv));
        const f2 us = unpack2(uv0[j]) * dsk;
        const f2 t = unpack2(pack2(us.x, us.y)) + y;
        u32 ov = pack2(t.x, t.y);
        if ((ov & 0xffffu) == 0xffffu) ov &= 0xffffffc0u;
        if ((ov >> 16) == 0xffffu) ov &= 0xffc0ffffu;
        o[j] = ov;
      }
      st16_wt(at + c * 8, o);
    }
  }

  {
    f32x4 acc[4][4] = {};
    bf16x8 hb[4][4];
#pragma unroll
    for (int k = 0; k < 4; ++k)
#pragma unroll
      for (int i = 0; i < 4; ++i) hb[k][i] = G2_LD(hr, i, k, 16);
    const int m = tid >> 3, n0 = (tid & 7) * 8;
    const size_t go = (size_t)(mt2 * 64 + m) * D_ + nt2 * 64 + n0;
    const u32x4 xv = *(const u32x4*)(x + go);
    __builtin_amdgcn_sched_barrier(0);
#pragma unroll
    for (int k = 0; k < 7; ++k) {
#pragma unroll
      for (int j = 0; j < 4; ++j)
#pragma unroll
        for (int i = 0; i < 4; ++i) acc[j][i] = __builtin_amdgcn_mfma_f32_16x16x32_bf16(wd[k][j], hb[k & 3][i], acc[j][i], 0, 0, 0);
      if (k < 3) {
#pragma unroll
        for (int i = 0; i < 4; ++i) hb[k][i] = G2_LD(hr, i, k + 4, 16);
      }
      __builtin_amdgcn_sched_barrier(0);
    }
    u32 a0, a1, a2, a3, a4, a5, a6, a7;
    const u16* at = (const u16*)(ws + WS_AT) + (size_t)(nt2 * 64 + n0) * L_ + mt2 * 64 + m;
#define A_LOADS(tail)                                                                                              \
  asm volatile("global_load_ushort %0, %8, off sc1\n\t"                                                            \
               "global_load_ushort %1, %9, off sc1\n\t"                                                            \
               "global_load_ushort %2, %10, off sc1\n\t"                                                           \
               "global_load_ushort %3, %11, off sc1\n\t"                                                           \
               "global_load_ushort %4, %12, off sc1\n\t"                                                           \
               "global_load_ushort %5, %13, off sc1\n\t"                                                           \
               "global_load_ushort %6, %14, off sc1\n\t"                                                           \
               "global_load_ushort %7, %15, off sc1" tail                                                          \
               : "=&v"(a0), "=&v"(a1), "=&v"(a2), "=&v"(a3), "=&v"(a4), "=&v"(a5), "=&v"(a6), "=&v"(a7)            \
               : "v"(at), "v"(at + L_), "v"(at + 2 * L_), "v"(at + 3 * L_), "v"(at + 4 * L_), "v"(at + 5 * L_),   \
                 "v"(at + 6 * L_), "v"(at + 7 * L_)                                                                \
               : "memory")
    A_LOADS("");
    RAW_BAR();
    float* red = (float*)smem;
    float* rp = red + (wid * 64 + (lane & 15)) * 68 + 4 * (lane >> 4);
#pragma unroll
    for (int j = 0; j < 4; ++j)
#pragma unroll
      for (int i = 0; i < 4; ++i) *(f32x4*)(rp + i * 16 * 68 + j * 16) = acc[j][i];
    asm volatile("s_waitcnt vmcnt(0)" : "+v"(a0), "+v"(a1), "+v"(a2), "+v"(a3), "+v"(a4), "+v"(a5), "+v"(a6), "+v"(a7)::"memory");
    for (unsigned spins = 0; spins < (1u << 20); ++spins) {
      const bool pend = (a0 == 0xffffu) | (a1 == 0xffffu) | (a2 == 0xffffu) | (a3 == 0xffffu) | (a4 == 0xffffu) |
                        (a5 == 0xffffu) | (a6 == 0xffffu) | (a7 == 0xffffu);
      if (!__any(pend)) break;
      __builtin_amdgcn_s_sleep(2);
      A_LOADS("\n\ts_waitcnt vmcnt(0)");
    }
    RAW_BAR();
    f32x4 p0 = {}, p1 = {};
#pragma unroll
    for (int w = 0; w < 8; ++w) {
      p0 += *(const f32x4*)(red + (w * 64 + m) * 68 + n0);
      p1 += *(const f32x4*)(red + (w * 64 + m) * 68 + n0 + 4);
    }
    const u32 av[8] = {a0, a1, a2, a3, a4, a5, a6, a7};
    u32x4 o;
#pragma unroll
    for (int j = 0; j < 4; ++j) {
      const f2 xx = unpack2(xv[j]);
      const f2 aa = f2{__uint_as_float(av[2 * j] << 16), __uint_as_float(av[2 * j + 1] << 16)};
      const f2 r1 = xx + aa;
      const f2 bb = unpack2((j < 2) ? pack2(p0[2 * j], p0[2 * j + 1]) : pack2(p1[2 * j - 4], p1[2 * j - 3]));
      const f2 r2 = unpack2(pack2(r1.x, r1.y)) + bb;
      o[j] = pack2(r2.x, r2.y);
    }
    *(u32x4*)(out + go) = o;
  }
}

extern "C" void kernel_launch(void* const* d_in, const int* in_sizes, int n_in, void* d_out, int out_size,
                              void* d_ws, size_t ws_size, hipStream_t stream) {
  const u16* x = (const u16*)d_in[0];
  const u16* logA = (const u16*)d_in[1];
  const u16* Aim = (const u16*)d_in[2];
  const u16* Cre = (const u16*)d_in[3];
  const u16* Cim = (const u16*)d_in[4];
  const u16* logdt = (const u16*)d_in[5];
  const u16* dskip = (const u16*)d_in[6];
  const u16* wg = (const u16*)d_in[8];
  const u16* wu = (const u16*)d_in[9];
  const u16* wd = (const u16*)d_in[10];
  char* ws = (char*)d_ws;
  (void)hipFuncSetAttribute((const void*)k2_kernel, hipFuncAttributeMaxDynamicSharedMemorySize, K2_LDS);
  k1_kernel<<<K1_LN_BLOCKS + K1_WT_BLOCKS + K1_KG_BLOCKS, 256, 0, stream>>>(x, logA, Aim, Cre, Cim, logdt, wg, wu, wd, ws);
  k2_kernel<<<256, 512, K2_LDS, stream>>>(x, dskip, ws, (u16*)d_out);
}
```
